```python
import torch

def _softmax(x, axis=-1):
    return torch.softmax(x, dim=axis)
def _gelu(x):
    return torch.nn.functional.gelu(x, approximate="tanh")
def _silu(x):
    return torch.nn.functional.silu(x)
def _sigmoid(x):
    return torch.sigmoid(x)
def _layernorm(x, axis=-1, eps=1e-5):
    m = x.mean(dim=axis, keepdim=True)
    v = x.var(dim=axis, keepdim=True, unbiased=False)
    return (x - m) / torch.sqrt(v + eps)
def _scatter_add(tgt, idx, src, axis=0):
    return tgt.clone().index_add_(axis, idx, src)
def _segment_sum(src, seg, n):
    out = torch.zeros((n,) + src.shape[1:], dtype=src.dtype)
    return out.index_add_(0, seg, src)
def _pad(x, pw):
    flat = tuple(v for pair in reversed(pw) for v in pair)
    return torch.nn.functional.pad(x, flat)

kernel_name = 'interleave_layers__spectral_fnet__dense_activation_relu__prompt'

def setup_inputs():

    _w = (lambda a: (lambda t: t.to(torch.bfloat16) if t.is_floating_point() else t)(torch.as_tensor(a)))
    def _A_setup():
        B, S, D = 1, 2048, 576
        ns = torch.arange(S)[:, None] * torch.arange(S)[None, :]
        nd = torch.arange(D)[:, None] * torch.arange(D)[None, :]
        Ws_re = torch.cos(-2.0 * torch.pi * ns / S).to(float)
        Ws_im = torch.sin(-2.0 * torch.pi * ns / S).to(float)
        Wd_re = torch.cos(-2.0 * torch.pi * nd / D).to(float)
        Wd_im = torch.sin(-2.0 * torch.pi * nd / D).to(float)
        return {
            "x": _w((lambda *s: torch.randn(*s, dtype=torch.bfloat16) * 0.05)(B, S, D)),
            "dft_seq_re": _w(Ws_re),
            "dft_seq_im": _w(Ws_im),
            "dft_hid_re": _w(Wd_re),
            "dft_hid_im": _w(Wd_im),
        }
    _A_in = _A_setup()

    def _B_setup():
        B, S, D, Dff = 1, 2048, 576, 4608
        return {
            "x": _w((lambda *s: torch.randn(*s, dtype=torch.bfloat16) * 0.05)(B, S, D)),
            "w1": _w((lambda *s: torch.randn(*s, dtype=torch.bfloat16) * 1.6)(D, Dff)),
            "w2": _w((lambda *s: torch.randn(*s, dtype=torch.bfloat16) * 1.6)(Dff, D)),
        }
    _B_in = _B_setup()

    return {
        "A_x": _A_in["x"],
        "A_dft_seq_re": _A_in["dft_seq_re"],
        "A_dft_seq_im": _A_in["dft_seq_im"],
        "A_dft_hid_re": _A_in["dft_hid_re"],
        "A_dft_hid_im": _A_in["dft_hid_im"],
        "B_x": _B_in["x"],
        "B_w1": _B_in["w1"],
        "B_w2": _B_in["w2"],
    }

def reference(A_x, A_dft_seq_re, A_dft_seq_im, A_dft_hid_re, A_dft_hid_im, B_x, B_w1, B_w2):
    def _A_ref(x, dft_seq_re, dft_seq_im, dft_hid_re, dft_hid_im):
        a = torch.einsum("bsd,ed->bse", x, dft_hid_re)
        b = torch.einsum("bsd,ed->bse", x, dft_hid_im)
        y = torch.einsum("ts,bse->bte", dft_seq_re, a) - torch.einsum("ts,bse->bte", dft_seq_im, b)
        return y

    def _B_ref(x, w1, w2):
        h = torch.einsum("bsd,df->bsf", x, w1)
        zero = torch.zeros_like(h)
        h = torch.maximum(h, zero)
        return torch.einsum("bsf,fd->bsd", h, w2)

    x = A_x
    x = x + _A_ref(_layernorm(x, axis=-1), A_dft_seq_re, A_dft_seq_im, A_dft_hid_re, A_dft_hid_im)
    x = x + _B_ref(_layernorm(x, axis=-1), B_w1, B_w2)
    x = x + _A_ref(_layernorm(x, axis=-1), A_dft_seq_re, A_dft_seq_im, A_dft_hid_re, A_dft_hid_im)
    x = x + _B_ref(_layernorm(x, axis=-1), B_w1, B_w2)
    return x
```

```cpp
#include <hip/hip_runtime.h>
#include <hip/hip_bf16.h>
#include <stdint.h>

typedef unsigned short u16;
static __device__ __forceinline__ float b2f(u16 v) { return __uint_as_float(((unsigned)v) << 16); }
static __device__ __forceinline__ u16 f2b(float f) { __hip_bfloat16 b = __float2bfloat16(f); return *(u16*)&b; }
static __device__ __forceinline__ float rb(float f) { return b2f(f2b(f)); }

constexpr int S = 2048, D = 576, F = 4608;

__global__ void n_ln(const u16* x, u16* y) {
    int row = blockIdx.x; int t = threadIdx.x;
    float v[9]; float s = 0;
    for (int i = 0; i < 9; i++) { v[i] = b2f(x[row * D + t + 64 * i]); s += v[i]; }
    for (int o = 32; o; o >>= 1) s += __shfl_xor(s, o);
    float m = s / D; float q = 0;
    for (int i = 0; i < 9; i++) { float d = v[i] - m; q += d * d; }
    for (int o = 32; o; o >>= 1) q += __shfl_xor(q, o);
    float var = q / D; float mb = m;
    float sd = sqrtf(var + 1e-5f);
    for (int i = 0; i < 9; i++) y[row * D + t + 64 * i] = f2b((v[i] - mb) / sd);
}
__global__ void n_gemm(const u16* A, int lda, const u16* B, int sbk, int sbn, u16* C, int ldc, int K, int relu) {
    __shared__ float As[32][33], Bs[32][33];
    int tx = threadIdx.x & 31, ty = threadIdx.x >> 5;
    int m0 = blockIdx.y * 32, n0 = blockIdx.x * 32;
    float acc[4] = {0, 0, 0, 0};
    for (int k0 = 0; k0 < K; k0 += 32) {
        for (int i = 0; i < 4; i++) { int r = ty + 8 * i;
            As[r][tx] = b2f(A[(size_t)(m0 + r) * lda + k0 + tx]);
            if (sbn == 1) Bs[r][tx] = b2f(B[(size_t)(k0 + r) * sbk + (n0 + tx)]);
            else Bs[tx][r] = b2f(B[(size_t)(k0 + tx) * sbk + (size_t)(n0 + r) * sbn]); }
        __syncthreads();
        for (int k = 0; k < 32; k++) { float b = Bs[k][tx];
            for (int i = 0; i < 4; i++) acc[i] += As[ty + 8 * i][k] * b; }
        __syncthreads();
    }
    for (int i = 0; i < 4; i++) { float v = acc[i]; if (relu) v = fmaxf(rb(v), 0.f); C[(size_t)(m0 + ty + 8 * i) * ldc + n0 + tx] = f2b(v); }
}
__global__ void n_comb(const u16* x, const u16* p, const u16* q, u16* o, int n) {
    int i = blockIdx.x * 256 + threadIdx.x; if (i >= n) return;
    float y = q ? rb(b2f(p[i]) - b2f(q[i])) : b2f(p[i]);
    o[i] = f2b(b2f(x[i]) + y);
}

extern "C" void kernel_launch(void* const* d_in, const int* in_sizes, int n_in,
                              void* d_out, int out_size, void* d_ws, size_t ws_size,
                              hipStream_t stream) {
    const u16* x0 = (const u16*)d_in[0]; const u16* Cr = (const u16*)d_in[1]; const u16* Ci = (const u16*)d_in[2];
    const u16* Wr = (const u16*)d_in[3]; const u16* Wi = (const u16*)d_in[4];
    const u16* w1 = (const u16*)d_in[6]; const u16* w2 = (const u16*)d_in[7];
    u16* ws = (u16*)d_ws; size_t n = (size_t)S * D;
    u16 *xn = ws, *a = ws + n, *b = ws + 2 * n, *p = ws + 3 * n, *q = ws + 4 * n, *x1 = ws + 5 * n, *x2 = ws + 6 * n, *h = ws + 7 * n;
    const u16* x = x0;
    for (int l = 0; l < 2; l++) {
        n_ln<<<S, 64, 0, stream>>>(x, xn);
        n_gemm<<<dim3(D / 32, S / 32), 256, 0, stream>>>(xn, D, Wr, 1, D, a, D, D, 0);
        n_gemm<<<dim3(D / 32, S / 32), 256, 0, stream>>>(xn, D, Wi, 1, D, b, D, D, 0);
        n_gemm<<<dim3(D / 32, S / 32), 256, 0, stream>>>(Cr, S, a, D, 1, p, D, S, 0);
        n_gemm<<<dim3(D / 32, S / 32), 256, 0, stream>>>(Ci, S, b, D, 1, q, D, S, 0);
        n_comb<<<(n + 255) / 256, 256, 0, stream>>>(x, p, q, x1, n);
        n_ln<<<S, 64, 0, stream>>>(x1, xn);
        n_gemm<<<dim3(F / 32, S / 32), 256, 0, stream>>>(xn, D, w1, F, 1, h, F, D, 1);
        n_gemm<<<dim3(D / 32, S / 32), 256, 0, stream>>>(h, F, w2, D, 1, p, D, F, 0);
        u16* o = l == 0 ? x2 : (u16*)d_out;
        n_comb<<<(n + 255) / 256, 256, 0, stream>>>(x1, p, nullptr, o, n);
        x = x2;
    }
}
```

```cpp
#include <hip/hip_runtime.h>
#include <hip/hip_bf16.h>

__global__ __launch_bounds__(256, 1) void fnet_ffn_persistent(void* ws, const void* x0, const void* Cr, const void* Ci, const void* Wr, const void* Wi,
                                                              const void* w1, const void* w2, void* out) {
    __shared__ __attribute__((aligned(16))) char smem[163840];
    const int w = __builtin_amdgcn_readfirstlane(threadIdx.x >> 6);
    asm volatile(
"s_getpc_b64 s[40:41]\n"
"Lbeg%=:\n"
"s_load_dwordx16 s[64:79], %0, 0x0\n"
"s_load_dwordx2 s[54:55], %0, 0x40\n"
"s_mov_b32 s56, %1\n"
"s_mov_b32 s62, %2\n"
"s_bfe_u32 s58, s62, 0x10000\n"
"s_bfe_u32 s63, s62, 0x20006\n"
"s_lshl2_add_u32 s58, s58, s63\n"
"s_bfe_u32 s57, s62, 0x20001\n"
"s_bfe_u32 s63, s62, 0x30003\n"
"s_lshl3_add_u32 s57, s57, s63\n"
"v_mbcnt_lo_u32_b32 v0, -1, 0\n"
"v_mbcnt_hi_u32_b32 v0, -1, v0\n"
"v_lshlrev_b32 v1, 4, v0\n"
"v_lshrrev_b32 v18, 3, v0\n"
"v_and_b32 v19, 6, v18\n"
"v_xor_b32 v19, v19, v0\n"
"v_and_b32 v19, 7, v19\n"
"v_lshlrev_b32 v19, 4, v19\n"
"v_and_b32 v6, 4, v18\n"
"v_add_u32 v18, v6, v18\n"
"v_lshl_add_u32 v7, v18, 12, v19\n"
"v_mul_u32_u24 v6, 0x480, v18\n"
"v_add_u32 v6, v6, v19\n"
"s_mov_b32 s59, 0\n"
"s_mov_b32 s60, 0xf4240\n"
"s_lshr_b32 s63, s56, 1\n"
"s_lshl_b32 s63, s63, 5\n"
"s_and_b32 s62, s56, 1\n"
"s_lshl2_add_u32 s63, s62, s63\n"
"s_lshl_b32 s62, s57, 6\n"
"s_add_u32 s63, s63, s62\n"
"s_mul_i32 s92, s63, 0x480\n"
"s_add_u32 s93, s92, 0x4800\n"
"s_mul_i32 s61, s56, 0x4800\n"
"s_and_b32 s41, s41, 0xffff\n"
"s_mov_b32 s42, Lend%=-Lbeg%=\n"
"s_mov_b32 s43, 0x20000\n"
"s_and_b32 s62, s57, 7\n"
"s_and_b32 s63, s58, 3\n"
"s_lshl2_add_u32 s62, s62, s63\n"
"s_lshl2_add_u32 s62, s62, s56\n"
"s_lshl_b32 s62, s62, 9\n"
"v_lshl_add_u32 v18, v0, 3, s62\n"
"buffer_load_dwordx2 a[48:49], v18, s[40:43], 0 offen\n"
"s_waitcnt lgkmcnt(0)\n"
"s_mov_b64 s[36:37], s[66:67]\n"
"s_and_b32 s37, s37, 0xffff\n"
"s_mov_b32 s38, 0x240000\n"
"s_mov_b32 s39, 0x20000\n"
"s_cmp_ge_u32 s58, 4\n"
"s_cselect_b64 s[68:69], s[70:71], s[68:69]\n"
"s_cselect_b64 s[72:73], s[74:75], s[72:73]\n"
"s_mov_b64 s[48:49], s[72:73]\n"
"s_and_b32 s49, s49, 0xffff\n"
"s_mov_b32 s50, 0xa2000\n"
"s_mov_b32 s51, 0x20000\n"
"s_and_b32 s62, s58, 3\n"
"s_lshl2_add_u32 s62, s57, s62\n"
"s_lshl2_add_u32 s62, s62, s56\n"
"s_mul_i32 s63, s62, 0x510\n"
"v_mul_u32_u24 v18, 64, v0\n"
"s_bfm_b64 exec, 21, 0\n"
"buffer_load_dword a20, v18, s[48:51], s63 offen\n"
"s_mov_b64 exec, -1\n"
"s_lshl_b32 s20, s56, 14\n"
"s_add_u32 s20, s20, 0x12000\n"
"s_mov_b64 s[24:25], s[64:65]\n"
"s_and_b32 s25, s25, 0xffff\n"
"s_mov_b32 s26, 0x46a0000\n"
"s_mov_b32 s27, 0x20000\n"
"s_cmp_lg_u32 s56, 0\n"
"s_cbranch_scc1 Lhs0%=\n"
"v_mov_b32 v104, 0\n"
"v_mov_b32 v105, 0\n"
"v_mov_b32 v106, 0\n"
"v_mov_b32 v107, 0\n"
"s_lshl3_add_u32 s62, s57, s58\n"
"s_lshl_b32 s63, s62, 11\n"
"buffer_store_dwordx4 v[104:107], v1, s[24:27], s63 offen sc1\n"
"s_lshl_b32 s63, s62, 7\n"
"s_add_u32 s63, s63, 0x80000\n"
"s_mov_b64 exec, 1\n"
"v_mov_b32 v15, 0\n"
"buffer_atomic_add v15, off, s[24:27], s63 sc0 sc1\n"
"s_mov_b64 exec, -1\n"
"Lhs0%=:\n"
"s_add_u32 m0, s61, 0\n"
"s_nop 0\n"
"buffer_load_dwordx4 v6, s[36:39], s92 offen lds\n"
"s_add_u32 m0, s61, 0x400\n"
"s_nop 0\n"
"buffer_load_dwordx4 v6, s[36:39], s93 offen lds\n"
"s_add_u32 m0, s61, 0x780\n"
"s_nop 0\n"
"buffer_load_dwordx4 v6, s[36:39], s92 offen offset:128 lds\n"
"s_add_u32 m0, s61, 0xb80\n"
"s_nop 0\n"
"buffer_load_dwordx4 v6, s[36:39], s93 offen offset:128 lds\n"
"s_add_u32 m0, s61, 0xf00\n"
"s_nop 0\n"
"buffer_load_dwordx4 v6, s[36:39], s92 offen offset:256 lds\n"
"s_add_u32 m0, s61, 0x1300\n"
"s_nop 0\n"
"buffer_load_dwordx4 v6, s[36:39], s93 offen offset:256 lds\n"
"s_add_u32 m0, s61, 0x1680\n"
"s_nop 0\n"
"buffer_load_dwordx4 v6, s[36:39], s92 offen offset:384 lds\n"
"s_add_u32 m0, s61, 0x1a80\n"
"s_nop 0\n"
"buffer_load_dwordx4 v6, s[36:39], s93 offen offset:384 lds\n"
"s_and_b32 s62, s57, 7\n"
"s_lshr_b32 s63, s57, 3\n"
"s_lshl2_add_u32 s62, s62, s63\n"
"s_xor_b32 s91, s56, 3\n"
"s_lshl_b32 s91, s91, 5\n"
"s_add_u32 s91, s91, s62\n"
"s_min_u32 s62, s91, 0x44\n"
"s_lshl1_add_u32 s91, s91, s62\n"
"s_mul_i32 s33, s91, 0xe39\n"
"s_lshr_b32 s33, s33, 16\n"
"s_mul_i32 s63, s33, 18\n"
"s_sub_u32 s99, s91, s63\n"
"s_mul_i32 s34, s99, 11\n"
"s_lshr_b32 s34, s34, 5\n"
"s_mul_i32 s62, s34, 5\n"
"s_lshr_b32 s62, 0x1876b8c0, s62\n"
"s_and_b32 s62, s62, 31\n"
"s_mul_i32 s96, s62, 0x48000\n"
"s_mul_i32 s97, s58, 9\n"
"s_add_u32 s97, s97, s33\n"
"s_lshl_b32 s97, s97, 7\n"
"s_add_u32 s96, s96, s97\n"
"s_mul_i32 s98, s58, 18\n"
"s_add_u32 s98, s98, s99\n"
"s_mul_i32 s98, s98, 0x9000\n"
"s_add_u32 s97, s33, s58\n"
"s_sub_u32 s97, s97, 9\n"
"s_and_b32 s97, s97, 7\n"
"s_cmp_eq_u32 s33, 9\n"
"s_cselect_b32 s97, 8, s97\n"
"s_lshl_b32 s97, s97, 7\n"
"s_add_u32 s98, s98, s97\n"
"s_cmp_lt_u32 s33, 9\n"
"s_cselect_b32 s96, s96, s98\n"
"s_cselect_b64 s[44:45], s[76:77], s[78:79]\n"
"s_mov_b32 s62, 0x4800\n"
"s_cselect_b32 s97, 0x9000, s62\n"
"s_mov_b32 s62, 0x2400\n"
"s_cselect_b32 s98, 0x24000, s62\n"
"s_mov_b32 s62, 0x480\n"
"s_cselect_b32 s99, 0x2400, s62\n"
"s_cselect_b64 vcc, -1, 0\n"
"s_and_b32 s45, s45, 0xffff\n"
"s_mov_b32 s46, 0x510000\n"
"s_mov_b32 s47, 0x20000\n"
"v_lshrrev_b32 v18, 3, v0\n"
"v_lshrrev_b32 v19, 12, v7\n"
"v_cndmask_b32 v18, v18, v19, vcc\n"
"v_and_b32 v19, 0x70, v7\n"
"v_mad_u32_u24 v116, v18, s99, v19\n"
"s_lshl_b32 s99, s99, 5\n"
"s_mul_i32 s62, s34, 5\n"
"s_lshr_b32 s63, 0x21281213, s62\n"
"s_and_b32 s63, s63, 31\n"
"s_sub_u32 s63, s63, 16\n"
"s_and_b32 s63, s63, vcc_lo\n"
"s_add_u32 s63, s63, 1\n"
"s_mul_i32 s35, s99, s63\n"
"s_lshr_b32 s63, 0x27024250, s62\n"
"s_and_b32 s63, s63, 31\n"
"s_sub_u32 s63, s63, 16\n"
"s_and_b32 s63, s63, vcc_lo\n"
"s_add_u32 s63, s63, 1\n"
"s_mul_i32 s99, s99, s63\n"
"s_mov_b64 s[28:29], s[68:69]\n"
"s_and_b32 s29, s29, 0xffff\n"
"s_mov_b32 s30, 0x800000\n"
"s_mov_b32 s31, 0x20000\n"
"v_add_u32 v5, s61, v1\n"
"s_and_b32 s63, s58, 3\n"
"s_mul_i32 s67, s63, 9\n"
"s_and_b32 s63, s58, 1\n"
"s_xor_b32 s68, s63, 1\n"
"s_lshl3_add_u32 s68, s68, s67\n"
"s_add_u32 s67, s67, s63\n"
"s_lshr_b32 s62, s67, 1\n"
"s_add_u32 s62, s62, s56\n"
"s_mul_i32 s8, s62, 0x9000\n"
"s_add_u32 s10, s8, 0x1200\n"
"s_mov_b32 s62, s8\n"
"s_bitcmp1_b32 s57, 0\n"
"s_cselect_b32 s8, s10, s62\n"
"s_cselect_b32 s10, s62, s10\n"
"s_add_u32 s9, s8, 0x4800\n"
"s_add_u32 s11, s10, 0x4800\n"
"v_bfe_u32 v18, v0, 2, 3\n"
"v_lshrrev_b32 v19, 1, v18\n"
"v_lshlrev_b32 v19, 5, v19\n"
"v_lshl_add_u32 v19, v18, 7, v19\n"
"v_and_b32 v18, 3, v0\n"
"v_lshl_add_u32 v19, v18, 3, v19\n"
"v_lshrrev_b32 v18, 5, v0\n"
"v_lshl_add_u32 v19, v18, 11, v19\n"
"v_add_u32 v19, s20, v19\n"
"v_xor_b32 v112, 0, v19\n"
"v_xor_b32 v113, 32, v19\n"
"v_xor_b32 v114, 64, v19\n"
"v_xor_b32 v115, 0x60, v19\n"
"v_mov_b32 v156, 0x3f803f80\n"
"v_mov_b32 v157, 0x3f803f80\n"
"v_mov_b32 v158, 0x3f803f80\n"
"v_mov_b32 v159, 0x3f803f80\n"
"s_add_u32 m0, s20, 0x1000\n"
"s_mov_b32 s62, s96\n"
"buffer_load_dwordx4 v116, s[44:47], s62 offen lds\n"
"s_add_u32 m0, s20, 0x1400\n"
"s_add_u32 s62, s96, s97\n"
"buffer_load_dwordx4 v116, s[44:47], s62 offen lds\n"
"s_add_u32 m0, s20, 0x1800\n"
"s_add_u32 s62, s96, s98\n"
"buffer_load_dwordx4 v116, s[44:47], s62 offen lds\n"
"s_add_u32 m0, s20, 0x1c00\n"
"s_add_u32 s62, s62, s97\n"
"buffer_load_dwordx4 v116, s[44:47], s62 offen lds\n"
"s_add_u32 s96, s96, s99\n"
"s_add_u32 m0, s20, 0x2000\n"
"s_mov_b32 s62, s96\n"
"buffer_load_dwordx4 v116, s[44:47], s62 offen lds\n"
"s_add_u32 m0, s20, 0x2400\n"
"s_add_u32 s62, s96, s97\n"
"buffer_load_dwordx4 v116, s[44:47], s62 offen lds\n"
"s_add_u32 m0, s20, 0x2800\n"
"s_add_u32 s62, s96, s98\n"
"buffer_load_dwordx4 v116, s[44:47], s62 offen lds\n"
"s_add_u32 m0, s20, 0x2c00\n"
"s_add_u32 s62, s62, s97\n"
"buffer_load_dwordx4 v116, s[44:47], s62 offen lds\n"
"s_add_u32 s96, s96, s35\n"
"s_cmp_ge_u32 s91, 0xcc\n"
"s_cbranch_scc1 Lpi0_%=\n"
"s_add_u32 m0, s20, 0x3000\n"
"s_mov_b32 s62, s96\n"
"buffer_load_dwordx4 v116, s[44:47], s62 offen lds\n"
"s_add_u32 m0, s20, 0x3400\n"
"s_add_u32 s62, s96, s97\n"
"buffer_load_dwordx4 v116, s[44:47], s62 offen lds\n"
"Lpi0_%=:\n"
"s_cmp_ge_u32 s91, 0xcc\n"
"s_cbranch_scc1 Lpi1_%=\n"
"s_add_u32 m0, s20, 0x3800\n"
"s_add_u32 s62, s96, s98\n"
"buffer_load_dwordx4 v116, s[44:47], s62 offen lds\n"
"s_add_u32 m0, s20, 0x3c00\n"
"s_add_u32 s62, s62, s97\n"
"buffer_load_dwordx4 v116, s[44:47], s62 offen lds\n"
"Lpi1_%=:\n"
"s_waitcnt vmcnt(4)\n"
"ds_read_b64_tr_b16 a[208:209], v112 offset:4096\n"
"ds_read_b64_tr_b16 a[210:211], v112 offset:5120\n"
"ds_read_b64_tr_b16 a[212:213], v113 offset:4096\n"
"ds_read_b64_tr_b16 a[214:215], v113 offset:5120\n"
"ds_read_b64_tr_b16 a[216:217], v114 offset:4096\n"
"ds_read_b64_tr_b16 a[218:219], v114 offset:5120\n"
"ds_read_b64_tr_b16 a[220:221], v115 offset:4096\n"
"ds_read_b64_tr_b16 a[222:223], v115 offset:5120\n"
"s_waitcnt vmcnt(0)\n"
"ds_read_b64_tr_b16 a[224:225], v112 offset:8192\n"
"ds_read_b64_tr_b16 a[226:227], v112 offset:9216\n"
"ds_read_b64_tr_b16 a[228:229], v113 offset:8192\n"
"ds_read_b64_tr_b16 a[230:231], v113 offset:9216\n"
"ds_read_b64_tr_b16 a[232:233], v114 offset:8192\n"
"ds_read_b64_tr_b16 a[234:235], v114 offset:9216\n"
"ds_read_b64_tr_b16 a[236:237], v115 offset:8192\n"
"ds_read_b64_tr_b16 a[238:239], v115 offset:9216\n"
"s_cmp_ge_u32 s91, 0xcc\n"
"s_cbranch_scc1 Lpr2%=\n"
"s_waitcnt vmcnt(0)\n"
"ds_read_b64_tr_b16 a[240:241], v112 offset:12288\n"
"ds_read_b64_tr_b16 a[242:243], v112 offset:13312\n"
"ds_read_b64_tr_b16 a[244:245], v113 offset:12288\n"
"ds_read_b64_tr_b16 a[246:247], v113 offset:13312\n"
"ds_read_b64_tr_b16 a[248:249], v114 offset:12288\n"
"ds_read_b64_tr_b16 a[250:251], v114 offset:13312\n"
"ds_read_b64_tr_b16 a[252:253], v115 offset:12288\n"
"ds_read_b64_tr_b16 a[254:255], v115 offset:13312\n"
"Lpr2%=:\n"
"s_add_u32 m0, s61, 0x1e00\n"
"s_nop 0\n"
"buffer_load_dwordx4 v6, s[36:39], s92 offen offset:512 lds\n"
"s_add_u32 m0, s61, 0x2200\n"
"s_nop 0\n"
"buffer_load_dwordx4 v6, s[36:39], s93 offen offset:512 lds\n"
"s_add_u32 m0, s61, 0x2580\n"
"s_nop 0\n"
"buffer_load_dwordx4 v6, s[36:39], s92 offen offset:640 lds\n"
"s_add_u32 m0, s61, 0x2980\n"
"s_nop 0\n"
"buffer_load_dwordx4 v6, s[36:39], s93 offen offset:640 lds\n"
"s_add_u32 m0, s61, 0x2d00\n"
"s_nop 0\n"
"buffer_load_dwordx4 v6, s[36:39], s92 offen offset:768 lds\n"
"s_add_u32 m0, s61, 0x3100\n"
"s_nop 0\n"
"buffer_load_dwordx4 v6, s[36:39], s93 offen offset:768 lds\n"
"s_add_u32 m0, s61, 0x3480\n"
"s_nop 0\n"
"buffer_load_dwordx4 v6, s[36:39], s92 offen offset:896 lds\n"
"s_add_u32 m0, s61, 0x3880\n"
"s_nop 0\n"
"buffer_load_dwordx4 v6, s[36:39], s93 offen offset:896 lds\n"
"s_add_u32 m0, s61, 0x3c00\n"
"s_nop 0\n"
"buffer_load_dwordx4 v6, s[36:39], s92 offen offset:1024 lds\n"
"s_add_u32 m0, s61, 0x4000\n"
"s_nop 0\n"
"buffer_load_dwordx4 v6, s[36:39], s93 offen offset:1024 lds\n"
"s_add_u32 m0, s20, 0\n"
"s_nop 0\n"
"buffer_load_dwordx4 v6, s[48:51], s8 offen lds\n"
"s_add_u32 m0, s20, 0x400\n"
"s_nop 0\n"
"buffer_load_dwordx4 v6, s[48:51], s9 offen lds\n"
"s_lshl3_add_u32 s62, s57, s58\n"
"s_lshl_b32 s69, s62, 11\n"
"s_lshr_b32 s64, s68, 1\n"
"s_lshl_b32 s64, s64, 5\n"
"s_and_b32 s63, s68, 1\n"
"s_lshl2_add_u32 s64, s63, s64\n"
"s_and_b32 s63, s56, 1\n"
"s_lshl4_add_u32 s64, s63, s64\n"
"s_mul_i32 s64, s64, 0x480\n"
"s_lshr_b32 s63, s56, 1\n"
"s_lshl_b32 s63, s63, 7\n"
"s_add_u32 s90, s64, s63\n"
"s_lshl_b32 s63, s56, 10\n"
"s_add_u32 m0, s63, 0x22000\n"
"s_nop 0\n"
"buffer_load_dwordx4 v6, s[48:51], s90 offen lds\n"
"s_add_u32 m0, s63, 0x22f00\n"
"s_nop 0\n"
"buffer_load_dwordx4 v6, s[48:51], s90 offen offset:256 lds\n"
"s_add_u32 m0, s63, 0x23e00\n"
"s_nop 0\n"
"buffer_load_dwordx4 v6, s[48:51], s90 offen offset:512 lds\n"
"s_add_u32 m0, s63, 0x24d00\n"
"s_nop 0\n"
"buffer_load_dwordx4 v6, s[48:51], s90 offen offset:768 lds\n"
"s_add_u32 m0, s63, 0x25c00\n"
"s_nop 0\n"
"buffer_load_dwordx4 v6, s[48:51], s90 offen offset:1024 lds\n"
"s_lshl_b32 s63, s56, 7\n"
"s_lshl_b32 s64, s57, 14\n"
"s_add_u32 s22, s63, s64\n"
"v_and_b32 v31, 7, v0\n"
"v_lshlrev_b32 v31, 11, v31\n"
"s_and_b32 s80, s58, 3\n"
"s_xor_b32 s80, s80, s56\n"
"s_xor_b32 s62, s56, 0\n"
"s_lshr_b32 s63, s62, 1\n"
"s_lshl_b32 s63, s63, 5\n"
"s_and_b32 s62, s62, 1\n"
"s_lshl2_add_u32 s62, s62, s63\n"
"s_lshl_b32 s63, s57, 6\n"
"s_add_u32 s62, s62, s63\n"
"s_lshl2_add_u32 s62, s62, s80\n"
"s_lshl_b32 s12, s62, 10\n"
"s_add_u32 s13, s12, 0x10000\n"
"s_xor_b32 s62, s56, 1\n"
"s_lshr_b32 s63, s62, 1\n"
"s_lshl_b32 s63, s63, 5\n"
"s_and_b32 s62, s62, 1\n"
"s_lshl2_add_u32 s62, s62, s63\n"
"s_lshl_b32 s63, s57, 6\n"
"s_add_u32 s62, s62, s63\n"
"s_lshl2_add_u32 s62, s62, s80\n"
"s_lshl_b32 s14, s62, 10\n"
"s_add_u32 s15, s14, 0x10000\n"
"s_xor_b32 s62, s56, 2\n"
"s_lshr_b32 s63, s62, 1\n"
"s_lshl_b32 s63, s63, 5\n"
"s_and_b32 s62, s62, 1\n"
"s_lshl2_add_u32 s62, s62, s63\n"
"s_lshl_b32 s63, s57, 6\n"
"s_add_u32 s62, s62, s63\n"
"s_lshl2_add_u32 s62, s62, s80\n"
"s_lshl_b32 s16, s62, 10\n"
"s_add_u32 s17, s16, 0x10000\n"
"s_xor_b32 s62, s56, 3\n"
"s_lshr_b32 s63, s62, 1\n"
"s_lshl_b32 s63, s63, 5\n"
"s_and_b32 s62, s62, 1\n"
"s_lshl2_add_u32 s62, s62, s63\n"
"s_lshl_b32 s63, s57, 6\n"
"s_add_u32 s62, s62, s63\n"
"s_lshl2_add_u32 s62, s62, s80\n"
"s_lshl_b32 s18, s62, 10\n"
"s_add_u32 s19, s18, 0x10000\n"
"v_add_u32 v2, 0x9000, v1\n"
"v_add_u32 v3, 0x12000, v1\n"
"v_add_u32 v4, 0x1b000, v1\n"
"v_mov_b32 v27, 1\n"
"v_lshrrev_b32 v18, 4, v0\n"
"v_and_b32 v19, 6, v0\n"
"v_xor_b32 v18, v18, v19\n"
"v_and_b32 v19, 15, v0\n"
"v_lshlrev_b32 v19, 7, v19\n"
"v_lshl_add_u32 v18, v18, 4, v19\n"
"v_add_u32 v8, s20, v18\n"
"v_xor_b32 v9, 64, v8\n"
"v_add_u32 v12, s61, v18\n"
"v_xor_b32 v13, 64, v12\n"
"s_cmp_ge_u32 s58, 4\n"
"s_cselect_b32 s70, -1.0, 1.0\n"
"s_cselect_b32 s71, 0, -1\n"
"s_mov_b32 s94, 0xf0000f\n"
"s_mov_b32 s95, 0xf0000f00\n"
"v_mov_b32 v20, 0\n"
"v_mov_b32 v21, 0\n"
"v_mov_b32 v22, 0\n"
"v_mov_b32 v23, 0\n"
"v_mov_b32 v24, 0\n"
"v_mov_b32 v25, 0\n"
"v_and_b32 v18, 1, v0\n"
"v_lshlrev_b32 v18, 4, v18\n"
"v_mov_b32 v19, 0x3f80\n"
"v_lshlrev_b32 v18, v18, v19\n"
"s_and_b32 exec_lo, s94, 0x33333333\n"
"s_and_b32 exec_hi, s95, 0x33333333\n"
"v_mov_b32 v22, v18\n"
"s_and_b32 exec_lo, s94, 0xcccccccc\n"
"s_and_b32 exec_hi, s95, 0xcccccccc\n"
"v_mov_b32 v23, v18\n"
"s_mov_b64 exec, -1\n"
"s_cmp_lg_u32 s56, 0\n"
"s_cbranch_scc1 Lhs1%=\n"
"v_readfirstlane_b32 s62, v15\n"
"s_nop 0\n"
"s_add_u32 s62, s62, 1\n"
"s_max_u32 s62, s62, 1\n"
"v_mov_b32 v15, s62\n"
"s_lshr_b32 s62, s69, 4\n"
"s_add_u32 s62, s62, 0x80000\n"
"s_mov_b64 exec, 1\n"
"buffer_store_dword v15, off, s[24:27], s62 sc1\n"
"s_mov_b64 exec, -1\n"
"v_lshl_add_u32 v18, v0, 3, s58\n"
"v_and_b32 v19, 7, v0\n"
"s_lshl_b32 s62, s57, 3\n"
"v_add_u32 v19, s62, v19\n"
"v_cmp_gt_u32 vcc, 32, v0\n"
"s_nop 1\n"
"v_cndmask_b32 v26, v19, v18, vcc\n"
"v_lshlrev_b32 v26, 11, v26\n"
"Lhs1%=:\n"
"v_subrev_u32 v180, s20, v8\n"
"v_add_u32 v181, 0x9000, v180\n"
"v_add_u32 v10, 0x22000, v180\n"
"v_subrev_u32 v182, s20, v9\n"
"v_add_u32 v183, 0x9000, v182\n"
"v_add_u32 v11, 0x22000, v182\n"
"s_add_u32 m0, s20, 0x780\n"
"s_nop 0\n"
"buffer_load_dwordx4 v6, s[48:51], s8 offen offset:128 lds\n"
"s_add_u32 m0, s20, 0xb80\n"
"s_nop 0\n"
"buffer_load_dwordx4 v6, s[48:51], s9 offen offset:128 lds\n"
"s_add_u32 m0, s20, 0xf00\n"
"s_waitcnt lgkmcnt(8)\n"
"buffer_load_dwordx4 v6, s[48:51], s8 offen offset:256 lds\n"
"s_add_u32 m0, s20, 0x1300\n"
"s_nop 0\n"
"buffer_load_dwordx4 v6, s[48:51], s9 offen offset:256 lds\n"
"s_add_u32 m0, s20, 0x1680\n"
"s_nop 0\n"
"buffer_load_dwordx4 v6, s[48:51], s8 offen offset:384 lds\n"
"s_add_u32 m0, s20, 0x1a80\n"
"s_nop 0\n"
"buffer_load_dwordx4 v6, s[48:51], s9 offen offset:384 lds\n"
"s_add_u32 m0, s20, 0x1e00\n"
"s_waitcnt lgkmcnt(0)\n"
"buffer_load_dwordx4 v6, s[48:51], s8 offen offset:512 lds\n"
"s_add_u32 m0, s20, 0x2200\n"
"s_nop 0\n"
"buffer_load_dwordx4 v6, s[48:51], s9 offen offset:512 lds\n"
"s_add_u32 m0, s20, 0x2580\n"
"s_nop 0\n"
"buffer_load_dwordx4 v6, s[48:51], s8 offen offset:640 lds\n"
"s_add_u32 m0, s20, 0x2980\n"
"s_nop 0\n"
"buffer_load_dwordx4 v6, s[48:51], s9 offen offset:640 lds\n"
"ds_read_b128 v[184:187], v12\n"
"ds_read_b128 v[188:191], v13\n"
"s_add_u32 m0, s20, 0x2d00\n"
"s_waitcnt lgkmcnt(2)\n"
"buffer_load_dwordx4 v6, s[48:51], s8 offen offset:768 lds\n"
"s_add_u32 m0, s20, 0x3100\n"
"s_nop 0\n"
"buffer_load_dwordx4 v6, s[48:51], s9 offen offset:768 lds\n"
"s_waitcnt lgkmcnt(0)\n"
"v_mfma_f32_16x16x32_bf16 v[148:151], v[156:159], v[184:187], 0\n"
"v_mfma_f32_16x16x32_bf16 v[152:155], v[184:187], v[184:187], 0\n"
"v_mfma_f32_16x16x32_bf16 v[148:151], v[156:159], v[188:191], v[148:151]\n"
"v_mfma_f32_16x16x32_bf16 v[152:155], v[188:191], v[188:191], v[152:155]\n"
"ds_read_b128 v[192:195], v12 offset:2048\n"
"ds_read_b128 v[196:199], v13 offset:2048\n"
"s_waitcnt vmcnt(12)\n"
"s_barrier\n"
"ds_read_b128 v[32:35], v8\n"
"ds_read_b128 v[36:39], v9\n"
"s_add_u32 m0, s20, 0x3480\n"
"s_nop 0\n"
"buffer_load_dwordx4 v6, s[48:51], s8 offen offset:896 lds\n"
"s_add_u32 m0, s20, 0x3880\n"
"s_nop 0\n"
"buffer_load_dwordx4 v6, s[48:51], s9 offen offset:896 lds\n"
"s_waitcnt lgkmcnt(2)\n"
"v_mfma_f32_16x16x32_bf16 v[148:151], v[156:159], v[192:195], v[148:151]\n"
"v_mfma_f32_16x16x32_bf16 v[152:155], v[192:195], v[192:195], v[152:155]\n"
"v_mfma_f32_16x16x32_bf16 v[148:151], v[156:159], v[196:199], v[148:151]\n"
"v_mfma_f32_16x16x32_bf16 v[152:155], v[196:199], v[196:199], v[152:155]\n"
"ds_read_b128 v[200:203], v12 offset:4096\n"
"ds_read_b128 v[204:207], v13 offset:4096\n"
"ds_read_b128 v[136:139], v10\n"
"ds_read_b128 v[140:143], v11\n"
"s_waitcnt vmcnt(12)\n"
"ds_read_b128 v[40:43], v8 offset:2048\n"
"ds_read_b128 v[44:47], v9 offset:2048\n"
"s_add_u32 m0, s20, 0xfffffc00\n"
"s_waitcnt lgkmcnt(6)\n"
"buffer_load_dwordx4 v6, s[48:51], s8 offen offset:1024 lds\n"
"s_add_u32 m0, s20, 0\n"
"s_nop 0\n"
"buffer_load_dwordx4 v6, s[48:51], s9 offen offset:1024 lds\n"
"v_mfma_f32_16x16x32_bf16 v[160:163], v[156:159], v[32:35], 0\n"
"v_mfma_f32_16x16x32_bf16 v[160:163], v[156:159], v[36:39], v[160:163]\n"
"s_waitcnt lgkmcnt(4)\n"
"v_mfma_f32_16x16x32_bf16 v[148:151], v[156:159], v[200:203], v[148:151]\n"
"v_mfma_f32_16x16x32_bf16 v[152:155], v[200:203], v[200:203], v[152:155]\n"
"v_mfma_f32_16x16x32_bf16 v[148:151], v[156:159], v[204:207], v[148:151]\n"
"v_mfma_f32_16x16x32_bf16 v[152:155], v[204:207], v[204:207], v[152:155]\n"
"ds_read_b128 v[208:211], v12 offset:6144\n"
"ds_read_b128 v[212:215], v13 offset:6144\n"
"s_waitcnt lgkmcnt(4)\n"
"v_mfma_f32_16x16x32_bf16 v[168:171], v[156:159], v[136:139], 0\n"
"v_mfma_f32_16x16x32_bf16 v[168:171], v[156:159], v[140:143], v[168:171]\n"
"ds_read_b128 v[136:139], v10 offset:2048\n"
"ds_read_b128 v[140:143], v11 offset:2048\n"
"s_waitcnt vmcnt(12)\n"
"ds_read_b128 v[48:51], v8 offset:4096\n"
"ds_read_b128 v[52:55], v9 offset:4096\n"
"s_add_u32 m0, s20, 0x800\n"
"s_waitcnt lgkmcnt(6)\n"
"buffer_load_dwordx4 v6, s[48:51], s10 offen lds\n"
"s_add_u32 m0, s20, 0xc00\n"
"s_nop 0\n"
"buffer_load_dwordx4 v6, s[48:51], s11 offen lds\n"
"v_mfma_f32_16x16x32_bf16 v[160:163], v[156:159], v[40:43], v[160:163]\n"
"v_mfma_f32_16x16x32_bf16 v[160:163], v[156:159], v[44:47], v[160:163]\n"
"s_waitcnt lgkmcnt(4)\n"
"v_mfma_f32_16x16x32_bf16 v[148:151], v[156:159], v[208:211], v[148:151]\n"
"v_mfma_f32_16x16x32_bf16 v[152:155], v[208:211], v[208:211], v[152:155]\n"
"v_mfma_f32_16x16x32_bf16 v[148:151], v[156:159], v[212:215], v[148:151]\n"
"v_mfma_f32_16x16x32_bf16 v[152:155], v[212:215], v[212:215], v[152:155]\n"
"ds_read_b128 v[216:219], v12 offset:8192\n"
"ds_read_b128 v[220:223], v13 offset:8192\n"
"s_waitcnt lgkmcnt(4)\n"
"v_mfma_f32_16x16x32_bf16 v[168:171], v[156:159], v[136:139], v[168:171]\n"
"v_mfma_f32_16x16x32_bf16 v[168:171], v[156:159], v[140:143], v[168:171]\n"
"ds_read_b128 v[136:139], v10 offset:4096\n"
"ds_read_b128 v[140:143], v11 offset:4096\n"
"s_waitcnt vmcnt(12)\n"
"ds_read_b128 v[56:59], v8 offset:6144\n"
"ds_read_b128 v[60:63], v9 offset:6144\n"
"s_add_u32 m0, s20, 0xf80\n"
"s_waitcnt lgkmcnt(6)\n"
"buffer_load_dwordx4 v6, s[48:51], s10 offen offset:128 lds\n"
"s_add_u32 m0, s20, 0x1380\n"
"s_nop 0\n"
"buffer_load_dwordx4 v6, s[48:51], s11 offen offset:128 lds\n"
"v_mfma_f32_16x16x32_bf16 v[160:163], v[156:159], v[48:51], v[160:163]\n"
"v_mfma_f32_16x16x32_bf16 v[160:163], v[156:159], v[52:55], v[160:163]\n"
"s_waitcnt lgkmcnt(4)\n"
"v_mfma_f32_16x16x32_bf16 v[148:151], v[156:159], v[216:219], v[148:151]\n"
"v_mfma_f32_16x16x32_bf16 v[152:155], v[216:219], v[216:219], v[152:155]\n"
"v_mfma_f32_16x16x32_bf16 v[148:151], v[156:159], v[220:223], v[148:151]\n"
"v_mfma_f32_16x16x32_bf16 v[152:155], v[220:223], v[220:223], v[152:155]\n"
"ds_read_b128 v[224:227], v12 offset:10240\n"
"ds_read_b128 v[228:231], v13 offset:10240\n"
"s_waitcnt lgkmcnt(4)\n"
"v_mfma_f32_16x16x32_bf16 v[168:171], v[156:159], v[136:139], v[168:171]\n"
"v_mfma_f32_16x16x32_bf16 v[168:171], v[156:159], v[140:143], v[168:171]\n"
"ds_read_b128 v[136:139], v10 offset:6144\n"
"ds_read_b128 v[140:143], v11 offset:6144\n"
"s_waitcnt vmcnt(12)\n"
"ds_read_b128 v[64:67], v8 offset:8192\n"
"ds_read_b128 v[68:71], v9 offset:8192\n"
"s_add_u32 m0, s20, 0x1700\n"
"s_waitcnt lgkmcnt(6)\n"
"buffer_load_dwordx4 v6, s[48:51], s10 offen offset:256 lds\n"
"s_add_u32 m0, s20, 0x1b00\n"
"s_nop 0\n"
"buffer_load_dwordx4 v6, s[48:51], s11 offen offset:256 lds\n"
"v_mfma_f32_16x16x32_bf16 v[160:163], v[156:159], v[56:59], v[160:163]\n"
"v_mfma_f32_16x16x32_bf16 v[160:163], v[156:159], v[60:63], v[160:163]\n"
"s_waitcnt lgkmcnt(4)\n"
"v_mfma_f32_16x16x32_bf16 v[148:151], v[156:159], v[224:227], v[148:151]\n"
"v_mfma_f32_16x16x32_bf16 v[152:155], v[224:227], v[224:227], v[152:155]\n"
"v_mfma_f32_16x16x32_bf16 v[148:151], v[156:159], v[228:231], v[148:151]\n"
"v_mfma_f32_16x16x32_bf16 v[152:155], v[228:231], v[228:231], v[152:155]\n"
"ds_read_b128 v[232:235], v12 offset:12288\n"
"ds_read_b128 v[236:239], v13 offset:12288\n"
"s_waitcnt lgkmcnt(4)\n"
"v_mfma_f32_16x16x32_bf16 v[168:171], v[156:159], v[136:139], v[168:171]\n"
"v_mfma_f32_16x16x32_bf16 v[168:171], v[156:159], v[140:143], v[168:171]\n"
"ds_read_b128 v[136:139], v10 offset:8192\n"
"ds_read_b128 v[140:143], v11 offset:8192\n"
"s_waitcnt vmcnt(12)\n"
"ds_read_b128 v[72:75], v8 offset:10240\n"
"ds_read_b128 v[76:79], v9 offset:10240\n"
"s_add_u32 m0, s20, 0x1e80\n"
"s_waitcnt lgkmcnt(6)\n"
"buffer_load_dwordx4 v6, s[48:51], s10 offen offset:384 lds\n"
"s_add_u32 m0, s20, 0x2280\n"
"s_nop 0\n"
"buffer_load_dwordx4 v6, s[48:51], s11 offen offset:384 lds\n"
"v_mfma_f32_16x16x32_bf16 v[160:163], v[156:159], v[64:67], v[160:163]\n"
"v_mfma_f32_16x16x32_bf16 v[160:163], v[156:159], v[68:71], v[160:163]\n"
"s_waitcnt lgkmcnt(4)\n"
"v_mfma_f32_16x16x32_bf16 v[148:151], v[156:159], v[232:235], v[148:151]\n"
"v_mfma_f32_16x16x32_bf16 v[152:155], v[232:235], v[232:235], v[152:155]\n"
"v_mfma_f32_16x16x32_bf16 v[148:151], v[156:159], v[236:239], v[148:151]\n"
"v_mfma_f32_16x16x32_bf16 v[152:155], v[236:239], v[236:239], v[152:155]\n"
"ds_read_b128 v[240:243], v12 offset:14336\n"
"ds_read_b128 v[244:247], v13 offset:14336\n"
"s_waitcnt lgkmcnt(4)\n"
"v_mfma_f32_16x16x32_bf16 v[168:171], v[156:159], v[136:139], v[168:171]\n"
"v_mfma_f32_16x16x32_bf16 v[168:171], v[156:159], v[140:143], v[168:171]\n"
"ds_read_b128 v[136:139], v10 offset:10240\n"
"ds_read_b128 v[140:143], v11 offset:10240\n"
"s_waitcnt vmcnt(12)\n"
"ds_read_b128 v[80:83], v8 offset:12288\n"
"ds_read_b128 v[84:87], v9 offset:12288\n"
"s_add_u32 m0, s20, 0x2600\n"
"s_waitcnt lgkmcnt(6)\n"
"buffer_load_dwordx4 v6, s[48:51], s10 offen offset:512 lds\n"
"s_add_u32 m0, s20, 0x2a00\n"
"s_nop 0\n"
"buffer_load_dwordx4 v6, s[48:51], s11 offen offset:512 lds\n"
"v_mfma_f32_16x16x32_bf16 v[160:163], v[156:159], v[72:75], v[160:163]\n"
"v_mfma_f32_16x16x32_bf16 v[160:163], v[156:159], v[76:79], v[160:163]\n"
"s_waitcnt lgkmcnt(4)\n"
"v_mfma_f32_16x16x32_bf16 v[148:151], v[156:159], v[240:243], v[148:151]\n"
"v_mfma_f32_16x16x32_bf16 v[152:155], v[240:243], v[240:243], v[152:155]\n"
"v_mfma_f32_16x16x32_bf16 v[148:151], v[156:159], v[244:247], v[148:151]\n"
"v_mfma_f32_16x16x32_bf16 v[152:155], v[244:247], v[244:247], v[152:155]\n"
"ds_read_b128 v[248:251], v12 offset:16384\n"
"ds_read_b128 v[252:255], v13 offset:16384\n"
"s_waitcnt lgkmcnt(4)\n"
"v_mfma_f32_16x16x32_bf16 v[168:171], v[156:159], v[136:139], v[168:171]\n"
"v_mfma_f32_16x16x32_bf16 v[168:171], v[156:159], v[140:143], v[168:171]\n"
"ds_read_b128 v[136:139], v10 offset:12288\n"
"ds_read_b128 v[140:143], v11 offset:12288\n"
"s_waitcnt vmcnt(12)\n"
"ds_read_b128 v[88:91], v8 offset:14336\n"
"ds_read_b128 v[92:95], v9 offset:14336\n"
"s_add_u32 m0, s20, 0x2d80\n"
"s_waitcnt lgkmcnt(6)\n"
"buffer_load_dwordx4 v6, s[48:51], s10 offen offset:640 lds\n"
"s_add_u32 m0, s20, 0x3180\n"
"s_nop 0\n"
"buffer_load_dwordx4 v6, s[48:51], s11 offen offset:640 lds\n"
"v_mfma_f32_16x16x32_bf16 v[160:163], v[156:159], v[80:83], v[160:163]\n"
"v_mfma_f32_16x16x32_bf16 v[160:163], v[156:159], v[84:87], v[160:163]\n"
"s_waitcnt lgkmcnt(4)\n"
"v_mfma_f32_16x16x32_bf16 v[148:151], v[156:159], v[248:251], v[148:151]\n"
"v_mfma_f32_16x16x32_bf16 v[152:155], v[248:251], v[248:251], v[152:155]\n"
"v_mfma_f32_16x16x32_bf16 v[148:151], v[156:159], v[252:255], v[148:151]\n"
"v_mfma_f32_16x16x32_bf16 v[152:155], v[252:255], v[252:255], v[152:155]\n"
"s_waitcnt lgkmcnt(2)\n"
"v_mfma_f32_16x16x32_bf16 v[168:171], v[156:159], v[136:139], v[168:171]\n"
"v_mfma_f32_16x16x32_bf16 v[168:171], v[156:159], v[140:143], v[168:171]\n"
"ds_read_b128 v[136:139], v10 offset:14336\n"
"ds_read_b128 v[140:143], v11 offset:14336\n"
"s_waitcnt vmcnt(12)\n"
"ds_read_b128 v[96:99], v8\n"
"ds_read_b128 v[100:103], v9\n"
"s_add_u32 m0, s20, 0x3500\n"
"s_waitcnt lgkmcnt(4)\n"
"buffer_load_dwordx4 v6, s[48:51], s10 offen offset:768 lds\n"
"s_add_u32 m0, s20, 0x3900\n"
"s_nop 0\n"
"buffer_load_dwordx4 v6, s[48:51], s11 offen offset:768 lds\n"
"v_mfma_f32_16x16x32_bf16 v[160:163], v[156:159], v[88:91], v[160:163]\n"
"v_mfma_f32_16x16x32_bf16 v[160:163], v[156:159], v[92:95], v[160:163]\n"
"s_waitcnt lgkmcnt(2)\n"
"v_mfma_f32_16x16x32_bf16 v[168:171], v[156:159], v[136:139], v[168:171]\n"
"v_mfma_f32_16x16x32_bf16 v[168:171], v[156:159], v[140:143], v[168:171]\n"
"ds_read_b128 v[136:139], v10 offset:16384\n"
"ds_read_b128 v[140:143], v11 offset:16384\n"
"s_waitcnt vmcnt(12)\n"
"ds_read_b128 a[136:139], v8 offset:2048\n"
"ds_read_b128 a[140:143], v9 offset:2048\n"
"s_add_u32 m0, s20, 0xfffffc80\n"
"s_waitcnt lgkmcnt(4)\n"
"buffer_load_dwordx4 v6, s[48:51], s10 offen offset:896 lds\n"
"s_add_u32 m0, s20, 0x80\n"
"s_nop 0\n"
"buffer_load_dwordx4 v6, s[48:51], s11 offen offset:896 lds\n"
"v_mfma_f32_16x16x32_bf16 v[160:163], v[156:159], v[96:99], v[160:163]\n"
"v_mfma_f32_16x16x32_bf16 v[160:163], v[156:159], v[100:103], v[160:163]\n"
"s_waitcnt lgkmcnt(2)\n"
"v_mfma_f32_16x16x32_bf16 v[168:171], v[156:159], v[136:139], v[168:171]\n"
"v_mfma_f32_16x16x32_bf16 v[168:171], v[156:159], v[140:143], v[168:171]\n"
"s_waitcnt vmcnt(12)\n"
"ds_read_b128 a[144:147], v8 offset:4096\n"
"ds_read_b128 a[148:151], v9 offset:4096\n"
"s_add_u32 m0, s20, 0x400\n"
"s_waitcnt lgkmcnt(2)\n"
"buffer_load_dwordx4 v6, s[48:51], s10 offen offset:1024 lds\n"
"s_add_u32 m0, s20, 0x800\n"
"s_nop 0\n"
"buffer_load_dwordx4 v6, s[48:51], s11 offen offset:1024 lds\n"
"v_mfma_f32_16x16x32_bf16 v[164:167], v[156:159], a[136:139], 0\n"
"v_mfma_f32_16x16x32_bf16 v[164:167], v[156:159], a[140:143], v[164:167]\n"
"s_cmp_ge_u32 s91, 0xcc\n"
"s_cbranch_scc1 Lpt1%=\n"
"v_mfma_f32_16x16x32_bf16 v[120:123], a[208:211], v[156:159], 0\n"
"v_mfma_f32_16x16x32_bf16 v[124:127], a[212:215], v[156:159], 0\n"
"v_mfma_f32_16x16x32_bf16 v[128:131], a[216:219], v[156:159], 0\n"
"v_mfma_f32_16x16x32_bf16 v[132:135], a[220:223], v[156:159], 0\n"
"v_mfma_f32_16x16x32_bf16 v[120:123], a[224:227], v[156:159], v[120:123]\n"
"v_mfma_f32_16x16x32_bf16 v[124:127], a[228:231], v[156:159], v[124:127]\n"
"v_mfma_f32_16x16x32_bf16 v[128:131], a[232:235], v[156:159], v[128:131]\n"
"v_mfma_f32_16x16x32_bf16 v[132:135], a[236:239], v[156:159], v[132:135]\n"
"v_mfma_f32_16x16x32_bf16 v[120:123], a[240:243], v[156:159], v[120:123]\n"
"v_mfma_f32_16x16x32_bf16 v[124:127], a[244:247], v[156:159], v[124:127]\n"
"v_mfma_f32_16x16x32_bf16 v[128:131], a[248:251], v[156:159], v[128:131]\n"
"v_mfma_f32_16x16x32_bf16 v[132:135], a[252:255], v[156:159], v[132:135]\n"
"v_and_b32 v18, 3, v0\n"
"v_and_b32 v19, 48, v0\n"
"v_lshl_add_u32 v18, v18, 6, v19\n"
"s_lshl_b32 s62, s58, 15\n"
"s_mul_i32 s63, s34, 18\n"
"s_add_u32 s63, s63, s33\n"
"s_lshl_b32 s63, s63, 8\n"
"s_add_u32 s62, s62, s63\n"
"s_add_u32 s62, s62, 0x4660000\n"
"s_mov_b32 exec_lo, 0x20002\n"
"s_mov_b32 exec_hi, 0x20002\n"
"v_mov_b32 v120, v124\n"
"v_mov_b32 v121, v125\n"
"v_mov_b32 v122, v126\n"
"v_mov_b32 v123, v127\n"
"s_mov_b32 exec_lo, 0x40004\n"
"s_mov_b32 exec_hi, 0x40004\n"
"v_mov_b32 v120, v128\n"
"v_mov_b32 v121, v129\n"
"v_mov_b32 v122, v130\n"
"v_mov_b32 v123, v131\n"
"s_mov_b32 exec_lo, 0x80008\n"
"s_mov_b32 exec_hi, 0x80008\n"
"v_mov_b32 v120, v132\n"
"v_mov_b32 v121, v133\n"
"v_mov_b32 v122, v134\n"
"v_mov_b32 v123, v135\n"
"s_mov_b32 exec_lo, 0xf000f\n"
"s_mov_b32 exec_hi, 0xf000f\n"
"buffer_store_dwordx4 v[120:123], v18, s[24:27], s62 offen sc1\n"
"s_mov_b64 exec, -1\n"
"Lpt1%=:\n"
"s_waitcnt vmcnt(12)\n"
"ds_read_b128 a[152:155], v8 offset:6144\n"
"ds_read_b128 a[156:159], v9 offset:6144\n"
"s_add_u32 m0, s20, 0x1000\n"
"s_waitcnt lgkmcnt(2)\n"
"buffer_load_dwordx4 v7, s[28:31], s12 offen lds\n"
"s_add_u32 m0, s20, 0x1400\n"
"s_nop 0\n"
"buffer_load_dwordx4 v7, s[28:31], s13 offen lds\n"
"v_mfma_f32_16x16x32_bf16 v[164:167], v[156:159], a[144:147], v[164:167]\n"
"v_mfma_f32_16x16x32_bf16 v[164:167], v[156:159], a[148:151], v[164:167]\n"
"s_waitcnt vmcnt(12)\n"
"ds_read_b128 a[160:163], v8 offset:8192\n"
"ds_read_b128 a[164:167], v9 offset:8192\n"
"s_add_u32 m0, s20, 0x1780\n"
"s_waitcnt lgkmcnt(2)\n"
"buffer_load_dwordx4 v7, s[28:31], s12 offen offset:128 lds\n"
"s_add_u32 m0, s20, 0x1b80\n"
"s_nop 0\n"
"buffer_load_dwordx4 v7, s[28:31], s13 offen offset:128 lds\n"
"v_mfma_f32_16x16x32_bf16 v[164:167], v[156:159], a[152:155], v[164:167]\n"
"v_mfma_f32_16x16x32_bf16 v[164:167], v[156:159], a[156:159], v[164:167]\n"
"s_waitcnt vmcnt(12)\n"
"ds_read_b128 a[168:171], v8 offset:10240\n"
"ds_read_b128 a[172:175], v9 offset:10240\n"
"s_add_u32 m0, s20, 0x1f00\n"
"s_waitcnt lgkmcnt(2)\n"
"buffer_load_dwordx4 v7, s[28:31], s12 offen offset:256 lds\n"
"s_add_u32 m0, s20, 0x2300\n"
"s_nop 0\n"
"buffer_load_dwordx4 v7, s[28:31], s13 offen offset:256 lds\n"
"v_mfma_f32_16x16x32_bf16 v[164:167], v[156:159], a[160:163], v[164:167]\n"
"v_mfma_f32_16x16x32_bf16 v[164:167], v[156:159], a[164:167], v[164:167]\n"
"s_waitcnt vmcnt(12)\n"
"ds_read_b128 a[176:179], v8 offset:12288\n"
"ds_read_b128 a[180:183], v9 offset:12288\n"
"s_add_u32 m0, s20, 0x2680\n"
"s_waitcnt lgkmcnt(2)\n"
"buffer_load_dwordx4 v7, s[28:31], s12 offen offset:384 lds\n"
"s_add_u32 m0, s20, 0x2a80\n"
"s_nop 0\n"
"buffer_load_dwordx4 v7, s[28:31], s13 offen offset:384 lds\n"
"v_mfma_f32_16x16x32_bf16 v[164:167], v[156:159], a[168:171], v[164:167]\n"
"v_mfma_f32_16x16x32_bf16 v[164:167], v[156:159], a[172:175], v[164:167]\n"
"s_waitcnt vmcnt(12)\n"
"ds_read_b128 a[184:187], v8 offset:14336\n"
"ds_read_b128 a[188:191], v9 offset:14336\n"
"s_add_u32 m0, s20, 0x2e00\n"
"s_waitcnt lgkmcnt(2)\n"
"buffer_load_dwordx4 v7, s[28:31], s12 offen offset:512 lds\n"
"s_add_u32 m0, s20, 0x3200\n"
"s_nop 0\n"
"buffer_load_dwordx4 v7, s[28:31], s13 offen offset:512 lds\n"
"v_mfma_f32_16x16x32_bf16 v[164:167], v[156:159], a[176:179], v[164:167]\n"
"v_mfma_f32_16x16x32_bf16 v[164:167], v[156:159], a[180:183], v[164:167]\n"
"s_waitcnt vmcnt(12)\n"
"ds_read_b128 a[192:195], v8\n"
"ds_read_b128 a[196:199], v9\n"
"s_add_u32 m0, s20, 0x3580\n"
"s_waitcnt lgkmcnt(2)\n"
"buffer_load_dwordx4 v7, s[28:31], s12 offen offset:640 lds\n"
"s_add_u32 m0, s20, 0x3980\n"
"s_nop 0\n"
"buffer_load_dwordx4 v7, s[28:31], s13 offen offset:640 lds\n"
"v_mfma_f32_16x16x32_bf16 v[164:167], v[156:159], a[184:187], v[164:167]\n"
"v_mfma_f32_16x16x32_bf16 v[164:167], v[156:159], a[188:191], v[164:167]\n"
"s_waitcnt vmcnt(12)\n"
"ds_read_b128 a[200:203], v8 offset:2048\n"
"ds_read_b128 a[204:207], v9 offset:2048\n"
"s_add_u32 m0, s20, 0xfffffd00\n"
"s_waitcnt lgkmcnt(2)\n"
"buffer_load_dwordx4 v7, s[28:31], s12 offen offset:768 lds\n"
"s_add_u32 m0, s20, 0x100\n"
"s_nop 0\n"
"buffer_load_dwordx4 v7, s[28:31], s13 offen offset:768 lds\n"
"v_mfma_f32_16x16x32_bf16 v[164:167], v[156:159], a[192:195], v[164:167]\n"
"v_mfma_f32_16x16x32_bf16 v[164:167], v[156:159], a[196:199], v[164:167]\n"
"s_waitcnt lgkmcnt(0)\n"
"v_mfma_f32_16x16x32_bf16 v[164:167], v[156:159], a[200:203], v[164:167]\n"
"v_mfma_f32_16x16x32_bf16 v[164:167], v[156:159], a[204:207], v[164:167]\n"
"v_and_b32 v19, 15, v0\n"
"s_mul_i32 s62, s56, 0xc0\n"
"s_add_u32 s62, s62, 0x27b00\n"
"v_lshl_add_u32 v19, v19, 2, s62\n"
"ds_write_b32 v19, v160\n"
"s_nop 2\n"
"ds_write_b32 v19, v164 offset:64\n"
"ds_write_b32 v19, v168 offset:128\n"
"s_cmp_lg_u32 s56, 0\n"
"s_cbranch_scc1 Lhs2w%=\n"
"v_lshrrev_b32 v18, 4, v26\n"
"s_mov_b32 s62, 0x80000\n"
"v_mov_b32 v14, 0\n"
"s_bfm_b64 exec, 40, 0\n"
"buffer_atomic_or v14, v18, s[24:27], s62 offen sc0 sc1\n"
"s_mov_b64 exec, -1\n"
"Lhs2w%=:\n"
"Llayer%=:\n"
"v_and_b32 v18, 15, v0\n"
"s_lshl_b32 s62, s56, 6\n"
"s_add_u32 s62, s62, 0x27000\n"
"v_lshl_add_u32 v18, v18, 2, s62\n"
"s_mov_b32 vcc_lo, 0xaaaaaaaa\n"
"s_mov_b32 vcc_hi, 0xaaaaaaaa\n"
"v_mul_f32 v30, 0xbae38e39, v148\n"
"v_cndmask_b32 v152, v152, v153, vcc\n"
"v_cndmask_b32 v154, v154, v155, vcc\n"
"s_mov_b32 vcc_lo, 0xcccccccc\n"
"s_mov_b32 vcc_hi, 0xcccccccc\n"
"v_mul_f32 v19, v30, v30\n"
"v_cndmask_b32 v152, v152, v154, vcc\n"
"v_mul_f32 v152, 0x3ae38e39, v152\n"
"v_sub_f32 v152, v152, v19\n"
"v_max_f32 v152, 0, v152\n"
"v_cvt_pk_bf16_f32 v19, v30, v152\n"
"v_and_b32 v152, 0xffff0000, v19\n"
"v_lshlrev_b32 v30, 16, v19\n"
"v_add_f32 v152, 0x3727c5ac, v152\n"
"v_cvt_pk_bf16_f32 v152, 0, v152\n"
"v_sqrt_f32 v152, v152\n"
"s_nop 0\n"
"v_cvt_pk_bf16_f32 v152, 0, v152\n"
"v_rcp_f32 v152, v152\n"
"s_nop 0\n"
"v_mul_f32 v152, s70, v152\n"
"v_mul_f32 v30, v30, v152\n"
"s_mov_b64 exec, s[94:95]\n"
"ds_write_b32 v18, v30 offset:256\n"
"ds_write_b32 v18, v152\n"
"s_mov_b64 exec, -1\n"
"s_sub_u32 s62, 0x22000, s20\n"
"v_add_u32 v10, s62, v8\n"
"v_add_u32 v11, s62, v9\n"
"s_waitcnt lgkmcnt(0)\n"
"s_barrier\n"
"ds_read_b128 v[140:143], v180\n"
"ds_read_b128 v[144:147], v180 offset:18432\n"
"ds_read_b128 v[148:151], v10\n"
"ds_read_b128 v[152:155], v182\n"
"ds_read_b128 v[156:159], v182 offset:18432\n"
"ds_read_b128 v[160:163], v11\n"
"s_waitcnt lgkmcnt(3)\n"
"v_mfma_f32_16x16x32_bf16 v[104:107], v[140:143], v[32:35], 0\n"
"ds_read_b128 v[164:167], v180 offset:2048\n"
"s_lshr_b32 s62, s58, 2\n"
"v_mfma_f32_16x16x32_bf16 v[120:123], v[140:143], a[136:139], 0\n"
"ds_read_b128 v[168:171], v180 offset:20480\n"
"s_mul_i32 s62, s62, 0x240000\n"
"v_mfma_f32_16x16x32_bf16 v[108:111], v[144:147], v[32:35], 0\n"
"ds_read_b128 v[172:175], v10 offset:2048\n"
"s_mul_i32 s63, s59, 0x480000\n"
"v_mfma_f32_16x16x32_bf16 v[124:127], v[144:147], a[136:139], 0\n"
"s_waitcnt vmcnt(12)\n"
"ds_read_b128 a[0:3], v8 offset:4096\n"
"ds_read_b128 a[4:7], v9 offset:4096\n"
"v_mfma_f32_16x16x32_bf16 v[136:139], v[184:187], v[148:151], 0\n"
"s_add_u32 m0, s20, 0x480\n"
"s_nop 0\n"
"buffer_load_dwordx4 v7, s[28:31], s12 offen offset:896 lds\n"
"s_add_u32 m0, s20, 0x880\n"
"s_waitcnt lgkmcnt(5)\n"
"v_mfma_f32_16x16x32_bf16 v[104:107], v[152:155], v[36:39], v[104:107]\n"
"ds_read_b128 v[140:143], v182 offset:2048\n"
"s_add_u32 s63, s63, 0x400000\n"
"v_mfma_f32_16x16x32_bf16 v[120:123], v[152:155], a[140:143], v[120:123]\n"
"ds_read_b128 v[144:147], v182 offset:20480\n"
"s_add_u32 s77, s62, s63\n"
"v_mfma_f32_16x16x32_bf16 v[108:111], v[156:159], v[36:39], v[108:111]\n"
"ds_read_b128 v[148:151], v11 offset:2048\n"
"s_lshl1_add_u32 s62, s56, s67\n"
"v_mfma_f32_16x16x32_bf16 v[124:127], v[156:159], a[140:143], v[124:127]\n"
"buffer_load_dwordx4 v7, s[28:31], s13 offen offset:896 lds\n"
"v_mfma_f32_16x16x32_bf16 v[136:139], v[188:191], v[160:163], v[136:139]\n"
"s_waitcnt vmcnt(12)\n"
"ds_read_b128 a[8:11], v8 offset:6144\n"
"s_waitcnt lgkmcnt(6)\n"
"v_mfma_f32_16x16x32_bf16 v[104:107], v[164:167], v[40:43], v[104:107]\n"
"ds_read_b128 v[152:155], v180 offset:4096\n"
"s_mul_i32 s62, s62, 0x10000\n"
"v_mfma_f32_16x16x32_bf16 v[120:123], v[164:167], a[144:147], v[120:123]\n"
"ds_read_b128 v[156:159], v180 offset:22528\n"
"s_lshl_b32 s63, s57, 11\n"
"v_mfma_f32_16x16x32_bf16 v[108:111], v[168:171], v[40:43], v[108:111]\n"
"ds_read_b128 v[160:163], v10 offset:4096\n"
"s_add_u32 s63, s63, s77\n"
"v_mfma_f32_16x16x32_bf16 v[124:127], v[168:171], a[144:147], v[124:127]\n"
"ds_read_b128 a[12:15], v9 offset:6144\n"
"v_mfma_f32_16x16x32_bf16 v[136:139], v[192:195], v[172:175], v[136:139]\n"
"s_add_u32 m0, s20, 0x1000\n"
"s_waitcnt lgkmcnt(8)\n"
"buffer_load_dwordx4 v7, s[28:31], s14 offen lds\n"
"s_add_u32 m0, s20, 0x1400\n"
"s_waitcnt lgkmcnt(5)\n"
"v_mfma_f32_16x16x32_bf16 v[104:107], v[140:143], v[44:47], v[104:107]\n"
"ds_read_b128 v[164:167], v182 offset:4096\n"
"s_add_u32 s65, s62, s63\n"
"v_mfma_f32_16x16x32_bf16 v[120:123], v[140:143], a[148:151], v[120:123]\n"
"ds_read_b128 v[168:171], v182 offset:22528\n"
"s_add_u32 s66, s65, 0x10000\n"
"v_mfma_f32_16x16x32_bf16 v[108:111], v[144:147], v[44:47], v[108:111]\n"
"ds_read_b128 v[172:175], v11 offset:4096\n"
"s_mul_i32 s62, s68, 0x10000\n"
"v_mfma_f32_16x16x32_bf16 v[124:127], v[144:147], a[148:151], v[124:127]\n"
"buffer_load_dwordx4 v7, s[28:31], s15 offen lds\n"
"v_mfma_f32_16x16x32_bf16 v[136:139], v[196:199], v[148:151], v[136:139]\n"
"s_waitcnt vmcnt(12)\n"
"ds_read_b128 a[16:19], v8 offset:8192\n"
"ds_read_b128 a[20:23], v9 offset:8192\n"
"s_waitcnt lgkmcnt(6)\n"
"v_mfma_f32_16x16x32_bf16 v[104:107], v[152:155], v[48:51], v[104:107]\n"
"ds_read_b128 v[140:143], v180 offset:6144\n"
"s_add_u32 s63, s63, s62\n"
"v_mfma_f32_16x16x32_bf16 v[120:123], v[152:155], a[152:155], v[120:123]\n"
"ds_read_b128 v[144:147], v180 offset:24576\n"
"s_lshl_b32 s62, s56, 9\n"
"v_mfma_f32_16x16x32_bf16 v[108:111], v[156:159], v[48:51], v[108:111]\n"
"ds_read_b128 v[148:151], v10 offset:6144\n"
"s_add_u32 s64, s63, s62\n"
"v_mfma_f32_16x16x32_bf16 v[124:127], v[156:159], a[152:155], v[124:127]\n"
"s_add_u32 m0, s20, 0x1780\n"
"s_waitcnt lgkmcnt(8)\n"
"buffer_load_dwordx4 v7, s[28:31], s14 offen offset:128 lds\n"
"s_add_u32 m0, s20, 0x1b80\n"
"v_mfma_f32_16x16x32_bf16 v[136:139], v[200:203], v[160:163], v[136:139]\n"
"buffer_load_dwordx4 v7, s[28:31], s15 offen offset:128 lds\n"
"s_waitcnt lgkmcnt(5)\n"
"v_mfma_f32_16x16x32_bf16 v[104:107], v[164:167], v[52:55], v[104:107]\n"
"ds_read_b128 v[152:155], v182 offset:6144\n"
"v_mfma_f32_16x16x32_bf16 v[120:123], v[164:167], a[156:159], v[120:123]\n"
"ds_read_b128 v[156:159], v182 offset:24576\n"
"v_mfma_f32_16x16x32_bf16 v[108:111], v[168:171], v[52:55], v[108:111]\n"
"ds_read_b128 v[160:163], v11 offset:6144\n"
"v_mfma_f32_16x16x32_bf16 v[124:127], v[168:171], a[156:159], v[124:127]\n"
"s_waitcnt vmcnt(12)\n"
"ds_read_b128 a[24:27], v8 offset:10240\n"
"v_mfma_f32_16x16x32_bf16 v[136:139], v[204:207], v[172:175], v[136:139]\n"
"ds_read_b128 a[28:31], v9 offset:10240\n"
"s_waitcnt lgkmcnt(5)\n"
"v_mfma_f32_16x16x32_bf16 v[104:107], v[140:143], v[56:59], v[104:107]\n"
"ds_read_b128 v[164:167], v180 offset:8192\n"
"s_mov_b32 s62, s65\n"
"s_bitcmp1_b32 s57, 0\n"
"s_cselect_b32 s65, s66, s62\n"
"s_cselect_b32 s66, s62, s66\n"
"v_mfma_f32_16x16x32_bf16 v[120:123], v[140:143], a[160:163], v[120:123]\n"
"ds_read_b128 v[168:171], v180 offset:26624\n"
"v_mfma_f32_16x16x32_bf16 v[108:111], v[144:147], v[56:59], v[108:111]\n"
"ds_read_b128 v[172:175], v10 offset:8192\n"
"v_mfma_f32_16x16x32_bf16 v[124:127], v[144:147], a[160:163], v[124:127]\n"
"s_add_u32 m0, s20, 0x1f00\n"
"s_nop 0\n"
"buffer_load_dwordx4 v7, s[28:31], s14 offen offset:256 lds\n"
"s_add_u32 m0, s20, 0x2300\n"
"v_mfma_f32_16x16x32_bf16 v[136:139], v[208:211], v[148:151], v[136:139]\n"
"buffer_load_dwordx4 v7, s[28:31], s15 offen offset:256 lds\n"
"s_waitcnt lgkmcnt(5)\n"
"v_mfma_f32_16x16x32_bf16 v[104:107], v[152:155], v[60:63], v[104:107]\n"
"ds_read_b128 v[140:143], v182 offset:8192\n"
"v_and_b32 v19, 15, v0\n"
"s_mul_i32 s62, s56, 0xc0\n"
"s_add_u32 s62, s62, 0x27b00\n"
"v_lshl_add_u32 v19, v19, 2, s62\n"
"v_mfma_f32_16x16x32_bf16 v[120:123], v[152:155], a[164:167], v[120:123]\n"
"ds_read_b128 v[144:147], v182 offset:26624\n"
"v_mfma_f32_16x16x32_bf16 v[108:111], v[156:159], v[60:63], v[108:111]\n"
"ds_read_b128 v[148:151], v11 offset:8192\n"
"v_mfma_f32_16x16x32_bf16 v[124:127], v[156:159], a[164:167], v[124:127]\n"
"s_waitcnt vmcnt(12)\n"
"ds_read_b128 a[32:35], v8 offset:12288\n"
"ds_read_b128 a[36:39], v9 offset:12288\n"
"ds_read_b32 v16, v19\n"
"v_mfma_f32_16x16x32_bf16 v[136:139], v[212:215], v[160:163], v[136:139]\n"
"s_add_u32 m0, s20, 0x2680\n"
"s_waitcnt lgkmcnt(9)\n"
"buffer_load_dwordx4 v7, s[28:31], s14 offen offset:384 lds\n"
"s_add_u32 m0, s20, 0x2a80\n"
"ds_read_b32 v17, v19 offset:64\n"
"s_waitcnt lgkmcnt(7)\n"
"v_mfma_f32_16x16x32_bf16 v[104:107], v[164:167], v[64:67], v[104:107]\n"
"ds_read_b128 v[152:155], v180 offset:10240\n"
"v_mfma_f32_16x16x32_bf16 v[120:123], v[164:167], a[168:171], v[120:123]\n"
"ds_read_b128 v[156:159], v180 offset:28672\n"
"v_mfma_f32_16x16x32_bf16 v[108:111], v[168:171], v[64:67], v[108:111]\n"
"ds_read_b128 v[160:163], v10 offset:10240\n"
"v_mfma_f32_16x16x32_bf16 v[124:127], v[168:171], a[168:171], v[124:127]\n"
"buffer_load_dwordx4 v7, s[28:31], s15 offen offset:384 lds\n"
"ds_read_b32 v30, v19 offset:128\n"
"v_mfma_f32_16x16x32_bf16 v[136:139], v[216:219], v[172:175], v[136:139]\n"
"s_waitcnt vmcnt(12)\n"
"ds_read_b128 a[40:43], v8 offset:14336\n"
"s_waitcnt lgkmcnt(9)\n"
"v_mfma_f32_16x16x32_bf16 v[104:107], v[140:143], v[68:71], v[104:107]\n"
"ds_read_b128 v[164:167], v182 offset:10240\n"
"v_and_b32 v18, 48, v0\n"
"v_mfma_f32_16x16x32_bf16 v[120:123], v[140:143], a[172:175], v[120:123]\n"
"ds_read_b128 v[168:171], v182 offset:28672\n"
"v_add_u32 v18, 0x27000, v18\n"
"v_mfma_f32_16x16x32_bf16 v[108:111], v[144:147], v[68:71], v[108:111]\n"
"ds_read_b128 v[172:175], v11 offset:10240\n"
"s_lshl_b32 s62, s56, 6\n"
"v_mfma_f32_16x16x32_bf16 v[124:127], v[144:147], a[172:175], v[124:127]\n"
"ds_read_b128 a[44:47], v9 offset:14336\n"
"v_mfma_f32_16x16x32_bf16 v[136:139], v[220:223], v[148:151], v[136:139]\n"
"s_add_u32 m0, s20, 0x2e00\n"
"s_waitcnt lgkmcnt(11)\n"
"buffer_load_dwordx4 v7, s[28:31], s14 offen offset:512 lds\n"
"s_add_u32 m0, s20, 0x3200\n"
"s_waitcnt lgkmcnt(6)\n"
"v_mfma_f32_16x16x32_bf16 v[104:107], v[152:155], v[72:75], v[104:107]\n"
"ds_read_b128 v[140:143], v180 offset:12288\n"
"v_add_u32 v19, s62, v18\n"
"v_mfma_f32_16x16x32_bf16 v[120:123], v[152:155], a[176:179], v[120:123]\n"
"ds_read_b128 v[144:147], v180 offset:30720\n"
"v_mfma_f32_16x16x32_bf16 v[108:111], v[156:159], v[72:75], v[108:111]\n"
"ds_read_b128 v[148:151], v10 offset:12288\n"
"v_mfma_f32_16x16x32_bf16 v[124:127], v[156:159], a[176:179], v[124:127]\n"
"buffer_load_dwordx4 v7, s[28:31], s15 offen offset:512 lds\n"
"ds_read_b128 v[184:187], v18\n"
"v_mfma_f32_16x16x32_bf16 v[136:139], v[224:227], v[160:163], v[136:139]\n"
"s_waitcnt vmcnt(12)\n"
"ds_read_b128 a[48:51], v8\n"
"ds_read_b128 a[52:55], v9\n"
"ds_read_b128 v[188:191], v18 offset:64\n"
"s_waitcnt lgkmcnt(8)\n"
"v_mfma_f32_16x16x32_bf16 v[104:107], v[164:167], v[76:79], v[104:107]\n"
"ds_read_b128 v[152:155], v182 offset:12288\n"
"v_mfma_f32_16x16x32_bf16 v[120:123], v[164:167], a[180:183], v[120:123]\n"
"ds_read_b128 v[156:159], v182 offset:30720\n"
"v_mfma_f32_16x16x32_bf16 v[108:111], v[168:171], v[76:79], v[108:111]\n"
"ds_read_b128 v[160:163], v11 offset:12288\n"
"v_mfma_f32_16x16x32_bf16 v[124:127], v[168:171], a[180:183], v[124:127]\n"
"s_add_u32 m0, s20, 0x3580\n"
"s_waitcnt lgkmcnt(10)\n"
"buffer_load_dwordx4 v7, s[28:31], s14 offen offset:640 lds\n"
"s_add_u32 m0, s20, 0x3980\n"
"ds_read_b128 v[192:195], v18 offset:128\n"
"v_mfma_f32_16x16x32_bf16 v[136:139], v[228:231], v[172:175], v[136:139]\n"
"buffer_load_dwordx4 v7, s[28:31], s15 offen offset:640 lds\n"
"ds_read_b128 v[196:199], v18 offset:192\n"
"s_waitcnt lgkmcnt(9)\n"
"v_mfma_f32_16x16x32_bf16 v[104:107], v[140:143], v[80:83], v[104:107]\n"
"ds_read_b128 v[164:167], v180 offset:14336\n"
"v_mfma_f32_16x16x32_bf16 v[120:123], v[140:143], a[184:187], v[120:123]\n"
"ds_read_b128 v[168:171], v180 offset:32768\n"
"v_mfma_f32_16x16x32_bf16 v[108:111], v[144:147], v[80:83], v[108:111]\n"
"ds_read_b128 v[172:175], v10 offset:14336\n"
"v_mfma_f32_16x16x32_bf16 v[124:127], v[144:147], a[184:187], v[124:127]\n"
"s_waitcnt vmcnt(12)\n"
"ds_read_b128 a[56:59], v8 offset:2048\n"
"ds_read_b128 v[200:203], v19\n"
"v_mfma_f32_16x16x32_bf16 v[136:139], v[232:235], v[148:151], v[136:139]\n"
"ds_read_b128 a[60:63], v9 offset:2048\n"
"ds_read_b128 v[204:207], v18 offset:256\n"
"s_waitcnt lgkmcnt(9)\n"
"v_mfma_f32_16x16x32_bf16 v[104:107], v[152:155], v[84:87], v[104:107]\n"
"ds_read_b128 v[140:143], v182 offset:14336\n"
"v_mfma_f32_16x16x32_bf16 v[120:123], v[152:155], a[188:191], v[120:123]\n"
"ds_read_b128 v[144:147], v182 offset:32768\n"
"v_mfma_f32_16x16x32_bf16 v[108:111], v[156:159], v[84:87], v[108:111]\n"
"ds_read_b128 v[148:151], v11 offset:14336\n"
"v_mfma_f32_16x16x32_bf16 v[124:127], v[156:159], a[188:191], v[124:127]\n"
"s_add_u32 m0, s20, 0xfffffd00\n"
"s_nop 0\n"
"buffer_load_dwordx4 v7, s[28:31], s14 offen offset:768 lds\n"
"s_add_u32 m0, s20, 0x100\n"
"ds_read_b128 v[208:211], v18 offset:320\n"
"v_mfma_f32_16x16x32_bf16 v[136:139], v[236:239], v[160:163], v[136:139]\n"
"buffer_load_dwordx4 v7, s[28:31], s15 offen offset:768 lds\n"
"ds_read_b128 v[212:215], v18 offset:384\n"
"s_waitcnt lgkmcnt(9)\n"
"v_mfma_f32_16x16x32_bf16 v[104:107], v[164:167], v[88:91], v[104:107]\n"
"ds_read_b128 v[152:155], v180 offset:16384\n"
"v_mfma_f32_16x16x32_bf16 v[120:123], v[164:167], a[192:195], v[120:123]\n"
"ds_read_b128 v[156:159], v180 offset:34816\n"
"v_mfma_f32_16x16x32_bf16 v[108:111], v[168:171], v[88:91], v[108:111]\n"
"ds_read_b128 v[160:163], v10 offset:16384\n"
"v_mfma_f32_16x16x32_bf16 v[124:127], v[168:171], a[192:195], v[124:127]\n"
"s_waitcnt vmcnt(12)\n"
"ds_read_b128 a[64:67], v8 offset:4096\n"
"ds_read_b128 a[68:71], v9 offset:4096\n"
"ds_read_b128 v[216:219], v18 offset:448\n"
"v_mfma_f32_16x16x32_bf16 v[136:139], v[240:243], v[172:175], v[136:139]\n"
"s_add_u32 m0, s20, 0x480\n"
"s_waitcnt lgkmcnt(12)\n"
"buffer_load_dwordx4 v7, s[28:31], s14 offen offset:896 lds\n"
"s_add_u32 m0, s20, 0x880\n"
"ds_read_b128 v[220:223], v19 offset:256\n"
"s_waitcnt lgkmcnt(9)\n"
"v_mfma_f32_16x16x32_bf16 v[104:107], v[140:143], v[92:95], v[104:107]\n"
"ds_read_b128 v[164:167], v182 offset:16384\n"
"v_mfma_f32_16x16x32_bf16 v[120:123], v[140:143], a[196:199], v[120:123]\n"
"ds_read_b128 v[168:171], v182 offset:34816\n"
"v_mfma_f32_16x16x32_bf16 v[108:111], v[144:147], v[92:95], v[108:111]\n"
"ds_read_b128 v[172:175], v11 offset:16384\n"
"v_mfma_f32_16x16x32_bf16 v[124:127], v[144:147], a[196:199], v[124:127]\n"
"buffer_load_dwordx4 v7, s[28:31], s15 offen offset:896 lds\n"
"v_mfma_f32_16x16x32_bf16 v[136:139], v[244:247], v[148:151], v[136:139]\n"
"s_waitcnt vmcnt(12)\n"
"ds_read_b128 a[72:75], v8 offset:6144\n"
"s_waitcnt lgkmcnt(8)\n"
"v_mfma_f32_16x16x32_bf16 v[104:107], v[152:155], v[96:99], v[104:107]\n"
"ds_read_b128 v[140:143], v181\n"
"v_mfma_f32_16x16x32_bf16 v[120:123], v[152:155], a[200:203], v[120:123]\n"
"ds_read_b128 v[144:147], v181 offset:18432\n"
"v_mfma_f32_16x16x32_bf16 v[108:111], v[156:159], v[96:99], v[108:111]\n"
"ds_read_b128 a[76:79], v9 offset:6144\n"
"v_mfma_f32_16x16x32_bf16 v[124:127], v[156:159], a[200:203], v[124:127]\n"
"s_add_u32 m0, s20, 0x1000\n"
"s_waitcnt lgkmcnt(9)\n"
"buffer_load_dwordx4 v7, s[28:31], s16 offen lds\n"
"s_add_u32 m0, s20, 0x1400\n"
"v_mfma_f32_16x16x32_bf16 v[136:139], v[248:251], v[160:163], v[136:139]\n"
"buffer_load_dwordx4 v7, s[28:31], s17 offen lds\n"
"s_waitcnt lgkmcnt(4)\n"
"v_mfma_f32_16x16x32_bf16 v[104:107], v[164:167], v[100:103], v[104:107]\n"
"ds_read_b128 v[152:155], v183\n"
"v_mul_f32 v224, v204, v16\n"
"v_mfma_f32_16x16x32_bf16 v[120:123], v[164:167], a[204:207], v[120:123]\n"
"ds_read_b128 v[156:159], v183 offset:18432\n"
"v_mul_f32 v204, v204, v17\n"
"v_mfma_f32_16x16x32_bf16 v[108:111], v[168:171], v[100:103], v[108:111]\n"
"s_waitcnt vmcnt(12)\n"
"ds_read_b128 a[80:83], v8 offset:8192\n"
"ds_read_b128 a[84:87], v9 offset:8192\n"
"v_mul_f32 v225, v205, v16\n"
"v_mfma_f32_16x16x32_bf16 v[124:127], v[168:171], a[204:207], v[124:127]\n"
"s_add_u32 m0, s20, 0x1780\n"
"s_waitcnt lgkmcnt(4)\n"
"buffer_load_dwordx4 v7, s[28:31], s16 offen offset:128 lds\n"
"s_add_u32 m0, s20, 0x1b80\n"
"v_mul_f32 v205, v205, v17\n"
"v_mfma_f32_16x16x32_bf16 v[136:139], v[252:255], v[172:175], v[136:139]\n"
"buffer_load_dwordx4 v7, s[28:31], s17 offen offset:128 lds\n"
"v_mul_f32 v226, v206, v16\n"
"v_mfma_f32_16x16x32_bf16 v[112:115], v[140:143], v[32:35], 0\n"
"ds_read_b128 v[164:167], v181 offset:2048\n"
"v_mul_f32 v206, v206, v17\n"
"v_mfma_f32_16x16x32_bf16 v[128:131], v[140:143], a[136:139], 0\n"
"ds_read_b128 v[168:171], v181 offset:20480\n"
"v_mul_f32 v227, v207, v16\n"
"v_mfma_f32_16x16x32_bf16 v[116:119], v[144:147], v[32:35], 0\n"
"s_waitcnt vmcnt(12)\n"
"ds_read_b128 a[88:91], v8 offset:10240\n"
"v_mul_f32 v207, v207, v17\n"
"v_mfma_f32_16x16x32_bf16 v[132:135], v[144:147], a[136:139], 0\n"
"ds_read_b128 a[92:95], v9 offset:10240\n"
"v_mul_f32 v228, v208, v16\n"
"s_waitcnt lgkmcnt(6)\n"
"v_mfma_f32_16x16x32_bf16 v[112:115], v[152:155], v[36:39], v[112:115]\n"
"ds_read_b128 v[140:143], v183 offset:2048\n"
"v_mul_f32 v208, v208, v17\n"
"v_mfma_f32_16x16x32_bf16 v[128:131], v[152:155], a[140:143], v[128:131]\n"
"ds_read_b128 v[144:147], v183 offset:20480\n"
"v_mul_f32 v229, v209, v16\n"
"v_mfma_f32_16x16x32_bf16 v[116:119], v[156:159], v[36:39], v[116:119]\n"
"s_add_u32 m0, s20, 0x1f00\n"
"s_waitcnt lgkmcnt(6)\n"
"buffer_load_dwordx4 v7, s[28:31], s16 offen offset:256 lds\n"
"s_add_u32 m0, s20, 0x2300\n"
"v_mul_f32 v209, v209, v17\n"
"v_mfma_f32_16x16x32_bf16 v[132:135], v[156:159], a[140:143], v[132:135]\n"
"buffer_load_dwordx4 v7, s[28:31], s17 offen offset:256 lds\n"
"v_mul_f32 v230, v210, v16\n"
"s_waitcnt lgkmcnt(4)\n"
"v_mfma_f32_16x16x32_bf16 v[112:115], v[164:167], v[40:43], v[112:115]\n"
"ds_read_b128 v[152:155], v181 offset:4096\n"
"v_mul_f32 v210, v210, v17\n"
"v_mfma_f32_16x16x32_bf16 v[128:131], v[164:167], a[144:147], v[128:131]\n"
"ds_read_b128 v[156:159], v181 offset:22528\n"
"v_mul_f32 v231, v211, v16\n"
"v_mfma_f32_16x16x32_bf16 v[116:119], v[168:171], v[40:43], v[116:119]\n"
"s_waitcnt vmcnt(12)\n"
"ds_read_b128 a[96:99], v8 offset:12288\n"
"v_mul_f32 v211, v211, v17\n"
"v_mfma_f32_16x16x32_bf16 v[132:135], v[168:171], a[144:147], v[132:135]\n"
"v_fma_f32 v104, v104, v184, v224\n"
"s_waitcnt lgkmcnt(3)\n"
"v_mfma_f32_16x16x32_bf16 v[112:115], v[140:143], v[44:47], v[112:115]\n"
"ds_read_b128 v[164:167], v183 offset:4096\n"
"v_fma_f32 v105, v105, v185, v225\n"
"v_mfma_f32_16x16x32_bf16 v[128:131], v[140:143], a[148:151], v[128:131]\n"
"ds_read_b128 v[168:171], v183 offset:22528\n"
"v_fma_f32 v106, v106, v186, v226\n"
"v_mfma_f32_16x16x32_bf16 v[116:119], v[144:147], v[44:47], v[116:119]\n"
"v_fma_f32 v107, v107, v187, v227\n"
"v_mfma_f32_16x16x32_bf16 v[132:135], v[144:147], a[148:151], v[132:135]\n"
"ds_read_b128 a[100:103], v9 offset:12288\n"
"v_cvt_pk_bf16_f32 v240, v104, v105\n"
"s_waitcnt lgkmcnt(4)\n"
"v_mfma_f32_16x16x32_bf16 v[112:115], v[152:155], v[48:51], v[112:115]\n"
"ds_read_b128 v[140:143], v181 offset:6144\n"
"v_cvt_pk_bf16_f32 v241, v106, v107\n"
"v_mfma_f32_16x16x32_bf16 v[128:131], v[152:155], a[152:155], v[128:131]\n"
"ds_read_b128 v[144:147], v181 offset:24576\n"
"v_fma_f32 v108, v108, v188, v228\n"
"v_mfma_f32_16x16x32_bf16 v[116:119], v[156:159], v[48:51], v[116:119]\n"
"v_fma_f32 v109, v109, v189, v229\n"
"v_mfma_f32_16x16x32_bf16 v[132:135], v[156:159], a[152:155], v[132:135]\n"
"v_fma_f32 v110, v110, v190, v230\n"
"s_waitcnt lgkmcnt(3)\n"
"v_mfma_f32_16x16x32_bf16 v[112:115], v[164:167], v[52:55], v[112:115]\n"
"ds_read_b128 v[152:155], v183 offset:6144\n"
"v_fma_f32 v111, v111, v191, v231\n"
"v_mfma_f32_16x16x32_bf16 v[128:131], v[164:167], a[156:159], v[128:131]\n"
"ds_read_b128 v[156:159], v183 offset:24576\n"
"v_cvt_pk_bf16_f32 v242, v108, v109\n"
"v_mfma_f32_16x16x32_bf16 v[116:119], v[168:171], v[52:55], v[116:119]\n"
"v_cvt_pk_bf16_f32 v243, v110, v111\n"
"v_mfma_f32_16x16x32_bf16 v[132:135], v[168:171], a[156:159], v[132:135]\n"
"s_add_u32 m0, s20, 0x2680\n"
"s_nop 0\n"
"buffer_load_dwordx4 v7, s[28:31], s16 offen offset:384 lds\n"
"s_add_u32 m0, s20, 0x2a80\n"
"buffer_store_dwordx4 v[240:243], v1, s[24:27], s65 offen sc1\n"
"s_waitcnt lgkmcnt(2)\n"
"v_mfma_f32_16x16x32_bf16 v[112:115], v[140:143], v[56:59], v[112:115]\n"
"ds_read_b128 v[164:167], v181 offset:8192\n"
"v_fma_f32 v120, v120, v184, v204\n"
"v_mfma_f32_16x16x32_bf16 v[128:131], v[140:143], a[160:163], v[128:131]\n"
"ds_read_b128 v[168:171], v181 offset:26624\n"
"v_fma_f32 v121, v121, v185, v205\n"
"v_mfma_f32_16x16x32_bf16 v[116:119], v[144:147], v[56:59], v[116:119]\n"
"v_fma_f32 v122, v122, v186, v206\n"
"v_mfma_f32_16x16x32_bf16 v[132:135], v[144:147], a[160:163], v[132:135]\n"
"v_fma_f32 v123, v123, v187, v207\n"
"s_waitcnt lgkmcnt(2)\n"
"v_mfma_f32_16x16x32_bf16 v[112:115], v[152:155], v[60:63], v[112:115]\n"
"ds_read_b128 v[140:143], v183 offset:8192\n"
"v_cvt_pk_bf16_f32 v244, v120, v121\n"
"v_mfma_f32_16x16x32_bf16 v[128:131], v[152:155], a[164:167], v[128:131]\n"
"ds_read_b128 v[144:147], v183 offset:26624\n"
"v_cvt_pk_bf16_f32 v245, v122, v123\n"
"v_mfma_f32_16x16x32_bf16 v[116:119], v[156:159], v[60:63], v[116:119]\n"
"v_fma_f32 v124, v124, v188, v208\n"
"v_mfma_f32_16x16x32_bf16 v[132:135], v[156:159], a[164:167], v[132:135]\n"
"buffer_load_dwordx4 v7, s[28:31], s17 offen offset:384 lds\n"
"v_fma_f32 v125, v125, v189, v209\n"
"s_waitcnt lgkmcnt(2)\n"
"v_mfma_f32_16x16x32_bf16 v[112:115], v[164:167], v[64:67], v[112:115]\n"
"ds_read_b128 v[152:155], v181 offset:10240\n"
"v_fma_f32 v126, v126, v190, v210\n"
"v_mfma_f32_16x16x32_bf16 v[128:131], v[164:167], a[168:171], v[128:131]\n"
"ds_read_b128 v[156:159], v181 offset:28672\n"
"v_fma_f32 v127, v127, v191, v211\n"
"v_mfma_f32_16x16x32_bf16 v[116:119], v[168:171], v[64:67], v[116:119]\n"
"v_cvt_pk_bf16_f32 v246, v124, v125\n"
"v_mfma_f32_16x16x32_bf16 v[132:135], v[168:171], a[168:171], v[132:135]\n"
"v_cvt_pk_bf16_f32 v247, v126, v127\n"
"s_waitcnt lgkmcnt(2)\n"
"v_mfma_f32_16x16x32_bf16 v[112:115], v[140:143], v[68:71], v[112:115]\n"
"ds_read_b128 v[164:167], v183 offset:10240\n"
"buffer_store_dwordx4 v[244:247], v1, s[24:27], s66 offen sc1\n"
"v_mfma_f32_16x16x32_bf16 v[128:131], v[140:143], a[172:175], v[128:131]\n"
"ds_read_b128 v[168:171], v183 offset:28672\n"
"v_mul_f32 v220, v220, v30\n"
"v_mfma_f32_16x16x32_bf16 v[116:119], v[144:147], v[68:71], v[116:119]\n"
"v_mul_f32 v221, v221, v30\n"
"v_mfma_f32_16x16x32_bf16 v[132:135], v[144:147], a[172:175], v[132:135]\n"
"s_waitcnt vmcnt(14)\n"
"ds_read_b128 a[104:107], v8 offset:14336\n"
"v_mul_f32 v222, v222, v30\n"
"s_waitcnt lgkmcnt(3)\n"
"v_mfma_f32_16x16x32_bf16 v[112:115], v[152:155], v[72:75], v[112:115]\n"
"ds_read_b128 v[140:143], v181 offset:12288\n"
"v_mul_f32 v223, v223, v30\n"
"v_mfma_f32_16x16x32_bf16 v[128:131], v[152:155], a[176:179], v[128:131]\n"
"ds_read_b128 v[144:147], v181 offset:30720\n"
"v_fma_f32 v136, v136, v200, v220\n"
"v_mfma_f32_16x16x32_bf16 v[116:119], v[156:159], v[72:75], v[116:119]\n"
"v_fma_f32 v137, v137, v201, v221\n"
"v_mfma_f32_16x16x32_bf16 v[132:135], v[156:159], a[176:179], v[132:135]\n"
"v_fma_f32 v138, v138, v202, v222\n"
"s_waitcnt lgkmcnt(3)\n"
"v_mfma_f32_16x16x32_bf16 v[112:115], v[164:167], v[76:79], v[112:115]\n"
"ds_read_b128 v[152:155], v183 offset:12288\n"
"v_fma_f32 v139, v139, v203, v223\n"
"v_mfma_f32_16x16x32_bf16 v[128:131], v[164:167], a[180:183], v[128:131]\n"
"ds_read_b128 v[156:159], v183 offset:30720\n"
"v_cvt_pk_bf16_f32 v176, v136, v137\n"
"v_mfma_f32_16x16x32_bf16 v[116:119], v[168:171], v[76:79], v[116:119]\n"
"ds_read_b128 a[108:111], v9 offset:14336\n"
"v_cvt_pk_bf16_f32 v177, v138, v139\n"
"v_mfma_f32_16x16x32_bf16 v[132:135], v[168:171], a[180:183], v[132:135]\n"
"s_lshr_b32 s62, s56, 1\n"
"s_lshl_b32 s62, s62, 7\n"
"s_and_b32 s63, s56, 1\n"
"s_add_u32 s62, s62, s63\n"
"s_lshl3_add_u32 s62, s62, 0x26800\n"
"v_add_u32 v18, s62, v1\n"
"ds_write_b64 v18, v[176:177]\n"
"s_waitcnt lgkmcnt(4)\n"
"v_mfma_f32_16x16x32_bf16 v[112:115], v[140:143], v[80:83], v[112:115]\n"
"ds_read_b128 v[164:167], v181 offset:14336\n"
"v_mul_f32 v232, v212, v16\n"
"v_mfma_f32_16x16x32_bf16 v[128:131], v[140:143], a[184:187], v[128:131]\n"
"ds_read_b128 v[168:171], v181 offset:32768\n"
"v_mul_f32 v212, v212, v17\n"
"v_mfma_f32_16x16x32_bf16 v[116:119], v[144:147], v[80:83], v[116:119]\n"
"v_mul_f32 v233, v213, v16\n"
"v_mfma_f32_16x16x32_bf16 v[132:135], v[144:147], a[184:187], v[132:135]\n"
"v_mul_f32 v213, v213, v17\n"
"s_waitcnt lgkmcnt(4)\n"
"v_mfma_f32_16x16x32_bf16 v[112:115], v[152:155], v[84:87], v[112:115]\n"
"ds_read_b128 v[140:143], v183 offset:14336\n"
"v_mul_f32 v234, v214, v16\n"
"v_mfma_f32_16x16x32_bf16 v[128:131], v[152:155], a[188:191], v[128:131]\n"
"ds_read_b128 v[144:147], v183 offset:32768\n"
"v_mul_f32 v214, v214, v17\n"
"v_mfma_f32_16x16x32_bf16 v[116:119], v[156:159], v[84:87], v[116:119]\n"
"s_add_u32 m0, s20, 0x2e00\n"
"s_nop 0\n"
"buffer_load_dwordx4 v7, s[28:31], s16 offen offset:512 lds\n"
"s_add_u32 m0, s20, 0x3200\n"
"v_mul_f32 v235, v215, v16\n"
"v_mfma_f32_16x16x32_bf16 v[132:135], v[156:159], a[188:191], v[132:135]\n"
"s_lshl_b32 s62, s56, 9\n"
"s_add_u32 s62, s62, 0x26800\n"
"v_add_u32 v18, s62, v1\n"
"s_waitcnt lgkmcnt(4)\n"
"s_barrier\n"
"ds_read_b128 v[176:179], v18\n"
"v_mul_f32 v215, v215, v17\n"
"s_waitcnt lgkmcnt(3)\n"
"v_mfma_f32_16x16x32_bf16 v[112:115], v[164:167], v[88:91], v[112:115]\n"
"ds_read_b128 v[152:155], v181 offset:16384\n"
"v_mul_f32 v236, v216, v16\n"
"v_mfma_f32_16x16x32_bf16 v[128:131], v[164:167], a[192:195], v[128:131]\n"
"ds_read_b128 v[156:159], v181 offset:34816\n"
"v_mul_f32 v216, v216, v17\n"
"v_mfma_f32_16x16x32_bf16 v[116:119], v[168:171], v[88:91], v[116:119]\n"
"v_mul_f32 v237, v217, v16\n"
"v_mfma_f32_16x16x32_bf16 v[132:135], v[168:171], a[192:195], v[132:135]\n"
"v_mul_f32 v217, v217, v17\n"
"s_waitcnt lgkmcnt(3)\n"
"v_mfma_f32_16x16x32_bf16 v[112:115], v[140:143], v[92:95], v[112:115]\n"
"ds_read_b128 v[164:167], v183 offset:16384\n"
"v_mul_f32 v238, v218, v16\n"
"v_mfma_f32_16x16x32_bf16 v[128:131], v[140:143], a[196:199], v[128:131]\n"
"ds_read_b128 v[168:171], v183 offset:34816\n"
"v_mul_f32 v218, v218, v17\n"
"v_mfma_f32_16x16x32_bf16 v[116:119], v[144:147], v[92:95], v[116:119]\n"
"buffer_load_dwordx4 v7, s[28:31], s17 offen offset:512 lds\n"
"v_mul_f32 v239, v219, v16\n"
"v_mfma_f32_16x16x32_bf16 v[132:135], v[144:147], a[196:199], v[132:135]\n"
"s_waitcnt lgkmcnt(4)\n"
"s_mov_b32 exec_hi, 0\n"
"buffer_store_dwordx4 v[176:179], v1, s[24:27], s64 offen sc1\n"
"s_mov_b32 exec_hi, -1\n"
"v_mul_f32 v219, v219, v17\n"
"s_waitcnt lgkmcnt(2)\n"
"v_mfma_f32_16x16x32_bf16 v[112:115], v[152:155], v[96:99], v[112:115]\n"
"v_mfma_f32_16x16x32_bf16 v[128:131], v[152:155], a[200:203], v[128:131]\n"
"v_mfma_f32_16x16x32_bf16 v[116:119], v[156:159], v[96:99], v[116:119]\n"
"v_mfma_f32_16x16x32_bf16 v[132:135], v[156:159], a[200:203], v[132:135]\n"
"s_waitcnt lgkmcnt(0)\n"
"v_mfma_f32_16x16x32_bf16 v[112:115], v[164:167], v[100:103], v[112:115]\n"
"v_mfma_f32_16x16x32_bf16 v[128:131], v[164:167], a[204:207], v[128:131]\n"
"v_mfma_f32_16x16x32_bf16 v[116:119], v[168:171], v[100:103], v[116:119]\n"
"v_mfma_f32_16x16x32_bf16 v[132:135], v[168:171], a[204:207], v[132:135]\n"
"s_nop 4\n"
"v_fma_f32 v112, v112, v192, v232\n"
"v_fma_f32 v113, v113, v193, v233\n"
"v_fma_f32 v114, v114, v194, v234\n"
"v_fma_f32 v115, v115, v195, v235\n"
"v_cvt_pk_bf16_f32 v248, v112, v113\n"
"v_cvt_pk_bf16_f32 v249, v114, v115\n"
"v_fma_f32 v116, v116, v196, v236\n"
"v_fma_f32 v117, v117, v197, v237\n"
"v_fma_f32 v118, v118, v198, v238\n"
"v_fma_f32 v119, v119, v199, v239\n"
"v_cvt_pk_bf16_f32 v250, v116, v117\n"
"v_cvt_pk_bf16_f32 v251, v118, v119\n"
"buffer_store_dwordx4 v[248:251], v1, s[24:27], s65 offen offset:1024 sc1\n"
"v_fma_f32 v128, v128, v192, v212\n"
"v_fma_f32 v129, v129, v193, v213\n"
"v_fma_f32 v130, v130, v194, v214\n"
"v_fma_f32 v131, v131, v195, v215\n"
"v_cvt_pk_bf16_f32 v252, v128, v129\n"
"v_cvt_pk_bf16_f32 v253, v130, v131\n"
"v_fma_f32 v132, v132, v196, v216\n"
"v_fma_f32 v133, v133, v197, v217\n"
"v_fma_f32 v134, v134, v198, v218\n"
"v_fma_f32 v135, v135, v199, v219\n"
"v_cvt_pk_bf16_f32 v254, v132, v133\n"
"v_cvt_pk_bf16_f32 v255, v134, v135\n"
"buffer_store_dwordx4 v[252:255], v1, s[24:27], s66 offen offset:1024 sc1\n"
"s_waitcnt vmcnt(17)\n"
"ds_read_b128 a[112:115], v8\n"
"ds_read_b128 a[116:119], v9\n"
"s_add_u32 m0, s20, 0x3580\n"
"s_nop 0\n"
"buffer_load_dwordx4 v7, s[28:31], s16 offen offset:640 lds\n"
"s_add_u32 m0, s20, 0x3980\n"
"s_nop 0\n"
"buffer_load_dwordx4 v7, s[28:31], s17 offen offset:640 lds\n"
"s_waitcnt vmcnt(17)\n"
"ds_read_b128 a[120:123], v8 offset:2048\n"
"ds_read_b128 a[124:127], v9 offset:2048\n"
"s_add_u32 m0, s20, 0xfffffd00\n"
"s_waitcnt lgkmcnt(2)\n"
"buffer_load_dwordx4 v7, s[28:31], s16 offen offset:768 lds\n"
"s_add_u32 m0, s20, 0x100\n"
"s_nop 0\n"
"buffer_load_dwordx4 v7, s[28:31], s17 offen offset:768 lds\n"
"s_lshr_b32 s85, s58, 2\n"
"s_lshl_b32 s63, s80, 14\n"
"s_add_u32 s63, s63, s77\n"
"s_mul_i32 s62, s68, 0x10000\n"
"s_add_u32 s74, s62, s63\n"
"s_lshl1_add_u32 s62, s85, s67\n"
"s_mul_i32 s62, s62, 0x10000\n"
"s_add_u32 s84, s62, s63\n"
"s_xor_b32 s64, s85, 1\n"
"s_lshl1_add_u32 s64, s64, s67\n"
"s_mul_i32 s62, s64, 0x10000\n"
"s_add_u32 s65, s62, s63\n"
"s_mul_i32 s81, s59, 0x480000\n"
"s_mul_i32 s62, s57, 0x24000\n"
"s_add_u32 s81, s81, s62\n"
"s_add_u32 s81, s81, 0xd00000\n"
"s_mul_i32 s61, s56, 0x4800\n"
"s_add_u32 s81, s81, s61\n"
"s_lshl_b32 s62, s64, 9\n"
"s_add_u32 s62, s62, s81\n"
"s_add_u32 s72, s62, 0x12000\n"
"s_lshl1_add_u32 s64, s85, s67\n"
"s_lshl_b32 s62, s64, 9\n"
"s_add_u32 s73, s62, s81\n"
"s_lshr_b32 s62, s64, 2\n"
"s_lshl_b32 s62, s62, 11\n"
"s_add_u32 s63, s62, s61\n"
"s_and_b32 s62, s64, 2\n"
"s_cselect_b64 vcc, -1, 0\n"
"v_cndmask_b32 v12, v180, v182, vcc\n"
"v_add_u32 v12, s63, v12\n"
"s_lshr_b32 s62, s68, 2\n"
"s_lshl_b32 s62, s62, 11\n"
"s_add_u32 s63, s62, s61\n"
"s_and_b32 s62, s68, 2\n"
"s_cselect_b64 vcc, -1, 0\n"
"v_cndmask_b32 v11, v180, v182, vcc\n"
"v_add_u32 v11, s63, v11\n"
"s_mul_i32 s66, s85, 0x12000\n"
"s_add_u32 s66, s66, s81\n"
"s_lshr_b32 s62, s68, 1\n"
"s_lshl_b32 s62, s62, 10\n"
"s_add_u32 s66, s66, s62\n"
"s_and_b32 s62, s68, 1\n"
"s_cselect_b64 vcc, -1, 0\n"
"s_lshl_b32 s62, s62, 9\n"
"s_add_u32 s66, s62, s66\n"
"v_cndmask_b32 v166, v22, v20, vcc\n"
"v_and_b32 v166, s71, v166\n"
"v_cndmask_b32 v167, v23, v21, vcc\n"
"v_and_b32 v167, s71, v167\n"
"v_cndmask_b32 v168, v24, v22, vcc\n"
"v_and_b32 v168, s71, v168\n"
"v_cndmask_b32 v169, v25, v23, vcc\n"
"v_and_b32 v169, s71, v169\n"
"s_xor_b32 s62, s56, 1\n"
"s_mul_i32 s62, s62, 0xc00\n"
"v_add_u32 v170, s62, v3\n"
"s_xor_b32 s62, s56, 2\n"
"s_mul_i32 s62, s62, 0xc00\n"
"v_add_u32 v171, s62, v3\n"
"s_xor_b32 s62, s56, 3\n"
"s_mul_i32 s62, s62, 0xc00\n"
"v_add_u32 v172, s62, v3\n"
"s_mul_i32 s62, s56, 0xc00\n"
"v_add_u32 v10, s62, v3\n"
"s_mul_i32 s76, s58, 0x144000\n"
"s_add_u32 s76, s76, s62\n"
"s_add_u32 s76, s76, 0x1840000\n"
"s_lshl_b32 s62, s59, 6\n"
"s_add_u32 s21, s22, s62\n"
"s_lshl_b32 s62, s58, 11\n"
"s_add_u32 s75, s21, s62\n"
"s_waitcnt vmcnt(4)\n"
"s_barrier\n"
"s_cmp_lg_u32 s56, 0\n"
"s_cbranch_scc1 Lapub%=\n"
"s_cmp_lg_u32 s59, 0\n"
"s_cbranch_scc1 La0%=\n"
"s_add_u32 s62, s58, 32\n"
"v_mov_b32 v18, s62\n"
"v_mov_b32 v19, s57\n"
"v_cmp_gt_u32 vcc, 32, v0\n"
"s_nop 1\n"
"v_cndmask_b32 v18, v18, v19, vcc\n"
"v_lshl_add_u32 v13, v18, 3, v26\n"
"s_bfm_b64 exec, 40, 0\n"
"buffer_store_dwordx2 v[14:15], v13, s[24:27], 0 offen sc1\n"
"s_mov_b64 exec, -1\n"
"La0%=:\n"
"s_cmp_lg_u32 s59, 1\n"
"s_cbranch_scc1 La1%=\n"
"s_bfm_b64 exec, 32, 0\n"
"buffer_atomic_add v27, v26, s[24:27], 0 offen offset:448 sc1\n"
"s_mov_b64 exec, -1\n"
"La1%=:\n"
"Lapub%=:\n"
"ds_read_b128 a[128:131], v8 offset:4096\n"
"ds_read_b128 a[132:135], v9 offset:4096\n"
"s_add_u32 m0, s20, 0x480\n"
"s_waitcnt lgkmcnt(2)\n"
"buffer_load_dwordx4 v7, s[28:31], s16 offen offset:896 lds\n"
"s_add_u32 m0, s20, 0x880\n"
"s_nop 0\n"
"buffer_load_dwordx4 v7, s[28:31], s17 offen offset:896 lds\n"
"ds_read_b128 a[136:139], v8 offset:6144\n"
"ds_read_b128 a[140:143], v9 offset:6144\n"
"s_add_u32 m0, s20, 0x1000\n"
"s_waitcnt lgkmcnt(2)\n"
"buffer_load_dwordx4 v7, s[28:31], s18 offen lds\n"
"s_add_u32 m0, s20, 0x1400\n"
"s_nop 0\n"
"buffer_load_dwordx4 v7, s[28:31], s19 offen lds\n"
"ds_read_b128 a[144:147], v8 offset:8192\n"
"ds_read_b128 a[148:151], v9 offset:8192\n"
"s_add_u32 m0, s20, 0x1780\n"
"s_waitcnt lgkmcnt(2)\n"
"buffer_load_dwordx4 v7, s[28:31], s18 offen offset:128 lds\n"
"s_add_u32 m0, s20, 0x1b80\n"
"s_nop 0\n"
"buffer_load_dwordx4 v7, s[28:31], s19 offen offset:128 lds\n"
"ds_read_b128 a[152:155], v8 offset:10240\n"
"ds_read_b128 a[156:159], v9 offset:10240\n"
"s_add_u32 m0, s20, 0x1f00\n"
"s_waitcnt lgkmcnt(2)\n"
"buffer_load_dwordx4 v7, s[28:31], s18 offen offset:256 lds\n"
"s_add_u32 m0, s20, 0x2300\n"
"s_nop 0\n"
"buffer_load_dwordx4 v7, s[28:31], s19 offen offset:256 lds\n"
"s_cmp_lg_u32 s59, 1\n"
"s_cbranch_scc1 Lpk1%=\n"
"s_cmp_lg_u32 s56, 0\n"
"s_cbranch_scc1 Lpk1w%=\n"
"s_mov_b64 exec, 1\n"
"v_mov_b32 v16, 0\n"
"buffer_atomic_add v16, off, s[24:27], s69 offset:448 sc0 sc1\n"
"s_mov_b64 exec, -1\n"
"Lpk1w%=:\n"
"Lpk1%=:\n"
"ds_read_b128 a[160:163], v8 offset:12288\n"
"ds_read_b128 a[164:167], v9 offset:12288\n"
"s_add_u32 m0, s20, 0x2680\n"
"s_waitcnt lgkmcnt(2)\n"
"buffer_load_dwordx4 v7, s[28:31], s18 offen offset:384 lds\n"
"s_add_u32 m0, s20, 0x2a80\n"
"s_nop 0\n"
"buffer_load_dwordx4 v7, s[28:31], s19 offen offset:384 lds\n"
"s_waitcnt vmcnt(12)\n"
"ds_read_b128 a[168:171], v8 offset:14336\n"
"ds_read_b128 a[172:175], v9 offset:14336\n"
"s_add_u32 m0, s20, 0x2e00\n"
"s_waitcnt lgkmcnt(2)\n"
"buffer_load_dwordx4 v7, s[28:31], s18 offen offset:512 lds\n"
"s_add_u32 m0, s20, 0x3200\n"
"s_nop 0\n"
"buffer_load_dwordx4 v7, s[28:31], s19 offen offset:512 lds\n"
"s_waitcnt vmcnt(12)\n"
"ds_read_b128 a[176:179], v8\n"
"ds_read_b128 a[180:183], v9\n"
"s_add_u32 m0, s20, 0x3580\n"
"s_waitcnt lgkmcnt(2)\n"
"buffer_load_dwordx4 v7, s[28:31], s18 offen offset:640 lds\n"
"s_add_u32 m0, s20, 0x3980\n"
"s_nop 0\n"
"buffer_load_dwordx4 v7, s[28:31], s19 offen offset:640 lds\n"
"s_waitcnt vmcnt(12)\n"
"ds_read_b128 a[184:187], v8 offset:2048\n"
"ds_read_b128 a[188:191], v9 offset:2048\n"
"s_add_u32 m0, s20, 0xfffffd00\n"
"s_waitcnt lgkmcnt(2)\n"
"buffer_load_dwordx4 v7, s[28:31], s18 offen offset:768 lds\n"
"s_add_u32 m0, s20, 0x100\n"
"s_nop 0\n"
"buffer_load_dwordx4 v7, s[28:31], s19 offen offset:768 lds\n"
"s_waitcnt vmcnt(12)\n"
"ds_read_b128 a[192:195], v8 offset:4096\n"
"ds_read_b128 a[196:199], v9 offset:4096\n"
"s_add_u32 m0, s20, 0x480\n"
"s_waitcnt lgkmcnt(2)\n"
"buffer_load_dwordx4 v7, s[28:31], s18 offen offset:896 lds\n"
"s_add_u32 m0, s20, 0x880\n"
"s_nop 0\n"
"buffer_load_dwordx4 v7, s[28:31], s19 offen offset:896 lds\n"
"s_cmp_lg_u32 s56, 0\n"
"s_cbranch_scc1 Lapoll%=\n"
"s_cmp_lg_u32 s59, 0\n"
"s_cbranch_scc1 Lb0%=\n"
"v_lshlrev_b32 v18, 3, v0\n"
"s_bfm_b64 exec, 40, 0\n"
"Lap%=:\n"
"v_mov_b32 v16, 0\n"
"v_mov_b32 v17, 0\n"
"buffer_atomic_or_x2 v[16:17], v18, s[24:27], s69 offen sc0 sc1\n"
"s_waitcnt vmcnt(0)\n"
"v_cmp_eq_u32 s[78:79], v16, v15\n"
"v_cmp_ne_u32 vcc, v17, v14\n"
"s_nop 0\n"
"s_and_b64 vcc, vcc, s[78:79]\n"
"s_cbranch_scc0 Lap_r%=\n"
"s_mov_b64 exec, vcc\n"
"v_mov_b32 v14, v17\n"
"buffer_store_dwordx2 v[14:15], v13, s[24:27], 0 offen sc1\n"
"s_bfm_b64 exec, 40, 0\n"
"Lap_r%=:\n"
"s_andn2_b64 vcc, exec, s[78:79]\n"
"s_cbranch_vccz Lap_d%=\n"
"s_sleep 2\n"
"s_sub_u32 s60, s60, 1\n"
"s_cbranch_scc0 Lap%=\n"
"s_mov_b32 s60, 0\n"
"Lap_d%=:\n"
"s_mov_b64 exec, -1\n"
"Lb0%=:\n"
"s_cmp_lg_u32 s59, 1\n"
"s_cbranch_scc1 Lb1%=\n"
"s_waitcnt vmcnt(10)\n"
"v_readfirstlane_b32 vcc_lo, v16\n"
"s_nop 0\n"
"s_cmp_ge_u32 vcc_lo, 32\n"
"s_cbranch_scc1 Lap1_d%=\n"
"Lap1%=:\n"
"s_mov_b64 exec, 1\n"
"v_mov_b32 v16, 0\n"
"buffer_atomic_add v16, off, s[24:27], s69 offset:448 sc0 sc1\n"
"s_mov_b64 exec, -1\n"
"s_waitcnt vmcnt(0)\n"
"v_readfirstlane_b32 vcc_lo, v16\n"
"s_nop 0\n"
"s_cmp_ge_u32 vcc_lo, 32\n"
"s_cbranch_scc1 Lap1_d%=\n"
"s_sleep 2\n"
"s_sub_u32 s60, s60, 1\n"
"s_cbranch_scc0 Lap1%=\n"
"s_mov_b32 s60, 0\n"
"Lap1_d%=:\n"
"Lb1%=:\n"
"Lapoll%=:\n"
"s_cmp_lg_u32 s59, 0\n"
"s_cbranch_scc1 Lpt3%=\n"
"s_mul_i32 s96, s58, 0x144000\n"
"s_add_u32 s96, s96, 0x1840000\n"
"s_add_u32 s63, s91, 0\n"
"s_mul_i32 s62, s63, 0x5556\n"
"s_lshr_b32 s62, s62, 16\n"
"s_mul_i32 s62, s62, 9\n"
"s_add_u32 s62, s62, s63\n"
"s_lshl_b32 s62, s62, 10\n"
"s_add_u32 s62, s62, s96\n"
"s_add_u32 s63, s62, 0x1800\n"
"buffer_store_dwordx4 a[208:211], v1, s[24:27], s62 offen sc1\n"
"buffer_store_dwordx4 a[212:215], v1, s[24:27], s62 offen offset:3072 sc1\n"
"buffer_store_dwordx4 a[216:219], v1, s[24:27], s63 offen sc1\n"
"buffer_store_dwordx4 a[220:223], v1, s[24:27], s63 offen offset:3072 sc1\n"
"s_add_u32 s63, s91, 1\n"
"s_mul_i32 s62, s63, 0x5556\n"
"s_lshr_b32 s62, s62, 16\n"
"s_mul_i32 s62, s62, 9\n"
"s_add_u32 s62, s62, s63\n"
"s_lshl_b32 s62, s62, 10\n"
"s_add_u32 s62, s62, s96\n"
"s_add_u32 s63, s62, 0x1800\n"
"buffer_store_dwordx4 a[224:227], v1, s[24:27], s62 offen sc1\n"
"buffer_store_dwordx4 a[228:231], v1, s[24:27], s62 offen offset:3072 sc1\n"
"buffer_store_dwordx4 a[232:235], v1, s[24:27], s63 offen sc1\n"
"buffer_store_dwordx4 a[236:239], v1, s[24:27], s63 offen offset:3072 sc1\n"
"s_cmp_ge_u32 s91, 0xcc\n"
"s_cbranch_scc1 Lpt3b%=\n"
"s_add_u32 s63, s91, 2\n"
"s_mul_i32 s62, s63, 0x5556\n"
"s_lshr_b32 s62, s62, 16\n"
"s_mul_i32 s62, s62, 9\n"
"s_add_u32 s62, s62, s63\n"
"s_lshl_b32 s62, s62, 10\n"
"s_add_u32 s62, s62, s96\n"
"s_add_u32 s63, s62, 0x1800\n"
"buffer_store_dwordx4 a[240:243], v1, s[24:27], s62 offen sc1\n"
"buffer_store_dwordx4 a[244:247], v1, s[24:27], s62 offen offset:3072 sc1\n"
"buffer_store_dwordx4 a[248:251], v1, s[24:27], s63 offen sc1\n"
"buffer_store_dwordx4 a[252:255], v1, s[24:27], s63 offen offset:3072 sc1\n"
"Lpt3b%=:\n"
"Lpt3%=:\n"
"s_barrier\n"
"buffer_load_dwordx4 v[184:187], v1, s[24:27], s65 offen sc1\n"
"buffer_load_dwordx4 v[188:191], v1, s[24:27], s65 offen offset:1024 sc1\n"
"buffer_load_dwordx4 v[192:195], v1, s[24:27], s65 offen offset:2048 sc1\n"
"buffer_load_dwordx4 v[196:199], v1, s[24:27], s65 offen offset:3072 sc1\n"
"s_add_u32 s65, s65, 0x1000\n"
"s_waitcnt vmcnt(16)\n"
"ds_read_b128 a[200:203], v8 offset:6144\n"
"ds_read_b128 a[204:207], v9 offset:6144\n"
"s_waitcnt vmcnt(14)\n"
"ds_read_b128 a[208:211], v8 offset:8192\n"
"ds_read_b128 a[212:215], v9 offset:8192\n"
"s_waitcnt vmcnt(12)\n"
"ds_read_b128 a[216:219], v8 offset:10240\n"
"ds_read_b128 a[220:223], v9 offset:10240\n"
"s_waitcnt vmcnt(10)\n"
"ds_read_b128 a[224:227], v8 offset:12288\n"
"ds_read_b128 a[228:231], v9 offset:12288\n"
"buffer_load_dwordx4 v[200:203], v1, s[24:27], s65 offen sc1\n"
"buffer_load_dwordx4 v[204:207], v1, s[24:27], s65 offen offset:1024 sc1\n"
"buffer_load_dwordx4 v[208:211], v1, s[24:27], s65 offen offset:2048 sc1\n"
"buffer_load_dwordx4 v[212:215], v1, s[24:27], s65 offen offset:3072 sc1\n"
"s_add_u32 s65, s65, 0x1000\n"
"buffer_load_dwordx4 v[216:219], v1, s[24:27], s65 offen sc1\n"
"buffer_load_dwordx4 v[220:223], v1, s[24:27], s65 offen offset:1024 sc1\n"
"buffer_load_dwordx4 v[224:227], v1, s[24:27], s65 offen offset:2048 sc1\n"
"buffer_load_dwordx4 v[228:231], v1, s[24:27], s65 offen offset:3072 sc1\n"
"s_add_u32 s65, s65, 0x1000\n"
"buffer_load_dwordx4 v[232:235], v1, s[24:27], s65 offen sc1\n"
"buffer_load_dwordx4 v[236:239], v1, s[24:27], s65 offen offset:1024 sc1\n"
"buffer_load_dwordx4 v[240:243], v1, s[24:27], s65 offen offset:2048 sc1\n"
"buffer_load_dwordx4 v[244:247], v1, s[24:27], s65 offen offset:3072 sc1\n"
"s_add_u32 s65, s65, 0xd000\n"
"s_waitcnt vmcnt(20)\n"
"ds_read_b128 a[232:235], v8 offset:14336\n"
"ds_read_b128 a[236:239], v9 offset:14336\n"
"s_waitcnt vmcnt(18)\n"
"ds_read_b128 a[240:243], v8\n"
"ds_read_b128 a[244:247], v9\n"
"s_waitcnt vmcnt(16)\n"
"ds_read_b128 a[248:251], v8 offset:2048\n"
"ds_read_b128 a[252:255], v9 offset:2048\n"
"s_waitcnt lgkmcnt(0)\n"
"s_barrier\n"
"s_waitcnt vmcnt(14)\n"
"v_mfma_f32_16x16x32_bf16 v[108:111], v[184:187], a[64:67], 0\n"
"v_mfma_f32_16x16x32_bf16 v[108:111], v[188:191], a[68:71], v[108:111]\n"
"s_waitcnt vmcnt(12)\n"
"v_mfma_f32_16x16x32_bf16 v[108:111], v[192:195], a[72:75], v[108:111]\n"
"buffer_load_dwordx4 v[32:35], v1, s[24:27], s65 offen sc1\n"
"v_mfma_f32_16x16x32_bf16 v[108:111], v[196:199], a[76:79], v[108:111]\n"
"s_waitcnt vmcnt(11)\n"
"v_mfma_f32_16x16x32_bf16 v[108:111], v[200:203], a[80:83], v[108:111]\n"
"v_mfma_f32_16x16x32_bf16 v[108:111], v[204:207], a[84:87], v[108:111]\n"
"s_waitcnt vmcnt(9)\n"
"v_mfma_f32_16x16x32_bf16 v[108:111], v[208:211], a[88:91], v[108:111]\n"
"buffer_load_dwordx4 v[36:39], v1, s[24:27], s65 offen offset:1024 sc1\n"
"v_mfma_f32_16x16x32_bf16 v[108:111], v[212:215], a[92:95], v[108:111]\n"
"v_mfma_f32_16x16x32_bf16 v[112:115], v[184:187], a[128:131], 0\n"
"v_mfma_f32_16x16x32_bf16 v[112:115], v[188:191], a[132:135], v[112:115]\n"
"v_mfma_f32_16x16x32_bf16 v[112:115], v[192:195], a[136:139], v[112:115]\n"
"buffer_load_dwordx4 v[40:43], v1, s[24:27], s65 offen offset:2048 sc1\n"
"v_mfma_f32_16x16x32_bf16 v[112:115], v[196:199], a[140:143], v[112:115]\n"
"v_mfma_f32_16x16x32_bf16 v[112:115], v[200:203], a[144:147], v[112:115]\n"
"v_mfma_f32_16x16x32_bf16 v[112:115], v[204:207], a[148:151], v[112:115]\n"
"v_mfma_f32_16x16x32_bf16 v[112:115], v[208:211], a[152:155], v[112:115]\n"
"buffer_load_dwordx4 v[44:47], v1, s[24:27], s65 offen offset:3072 sc1\n"
"v_mfma_f32_16x16x32_bf16 v[112:115], v[212:215], a[156:159], v[112:115]\n"
"s_add_u32 s65, s65, 0x1000\n"
"v_mfma_f32_16x16x32_bf16 v[116:119], v[184:187], a[192:195], 0\n"
"v_mfma_f32_16x16x32_bf16 v[116:119], v[188:191], a[196:199], v[116:119]\n"
"v_mfma_f32_16x16x32_bf16 v[116:119], v[192:195], a[200:203], v[116:119]\n"
"buffer_load_dwordx4 v[48:51], v1, s[24:27], s65 offen sc1\n"
"v_mfma_f32_16x16x32_bf16 v[116:119], v[196:199], a[204:207], v[116:119]\n"
"v_mfma_f32_16x16x32_bf16 v[116:119], v[200:203], a[208:211], v[116:119]\n"
"v_mfma_f32_16x16x32_bf16 v[116:119], v[204:207], a[212:215], v[116:119]\n"
"v_mfma_f32_16x16x32_bf16 v[116:119], v[208:211], a[216:219], v[116:119]\n"
"buffer_load_dwordx4 v[52:55], v1, s[24:27], s65 offen offset:1024 sc1\n"
"v_mfma_f32_16x16x32_bf16 v[116:119], v[212:215], a[220:223], v[116:119]\n"
"v_mfma_f32_16x16x32_bf16 v[104:107], v[184:187], a[0:3], 0\n"
"v_mfma_f32_16x16x32_bf16 v[104:107], v[188:191], a[4:7], v[104:107]\n"
"v_mfma_f32_16x16x32_bf16 v[104:107], v[192:195], a[8:11], v[104:107]\n"
"buffer_load_dwordx4 v[56:59], v1, s[24:27], s65 offen offset:2048 sc1\n"
"v_mfma_f32_16x16x32_bf16 v[104:107], v[196:199], a[12:15], v[104:107]\n"
"v_mfma_f32_16x16x32_bf16 v[104:107], v[200:203], a[16:19], v[104:107]\n"
"v_mfma_f32_16x16x32_bf16 v[104:107], v[204:207], a[20:23], v[104:107]\n"
"v_mfma_f32_16x16x32_bf16 v[104:107], v[208:211], a[24:27], v[104:107]\n"
"buffer_load_dwordx4 v[60:63], v1, s[24:27], s65 offen offset:3072 sc1\n"
"v_mfma_f32_16x16x32_bf16 v[104:107], v[212:215], a[28:31], v[104:107]\n"
"s_add_u32 s65, s65, 0x1000\n"
"s_waitcnt vmcnt(14)\n"
"v_mfma_f32_16x16x32_bf16 v[108:111], v[216:219], a[96:99], v[108:111]\n"
"v_mfma_f32_16x16x32_bf16 v[108:111], v[220:223], a[100:103], v[108:111]\n"
"s_waitcnt vmcnt(12)\n"
"v_mfma_f32_16x16x32_bf16 v[108:111], v[224:227], a[104:107], v[108:111]\n"
"buffer_load_dwordx4 v[64:67], v1, s[24:27], s65 offen sc1\n"
"v_mfma_f32_16x16x32_bf16 v[108:111], v[228:231], a[108:111], v[108:111]\n"
"s_waitcnt vmcnt(11)\n"
"v_mfma_f32_16x16x32_bf16 v[108:111], v[232:235], a[112:115], v[108:111]\n"
"v_mfma_f32_16x16x32_bf16 v[108:111], v[236:239], a[116:119], v[108:111]\n"
"s_waitcnt vmcnt(9)\n"
"v_mfma_f32_16x16x32_bf16 v[108:111], v[240:243], a[120:123], v[108:111]\n"
"buffer_load_dwordx4 v[68:71], v1, s[24:27], s65 offen offset:1024 sc1\n"
"v_mfma_f32_16x16x32_bf16 v[108:111], v[244:247], a[124:127], v[108:111]\n"
"v_mfma_f32_16x16x32_bf16 v[112:115], v[216:219], a[160:163], v[112:115]\n"
"v_mfma_f32_16x16x32_bf16 v[112:115], v[220:223], a[164:167], v[112:115]\n"
"v_mfma_f32_16x16x32_bf16 v[112:115], v[224:227], a[168:171], v[112:115]\n"
"buffer_load_dwordx4 v[72:75], v1, s[24:27], s65 offen offset:2048 sc1\n"
"v_mfma_f32_16x16x32_bf16 v[112:115], v[228:231], a[172:175], v[112:115]\n"
"v_mfma_f32_16x16x32_bf16 v[112:115], v[232:235], a[176:179], v[112:115]\n"
"v_mfma_f32_16x16x32_bf16 v[112:115], v[236:239], a[180:183], v[112:115]\n"
"v_mfma_f32_16x16x32_bf16 v[112:115], v[240:243], a[184:187], v[112:115]\n"
"buffer_load_dwordx4 v[76:79], v1, s[24:27], s65 offen offset:3072 sc1\n"
"v_mfma_f32_16x16x32_bf16 v[112:115], v[244:247], a[188:191], v[112:115]\n"
"ds_write_b128 v170, v[108:111]\n"
"s_add_u32 s65, s65, 0x1000\n"
"v_mfma_f32_16x16x32_bf16 v[116:119], v[216:219], a[224:227], v[116:119]\n"
"v_mfma_f32_16x16x32_bf16 v[116:119], v[220:223], a[228:231], v[116:119]\n"
"v_mfma_f32_16x16x32_bf16 v[116:119], v[224:227], a[232:235], v[116:119]\n"
"buffer_load_dwordx4 v[80:83], v1, s[24:27], s65 offen sc1\n"
"v_mfma_f32_16x16x32_bf16 v[116:119], v[228:231], a[236:239], v[116:119]\n"
"v_mfma_f32_16x16x32_bf16 v[116:119], v[232:235], a[240:243], v[116:119]\n"
"v_mfma_f32_16x16x32_bf16 v[116:119], v[236:239], a[244:247], v[116:119]\n"
"v_mfma_f32_16x16x32_bf16 v[116:119], v[240:243], a[248:251], v[116:119]\n"
"buffer_load_dwordx4 v[84:87], v1, s[24:27], s65 offen offset:1024 sc1\n"
"v_mfma_f32_16x16x32_bf16 v[116:119], v[244:247], a[252:255], v[116:119]\n"
"ds_write_b128 v171, v[112:115] offset:1024\n"
"v_mfma_f32_16x16x32_bf16 v[104:107], v[216:219], a[32:35], v[104:107]\n"
"v_mfma_f32_16x16x32_bf16 v[104:107], v[220:223], a[36:39], v[104:107]\n"
"v_mfma_f32_16x16x32_bf16 v[104:107], v[224:227], a[40:43], v[104:107]\n"
"buffer_load_dwordx4 v[88:91], v1, s[24:27], s65 offen offset:2048 sc1\n"
"v_mfma_f32_16x16x32_bf16 v[104:107], v[228:231], a[44:47], v[104:107]\n"
"v_mfma_f32_16x16x32_bf16 v[104:107], v[232:235], a[48:51], v[104:107]\n"
"v_mfma_f32_16x16x32_bf16 v[104:107], v[236:239], a[52:55], v[104:107]\n"
"v_mfma_f32_16x16x32_bf16 v[104:107], v[240:243], a[56:59], v[104:107]\n"
"ds_write_b128 v172, v[116:119] offset:2048\n"
"buffer_load_dwordx4 v[92:95], v1, s[24:27], s65 offen offset:3072 sc1\n"
"v_mfma_f32_16x16x32_bf16 v[120:123], v[244:247], a[60:63], v[104:107]\n"
"s_add_u32 s65, s65, 0x2d000\n"
"s_waitcnt vmcnt(14)\n"
"v_mfma_f32_16x16x32_bf16 v[108:111], v[32:35], a[64:67], 0\n"
"v_mfma_f32_16x16x32_bf16 v[108:111], v[36:39], a[68:71], v[108:111]\n"
"s_waitcnt vmcnt(12)\n"
"v_mfma_f32_16x16x32_bf16 v[108:111], v[40:43], a[72:75], v[108:111]\n"
"buffer_load_dwordx4 v[184:187], v1, s[24:27], s65 offen sc1\n"
"v_mfma_f32_16x16x32_bf16 v[108:111], v[44:47], a[76:79], v[108:111]\n"
"s_waitcnt lgkmcnt(0)\n"
"s_barrier\n"
"s_waitcnt vmcnt(11)\n"
"v_mfma_f32_16x16x32_bf16 v[108:111], v[48:51], a[80:83], v[108:111]\n"
"ds_read_b128 v[136:139], v10\n"
"v_mfma_f32_16x16x32_bf16 v[108:111], v[52:55], a[84:87], v[108:111]\n"
"ds_read_b128 v[140:143], v10 offset:1024\n"
"s_waitcnt vmcnt(9)\n"
"v_mfma_f32_16x16x32_bf16 v[108:111], v[56:59], a[88:91], v[108:111]\n"
"buffer_load_dwordx4 v[188:191], v1, s[24:27], s65 offen offset:1024 sc1\n"
"v_mfma_f32_16x16x32_bf16 v[108:111], v[60:63], a[92:95], v[108:111]\n"
"ds_read_b128 v[144:147], v10 offset:2048\n"
"v_mfma_f32_16x16x32_bf16 v[112:115], v[32:35], a[128:131], 0\n"
"v_mfma_f32_16x16x32_bf16 v[112:115], v[36:39], a[132:135], v[112:115]\n"
"v_mfma_f32_16x16x32_bf16 v[112:115], v[40:43], a[136:139], v[112:115]\n"
"buffer_load_dwordx4 v[192:195], v1, s[24:27], s65 offen offset:2048 sc1\n"
"v_mfma_f32_16x16x32_bf16 v[112:115], v[44:47], a[140:143], v[112:115]\n"
"v_mfma_f32_16x16x32_bf16 v[112:115], v[48:51], a[144:147], v[112:115]\n"
"v_mfma_f32_16x16x32_bf16 v[112:115], v[52:55], a[148:151], v[112:115]\n"
"s_waitcnt lgkmcnt(0)\n"
"v_add_f32 v120, v120, v136\n"
"v_mfma_f32_16x16x32_bf16 v[112:115], v[56:59], a[152:155], v[112:115]\n"
"buffer_load_dwordx4 v[196:199], v1, s[24:27], s65 offen offset:3072 sc1\n"
"v_mfma_f32_16x16x32_bf16 v[112:115], v[60:63], a[156:159], v[112:115]\n"
"v_add_f32 v121, v121, v137\n"
"v_add_f32 v122, v122, v138\n"
"s_add_u32 s65, s65, 0x1000\n"
"v_mfma_f32_16x16x32_bf16 v[116:119], v[32:35], a[192:195], 0\n"
"v_add_f32 v123, v123, v139\n"
"v_add_f32 v120, v120, v140\n"
"v_mfma_f32_16x16x32_bf16 v[116:119], v[36:39], a[196:199], v[116:119]\n"
"v_add_f32 v121, v121, v141\n"
"v_add_f32 v122, v122, v142\n"
"v_mfma_f32_16x16x32_bf16 v[116:119], v[40:43], a[200:203], v[116:119]\n"
"buffer_load_dwordx4 v[200:203], v1, s[24:27], s65 offen sc1\n"
"v_mfma_f32_16x16x32_bf16 v[116:119], v[44:47], a[204:207], v[116:119]\n"
"v_add_f32 v123, v123, v143\n"
"v_add_f32 v120, v120, v144\n"
"v_mfma_f32_16x16x32_bf16 v[116:119], v[48:51], a[208:211], v[116:119]\n"
"v_add_f32 v121, v121, v145\n"
"v_add_f32 v122, v122, v146\n"
"v_mfma_f32_16x16x32_bf16 v[116:119], v[52:55], a[212:215], v[116:119]\n"
"v_add_f32 v123, v123, v147\n"
"v_mfma_f32_16x16x32_bf16 v[116:119], v[56:59], a[216:219], v[116:119]\n"
"buffer_load_dwordx4 v[204:207], v1, s[24:27], s65 offen offset:1024 sc1\n"
"v_mfma_f32_16x16x32_bf16 v[116:119], v[60:63], a[220:223], v[116:119]\n"
"v_cvt_pk_bf16_f32 v148, v120, v121\n"
"v_mfma_f32_16x16x32_bf16 v[104:107], v[32:35], a[0:3], 0\n"
"v_cvt_pk_bf16_f32 v149, v122, v123\n"
"v_mfma_f32_16x16x32_bf16 v[104:107], v[36:39], a[4:7], v[104:107]\n"
"v_mfma_f32_16x16x32_bf16 v[104:107], v[40:43], a[8:11], v[104:107]\n"
"buffer_load_dwordx4 v[208:211], v1, s[24:27], s65 offen offset:2048 sc1\n"
"v_mfma_f32_16x16x32_bf16 v[104:107], v[44:47], a[12:15], v[104:107]\n"
"v_mfma_f32_16x16x32_bf16 v[104:107], v[48:51], a[16:19], v[104:107]\n"
"v_mfma_f32_16x16x32_bf16 v[104:107], v[52:55], a[20:23], v[104:107]\n"
"v_mfma_f32_16x16x32_bf16 v[104:107], v[56:59], a[24:27], v[104:107]\n"
"buffer_load_dwordx4 v[212:215], v1, s[24:27], s65 offen offset:3072 sc1\n"
"v_mfma_f32_16x16x32_bf16 v[104:107], v[60:63], a[28:31], v[104:107]\n"
"s_add_u32 s65, s65, 0x1000\n"
"s_waitcnt vmcnt(14)\n"
"v_mfma_f32_16x16x32_bf16 v[108:111], v[64:67], a[96:99], v[108:111]\n"
"v_mfma_f32_16x16x32_bf16 v[108:111], v[68:71], a[100:103], v[108:111]\n"
"s_or_b32 s62, s59, s56\n"
"s_cbranch_scc1 Lptf%=\n"
"s_bfm_b64 exec, 32, 0\n"
"buffer_atomic_add v27, v26, s[24:27], 0 offen offset:384 sc1\n"
"s_mov_b64 exec, -1\n"
"Lptf%=:\n"
"s_waitcnt vmcnt(12)\n"
"v_mfma_f32_16x16x32_bf16 v[108:111], v[72:75], a[104:107], v[108:111]\n"
"buffer_load_dwordx4 v[216:219], v1, s[24:27], s65 offen sc1\n"
"v_mfma_f32_16x16x32_bf16 v[108:111], v[76:79], a[108:111], v[108:111]\n"
"s_waitcnt vmcnt(11)\n"
"v_mfma_f32_16x16x32_bf16 v[108:111], v[80:83], a[112:115], v[108:111]\n"
"v_mfma_f32_16x16x32_bf16 v[108:111], v[84:87], a[116:119], v[108:111]\n"
"s_waitcnt vmcnt(9)\n"
"v_mfma_f32_16x16x32_bf16 v[108:111], v[88:91], a[120:123], v[108:111]\n"
"buffer_load_dwordx4 v[220:223], v1, s[24:27], s65 offen offset:1024 sc1\n"
"v_mfma_f32_16x16x32_bf16 v[108:111], v[92:95], a[124:127], v[108:111]\n"
"v_mfma_f32_16x16x32_bf16 v[112:115], v[64:67], a[160:163], v[112:115]\n"
"v_mfma_f32_16x16x32_bf16 v[112:115], v[68:71], a[164:167], v[112:115]\n"
"v_mfma_f32_16x16x32_bf16 v[112:115], v[72:75], a[168:171], v[112:115]\n"
"buffer_load_dwordx4 v[224:227], v1, s[24:27], s65 offen offset:2048 sc1\n"
"v_mfma_f32_16x16x32_bf16 v[112:115], v[76:79], a[172:175], v[112:115]\n"
"v_mfma_f32_16x16x32_bf16 v[112:115], v[80:83], a[176:179], v[112:115]\n"
"v_mfma_f32_16x16x32_bf16 v[112:115], v[84:87], a[180:183], v[112:115]\n"
"v_mfma_f32_16x16x32_bf16 v[112:115], v[88:91], a[184:187], v[112:115]\n"
"buffer_load_dwordx4 v[228:231], v1, s[24:27], s65 offen offset:3072 sc1\n"
"v_mfma_f32_16x16x32_bf16 v[112:115], v[92:95], a[188:191], v[112:115]\n"
"ds_write_b128 v170, v[108:111] offset:12288\n"
"s_add_u32 s65, s65, 0x1000\n"
"v_mfma_f32_16x16x32_bf16 v[116:119], v[64:67], a[224:227], v[116:119]\n"
"v_mfma_f32_16x16x32_bf16 v[116:119], v[68:71], a[228:231], v[116:119]\n"
"v_mfma_f32_16x16x32_bf16 v[116:119], v[72:75], a[232:235], v[116:119]\n"
"buffer_load_dwordx4 v[232:235], v1, s[24:27], s65 offen sc1\n"
"v_mfma_f32_16x16x32_bf16 v[116:119], v[76:79], a[236:239], v[116:119]\n"
"v_mfma_f32_16x16x32_bf16 v[116:119], v[80:83], a[240:243], v[116:119]\n"
"v_mfma_f32_16x16x32_bf16 v[116:119], v[84:87], a[244:247], v[116:119]\n"
"v_mfma_f32_16x16x32_bf16 v[116:119], v[88:91], a[248:251], v[116:119]\n"
"buffer_load_dwordx4 v[236:239], v1, s[24:27], s65 offen offset:1024 sc1\n"
"v_mfma_f32_16x16x32_bf16 v[116:119], v[92:95], a[252:255], v[116:119]\n"
"ds_write_b128 v171, v[112:115] offset:13312\n"
"v_mfma_f32_16x16x32_bf16 v[104:107], v[64:67], a[32:35], v[104:107]\n"
"v_mfma_f32_16x16x32_bf16 v[104:107], v[68:71], a[36:39], v[104:107]\n"
"v_mfma_f32_16x16x32_bf16 v[104:107], v[72:75], a[40:43], v[104:107]\n"
"buffer_load_dwordx4 v[240:243], v1, s[24:27], s65 offen offset:2048 sc1\n"
"v_mfma_f32_16x16x32_bf16 v[104:107], v[76:79], a[44:47], v[104:107]\n"
"v_mfma_f32_16x16x32_bf16 v[104:107], v[80:83], a[48:51], v[104:107]\n"
"v_mfma_f32_16x16x32_bf16 v[104:107], v[84:87], a[52:55], v[104:107]\n"
"v_mfma_f32_16x16x32_bf16 v[104:107], v[88:91], a[56:59], v[104:107]\n"
"ds_write_b128 v172, v[116:119] offset:14336\n"
"buffer_load_dwordx4 v[244:247], v1, s[24:27], s65 offen offset:3072 sc1\n"
"v_mfma_f32_16x16x32_bf16 v[120:123], v[92:95], a[60:63], v[104:107]\n"
"s_add_u32 s65, s65, 0xd000\n"
"s_waitcnt vmcnt(14)\n"
"v_mfma_f32_16x16x32_bf16 v[108:111], v[184:187], a[64:67], 0\n"
"v_mfma_f32_16x16x32_bf16 v[108:111], v[188:191], a[68:71], v[108:111]\n"
"s_waitcnt vmcnt(12)\n"
"v_mfma_f32_16x16x32_bf16 v[108:111], v[192:195], a[72:75], v[108:111]\n"
"buffer_load_dwordx4 v[32:35], v1, s[24:27], s65 offen sc1\n"
"v_mfma_f32_16x16x32_bf16 v[108:111], v[196:199], a[76:79], v[108:111]\n"
"s_waitcnt lgkmcnt(0)\n"
"s_barrier\n"
"s_waitcnt vmcnt(11)\n"
"v_mfma_f32_16x16x32_bf16 v[108:111], v[200:203], a[80:83], v[108:111]\n"
"ds_read_b128 v[136:139], v10 offset:12288\n"
"v_mfma_f32_16x16x32_bf16 v[108:111], v[204:207], a[84:87], v[108:111]\n"
"ds_read_b128 v[140:143], v10 offset:13312\n"
"s_waitcnt vmcnt(9)\n"
"v_mfma_f32_16x16x32_bf16 v[108:111], v[208:211], a[88:91], v[108:111]\n"
"buffer_load_dwordx4 v[36:39], v1, s[24:27], s65 offen offset:1024 sc1\n"
"v_mfma_f32_16x16x32_bf16 v[108:111], v[212:215], a[92:95], v[108:111]\n"
"ds_read_b128 v[144:147], v10 offset:14336\n"
"v_mfma_f32_16x16x32_bf16 v[112:115], v[184:187], a[128:131], 0\n"
"v_mfma_f32_16x16x32_bf16 v[112:115], v[188:191], a[132:135], v[112:115]\n"
"v_mfma_f32_16x16x32_bf16 v[112:115], v[192:195], a[136:139], v[112:115]\n"
"buffer_load_dwordx4 v[40:43], v1, s[24:27], s65 offen offset:2048 sc1\n"
"v_mfma_f32_16x16x32_bf16 v[112:115], v[196:199], a[140:143], v[112:115]\n"
"v_mfma_f32_16x16x32_bf16 v[112:115], v[200:203], a[144:147], v[112:115]\n"
"v_mfma_f32_16x16x32_bf16 v[112:115], v[204:207], a[148:151], v[112:115]\n"
"s_waitcnt lgkmcnt(0)\n"
"v_add_f32 v120, v120, v136\n"
"v_mfma_f32_16x16x32_bf16 v[112:115], v[208:211], a[152:155], v[112:115]\n"
"buffer_load_dwordx4 v[44:47], v1, s[24:27], s65 offen offset:3072 sc1\n"
"v_mfma_f32_16x16x32_bf16 v[112:115], v[212:215], a[156:159], v[112:115]\n"
"v_add_f32 v121, v121, v137\n"
"v_add_f32 v122, v122, v138\n"
"s_add_u32 s65, s65, 0x1000\n"
"v_mfma_f32_16x16x32_bf16 v[116:119], v[184:187], a[192:195], 0\n"
"v_add_f32 v123, v123, v139\n"
"v_add_f32 v120, v120, v140\n"
"v_mfma_f32_16x16x32_bf16 v[116:119], v[188:191], a[196:199], v[116:119]\n"
"v_add_f32 v121, v121, v141\n"
"v_add_f32 v122, v122, v142\n"
"v_mfma_f32_16x16x32_bf16 v[116:119], v[192:195], a[200:203], v[116:119]\n"
"buffer_load_dwordx4 v[48:51], v1, s[24:27], s65 offen sc1\n"
"v_mfma_f32_16x16x32_bf16 v[116:119], v[196:199], a[204:207], v[116:119]\n"
"v_add_f32 v123, v123, v143\n"
"v_add_f32 v120, v120, v144\n"
"v_mfma_f32_16x16x32_bf16 v[116:119], v[200:203], a[208:211], v[116:119]\n"
"v_add_f32 v121, v121, v145\n"
"v_add_f32 v122, v122, v146\n"
"v_mfma_f32_16x16x32_bf16 v[116:119], v[204:207], a[212:215], v[116:119]\n"
"v_add_f32 v123, v123, v147\n"
"v_mfma_f32_16x16x32_bf16 v[116:119], v[208:211], a[216:219], v[116:119]\n"
"buffer_load_dwordx4 v[52:55], v1, s[24:27], s65 offen offset:1024 sc1\n"
"v_mfma_f32_16x16x32_bf16 v[116:119], v[212:215], a[220:223], v[116:119]\n"
"v_cvt_pk_bf16_f32 v150, v120, v121\n"
"v_mfma_f32_16x16x32_bf16 v[104:107], v[184:187], a[0:3], 0\n"
"v_cvt_pk_bf16_f32 v151, v122, v123\n"
"v_mfma_f32_16x16x32_bf16 v[104:107], v[188:191], a[4:7], v[104:107]\n"
"buffer_store_dwordx4 v[148:151], v1, s[24:27], s72 offen sc1\n"
"v_mfma_f32_16x16x32_bf16 v[104:107], v[192:195], a[8:11], v[104:107]\n"
"buffer_load_dwordx4 v[56:59], v1, s[24:27], s65 offen offset:2048 sc1\n"
"v_mfma_f32_16x16x32_bf16 v[104:107], v[196:199], a[12:15], v[104:107]\n"
"v_mfma_f32_16x16x32_bf16 v[104:107], v[200:203], a[16:19], v[104:107]\n"
"v_mfma_f32_16x16x32_bf16 v[104:107], v[204:207], a[20:23], v[104:107]\n"
"v_mfma_f32_16x16x32_bf16 v[104:107], v[208:211], a[24:27], v[104:107]\n"
"buffer_load_dwordx4 v[60:63], v1, s[24:27], s65 offen offset:3072 sc1\n"
"v_mfma_f32_16x16x32_bf16 v[104:107], v[212:215], a[28:31], v[104:107]\n"
"s_add_u32 s65, s65, 0x1000\n"
"s_waitcnt vmcnt(15)\n"
"v_mfma_f32_16x16x32_bf16 v[108:111], v[216:219], a[96:99], v[108:111]\n"
"v_mfma_f32_16x16x32_bf16 v[108:111], v[220:223], a[100:103], v[108:111]\n"
"s_waitcnt vmcnt(13)\n"
"v_mfma_f32_16x16x32_bf16 v[108:111], v[224:227], a[104:107], v[108:111]\n"
"buffer_load_dwordx4 v[64:67], v1, s[24:27], s65 offen sc1\n"
"v_mfma_f32_16x16x32_bf16 v[108:111], v[228:231], a[108:111], v[108:111]\n"
"s_waitcnt vmcnt(12)\n"
"v_mfma_f32_16x16x32_bf16 v[108:111], v[232:235], a[112:115], v[108:111]\n"
"v_mfma_f32_16x16x32_bf16 v[108:111], v[236:239], a[116:119], v[108:111]\n"
"s_waitcnt vmcnt(10)\n"
"v_mfma_f32_16x16x32_bf16 v[108:111], v[240:243], a[120:123], v[108:111]\n"
"buffer_load_dwordx4 v[68:71], v1, s[24:27], s65 offen offset:1024 sc1\n"
"v_mfma_f32_16x16x32_bf16 v[108:111], v[244:247], a[124:127], v[108:111]\n"
"v_mfma_f32_16x16x32_bf16 v[112:115], v[216:219], a[160:163], v[112:115]\n"
"v_mfma_f32_16x16x32_bf16 v[112:115], v[220:223], a[164:167], v[112:115]\n"
"v_mfma_f32_16x16x32_bf16 v[112:115], v[224:227], a[168:171], v[112:115]\n"
"buffer_load_dwordx4 v[72:75], v1, s[24:27], s65 offen offset:2048 sc1\n"
"v_mfma_f32_16x16x32_bf16 v[112:115], v[228:231], a[172:175], v[112:115]\n"
"v_mfma_f32_16x16x32_bf16 v[112:115], v[232:235], a[176:179], v[112:115]\n"
"v_mfma_f32_16x16x32_bf16 v[112:115], v[236:239], a[180:183], v[112:115]\n"
"v_mfma_f32_16x16x32_bf16 v[112:115], v[240:243], a[184:187], v[112:115]\n"
"buffer_load_dwordx4 v[76:79], v1, s[24:27], s65 offen offset:3072 sc1\n"
"v_mfma_f32_16x16x32_bf16 v[112:115], v[244:247], a[188:191], v[112:115]\n"
"ds_write_b128 v170, v[108:111]\n"
"s_add_u32 s65, s65, 0x1000\n"
"v_mfma_f32_16x16x32_bf16 v[116:119], v[216:219], a[224:227], v[116:119]\n"
"v_mfma_f32_16x16x32_bf16 v[116:119], v[220:223], a[228:231], v[116:119]\n"
"v_mfma_f32_16x16x32_bf16 v[116:119], v[224:227], a[232:235], v[116:119]\n"
"buffer_load_dwordx4 v[80:83], v1, s[24:27], s65 offen sc1\n"
"v_mfma_f32_16x16x32_bf16 v[116:119], v[228:231], a[236:239], v[116:119]\n"
"v_mfma_f32_16x16x32_bf16 v[116:119], v[232:235], a[240:243], v[116:119]\n"
"v_mfma_f32_16x16x32_bf16 v[116:119], v[236:239], a[244:247], v[116:119]\n"
"v_mfma_f32_16x16x32_bf16 v[116:119], v[240:243], a[248:251], v[116:119]\n"
"buffer_load_dwordx4 v[84:87], v1, s[24:27], s65 offen offset:1024 sc1\n"
"v_mfma_f32_16x16x32_bf16 v[116:119], v[244:247], a[252:255], v[116:119]\n"
"ds_write_b128 v171, v[112:115] offset:1024\n"
"v_mfma_f32_16x16x32_bf16 v[104:107], v[216:219], a[32:35], v[104:107]\n"
"v_mfma_f32_16x16x32_bf16 v[104:107], v[220:223], a[36:39], v[104:107]\n"
"v_mfma_f32_16x16x32_bf16 v[104:107], v[224:227], a[40:43], v[104:107]\n"
"buffer_load_dwordx4 v[88:91], v1, s[24:27], s65 offen offset:2048 sc1\n"
"v_mfma_f32_16x16x32_bf16 v[104:107], v[228:231], a[44:47], v[104:107]\n"
"v_mfma_f32_16x16x32_bf16 v[104:107], v[232:235], a[48:51], v[104:107]\n"
"v_mfma_f32_16x16x32_bf16 v[104:107], v[236:239], a[52:55], v[104:107]\n"
"v_mfma_f32_16x16x32_bf16 v[104:107], v[240:243], a[56:59], v[104:107]\n"
"ds_write_b128 v172, v[116:119] offset:2048\n"
"buffer_load_dwordx4 v[92:95], v1, s[24:27], s65 offen offset:3072 sc1\n"
"v_mfma_f32_16x16x32_bf16 v[120:123], v[244:247], a[60:63], v[104:107]\n"
"s_mov_b32 s65, s84\n"
"s_waitcnt vmcnt(15)\n"
"v_mfma_f32_16x16x32_bf16 v[108:111], v[32:35], a[64:67], 0\n"
"v_mfma_f32_16x16x32_bf16 v[108:111], v[36:39], a[68:71], v[108:111]\n"
"s_waitcnt vmcnt(13)\n"
"v_mfma_f32_16x16x32_bf16 v[108:111], v[40:43], a[72:75], v[108:111]\n"
"buffer_load_dwordx4 v[184:187], v1, s[24:27], s65 offen sc1\n"
"v_mfma_f32_16x16x32_bf16 v[108:111], v[44:47], a[76:79], v[108:111]\n"
"s_waitcnt lgkmcnt(0)\n"
"s_barrier\n"
"s_waitcnt vmcnt(12)\n"
"v_mfma_f32_16x16x32_bf16 v[108:111], v[48:51], a[80:83], v[108:111]\n"
"ds_read_b128 v[136:139], v10\n"
"v_mfma_f32_16x16x32_bf16 v[108:111], v[52:55], a[84:87], v[108:111]\n"
"ds_read_b128 v[140:143], v10 offset:1024\n"
"s_waitcnt vmcnt(9)\n"
"v_mfma_f32_16x16x32_bf16 v[108:111], v[56:59], a[88:91], v[108:111]\n"
"buffer_load_dwordx4 v[188:191], v1, s[24:27], s65 offen offset:1024 sc1\n"
"v_mfma_f32_16x16x32_bf16 v[108:111], v[60:63], a[92:95], v[108:111]\n"
"ds_read_b128 v[144:147], v10 offset:2048\n"
"v_mfma_f32_16x16x32_bf16 v[112:115], v[32:35], a[128:131], 0\n"
"v_mfma_f32_16x16x32_bf16 v[112:115], v[36:39], a[132:135], v[112:115]\n"
"v_mfma_f32_16x16x32_bf16 v[112:115], v[40:43], a[136:139], v[112:115]\n"
"buffer_load_dwordx4 v[192:195], v1, s[24:27], s65 offen offset:2048 sc1\n"
"v_mfma_f32_16x16x32_bf16 v[112:115], v[44:47], a[140:143], v[112:115]\n"
"v_mfma_f32_16x16x32_bf16 v[112:115], v[48:51], a[144:147], v[112:115]\n"
"v_mfma_f32_16x16x32_bf16 v[112:115], v[52:55], a[148:151], v[112:115]\n"
"s_waitcnt lgkmcnt(0)\n"
"v_add_f32 v120, v120, v136\n"
"v_mfma_f32_16x16x32_bf16 v[112:115], v[56:59], a[152:155], v[112:115]\n"
"buffer_load_dwordx4 v[196:199], v1, s[24:27], s65 offen offset:3072 sc1\n"
"v_mfma_f32_16x16x32_bf16 v[112:115], v[60:63], a[156:159], v[112:115]\n"
"v_add_f32 v121, v121, v137\n"
"v_add_f32 v122, v122, v138\n"
"s_add_u32 s65, s65, 0x1000\n"
"v_mfma_f32_16x16x32_bf16 v[116:119], v[32:35], a[192:195], 0\n"
"v_add_f32 v123, v123, v139\n"
"v_add_f32 v120, v120, v140\n"
"v_mfma_f32_16x16x32_bf16 v[116:119], v[36:39], a[196:199], v[116:119]\n"
"v_add_f32 v121, v121, v141\n"
"v_add_f32 v122, v122, v142\n"
"v_mfma_f32_16x16x32_bf16 v[116:119], v[40:43], a[200:203], v[116:119]\n"
"buffer_load_dwordx4 v[200:203], v1, s[24:27], s65 offen sc1\n"
"v_mfma_f32_16x16x32_bf16 v[116:119], v[44:47], a[204:207], v[116:119]\n"
"v_add_f32 v123, v123, v143\n"
"v_add_f32 v120, v120, v144\n"
"v_mfma_f32_16x16x32_bf16 v[116:119], v[48:51], a[208:211], v[116:119]\n"
"v_add_f32 v121, v121, v145\n"
"v_add_f32 v122, v122, v146\n"
"v_mfma_f32_16x16x32_bf16 v[116:119], v[52:55], a[212:215], v[116:119]\n"
"v_add_f32 v123, v123, v147\n"
"v_mfma_f32_16x16x32_bf16 v[116:119], v[56:59], a[216:219], v[116:119]\n"
"buffer_load_dwordx4 v[204:207], v1, s[24:27], s65 offen offset:1024 sc1\n"
"v_mfma_f32_16x16x32_bf16 v[116:119], v[60:63], a[220:223], v[116:119]\n"
"v_cvt_pk_bf16_f32 v148, v120, v121\n"
"v_mfma_f32_16x16x32_bf16 v[104:107], v[32:35], a[0:3], 0\n"
"v_cvt_pk_bf16_f32 v149, v122, v123\n"
"v_mfma_f32_16x16x32_bf16 v[104:107], v[36:39], a[4:7], v[104:107]\n"
"v_mfma_f32_16x16x32_bf16 v[104:107], v[40:43], a[8:11], v[104:107]\n"
"buffer_load_dwordx4 v[208:211], v1, s[24:27], s65 offen offset:2048 sc1\n"
"v_mfma_f32_16x16x32_bf16 v[104:107], v[44:47], a[12:15], v[104:107]\n"
"v_mfma_f32_16x16x32_bf16 v[104:107], v[48:51], a[16:19], v[104:107]\n"
"v_mfma_f32_16x16x32_bf16 v[104:107], v[52:55], a[20:23], v[104:107]\n"
"ds_read_b128 v[152:155], v12\n"
"v_mfma_f32_16x16x32_bf16 v[104:107], v[56:59], a[24:27], v[104:107]\n"
"buffer_load_dwordx4 v[212:215], v1, s[24:27], s65 offen offset:3072 sc1\n"
"v_mfma_f32_16x16x32_bf16 v[104:107], v[60:63], a[28:31], v[104:107]\n"
"s_add_u32 s65, s65, 0x1000\n"
"s_waitcnt vmcnt(14)\n"
"v_mfma_f32_16x16x32_bf16 v[108:111], v[64:67], a[96:99], v[108:111]\n"
"v_mfma_f32_16x16x32_bf16 v[108:111], v[68:71], a[100:103], v[108:111]\n"
"s_waitcnt vmcnt(12)\n"
"v_mfma_f32_16x16x32_bf16 v[108:111], v[72:75], a[104:107], v[108:111]\n"
"buffer_load_dwordx4 v[216:219], v1, s[24:27], s65 offen sc1\n"
"v_mfma_f32_16x16x32_bf16 v[108:111], v[76:79], a[108:111], v[108:111]\n"
"s_waitcnt vmcnt(11)\n"
"v_mfma_f32_16x16x32_bf16 v[108:111], v[80:83], a[112:115], v[108:111]\n"
"v_mfma_f32_16x16x32_bf16 v[108:111], v[84:87], a[116:119], v[108:111]\n"
"s_waitcnt vmcnt(9)\n"
"v_mfma_f32_16x16x32_bf16 v[108:111], v[88:91], a[120:123], v[108:111]\n"
"buffer_load_dwordx4 v[220:223], v1, s[24:27], s65 offen offset:1024 sc1\n"
"v_mfma_f32_16x16x32_bf16 v[108:111], v[92:95], a[124:127], v[108:111]\n"
"v_mfma_f32_16x16x32_bf16 v[112:115], v[64:67], a[160:163], v[112:115]\n"
"v_mfma_f32_16x16x32_bf16 v[112:115], v[68:71], a[164:167], v[112:115]\n"
"v_mfma_f32_16x16x32_bf16 v[112:115], v[72:75], a[168:171], v[112:115]\n"
"buffer_load_dwordx4 v[224:227], v1, s[24:27], s65 offen offset:2048 sc1\n"
"v_mfma_f32_16x16x32_bf16 v[112:115], v[76:79], a[172:175], v[112:115]\n"
"v_mfma_f32_16x16x32_bf16 v[112:115], v[80:83], a[176:179], v[112:115]\n"
"v_mfma_f32_16x16x32_bf16 v[112:115], v[84:87], a[180:183], v[112:115]\n"
"v_mfma_f32_16x16x32_bf16 v[112:115], v[88:91], a[184:187], v[112:115]\n"
"buffer_load_dwordx4 v[228:231], v1, s[24:27], s65 offen offset:3072 sc1\n"
"v_mfma_f32_16x16x32_bf16 v[112:115], v[92:95], a[188:191], v[112:115]\n"
"ds_write_b128 v170, v[108:111] offset:12288\n"
"s_add_u32 s65, s65, 0x1000\n"
"v_mfma_f32_16x16x32_bf16 v[116:119], v[64:67], a[224:227], v[116:119]\n"
"v_mfma_f32_16x16x32_bf16 v[116:119], v[68:71], a[228:231], v[116:119]\n"
"v_mfma_f32_16x16x32_bf16 v[116:119], v[72:75], a[232:235], v[116:119]\n"
"buffer_load_dwordx4 v[232:235], v1, s[24:27], s65 offen sc1\n"
"v_mfma_f32_16x16x32_bf16 v[116:119], v[76:79], a[236:239], v[116:119]\n"
"v_mfma_f32_16x16x32_bf16 v[116:119], v[80:83], a[240:243], v[116:119]\n"
"v_mfma_f32_16x16x32_bf16 v[116:119], v[84:87], a[244:247], v[116:119]\n"
"v_mfma_f32_16x16x32_bf16 v[116:119], v[88:91], a[248:251], v[116:119]\n"
"buffer_load_dwordx4 v[236:239], v1, s[24:27], s65 offen offset:1024 sc1\n"
"v_mfma_f32_16x16x32_bf16 v[116:119], v[92:95], a[252:255], v[116:119]\n"
"ds_write_b128 v171, v[112:115] offset:13312\n"
"v_mfma_f32_16x16x32_bf16 v[104:107], v[64:67], a[32:35], v[104:107]\n"
"v_mfma_f32_16x16x32_bf16 v[104:107], v[68:71], a[36:39], v[104:107]\n"
"v_mfma_f32_16x16x32_bf16 v[104:107], v[72:75], a[40:43], v[104:107]\n"
"buffer_load_dwordx4 v[240:243], v1, s[24:27], s65 offen offset:2048 sc1\n"
"v_mfma_f32_16x16x32_bf16 v[104:107], v[76:79], a[44:47], v[104:107]\n"
"v_mfma_f32_16x16x32_bf16 v[104:107], v[80:83], a[48:51], v[104:107]\n"
"v_mfma_f32_16x16x32_bf16 v[104:107], v[84:87], a[52:55], v[104:107]\n"
"v_mfma_f32_16x16x32_bf16 v[104:107], v[88:91], a[56:59], v[104:107]\n"
"ds_write_b128 v172, v[116:119] offset:14336\n"
"buffer_load_dwordx4 v[244:247], v1, s[24:27], s65 offen offset:3072 sc1\n"
"v_mfma_f32_16x16x32_bf16 v[120:123], v[92:95], a[60:63], v[104:107]\n"
"s_add_u32 s65, s65, 0xd000\n"
"s_waitcnt vmcnt(14)\n"
"v_mfma_f32_16x16x32_bf16 v[108:111], v[184:187], a[64:67], 0\n"
"v_mfma_f32_16x16x32_bf16 v[108:111], v[188:191], a[68:71], v[108:111]\n"
"s_waitcnt vmcnt(12)\n"
"v_mfma_f32_16x16x32_bf16 v[108:111], v[192:195], a[72:75], v[108:111]\n"
"buffer_load_dwordx4 v[32:35], v1, s[24:27], s65 offen sc1\n"
"v_mfma_f32_16x16x32_bf16 v[108:111], v[196:199], a[76:79], v[108:111]\n"
"s_waitcnt lgkmcnt(0)\n"
"s_barrier\n"
"s_waitcnt vmcnt(11)\n"
"v_mfma_f32_16x16x32_bf16 v[108:111], v[200:203], a[80:83], v[108:111]\n"
"ds_read_b128 v[136:139], v10 offset:12288\n"
"v_mfma_f32_16x16x32_bf16 v[108:111], v[204:207], a[84:87], v[108:111]\n"
"ds_read_b128 v[140:143], v10 offset:13312\n"
"s_waitcnt vmcnt(9)\n"
"v_mfma_f32_16x16x32_bf16 v[108:111], v[208:211], a[88:91], v[108:111]\n"
"buffer_load_dwordx4 v[36:39], v1, s[24:27], s65 offen offset:1024 sc1\n"
"v_mfma_f32_16x16x32_bf16 v[108:111], v[212:215], a[92:95], v[108:111]\n"
"ds_read_b128 v[144:147], v10 offset:14336\n"
"v_mfma_f32_16x16x32_bf16 v[112:115], v[184:187], a[128:131], 0\n"
"v_mfma_f32_16x16x32_bf16 v[112:115], v[188:191], a[132:135], v[112:115]\n"
"v_mfma_f32_16x16x32_bf16 v[112:115], v[192:195], a[136:139], v[112:115]\n"
"buffer_load_dwordx4 v[40:43], v1, s[24:27], s65 offen offset:2048 sc1\n"
"v_mfma_f32_16x16x32_bf16 v[112:115], v[196:199], a[140:143], v[112:115]\n"
"v_mfma_f32_16x16x32_bf16 v[112:115], v[200:203], a[144:147], v[112:115]\n"
"v_mfma_f32_16x16x32_bf16 v[112:115], v[204:207], a[148:151], v[112:115]\n"
"s_waitcnt lgkmcnt(0)\n"
"v_add_f32 v120, v120, v136\n"
"v_mfma_f32_16x16x32_bf16 v[112:115], v[208:211], a[152:155], v[112:115]\n"
"buffer_load_dwordx4 v[44:47], v1, s[24:27], s65 offen offset:3072 sc1\n"
"v_mfma_f32_16x16x32_bf16 v[112:115], v[212:215], a[156:159], v[112:115]\n"
"v_add_f32 v121, v121, v137\n"
"v_add_f32 v122, v122, v138\n"
"s_add_u32 s65, s65, 0x1000\n"
"v_mfma_f32_16x16x32_bf16 v[116:119], v[184:187], a[192:195], 0\n"
"v_add_f32 v123, v123, v139\n"
"v_add_f32 v120, v120, v140\n"
"v_mfma_f32_16x16x32_bf16 v[116:119], v[188:191], a[196:199], v[116:119]\n"
"v_add_f32 v121, v121, v141\n"
"v_add_f32 v122, v122, v142\n"
"v_mfma_f32_16x16x32_bf16 v[116:119], v[192:195], a[200:203], v[116:119]\n"
"buffer_load_dwordx4 v[48:51], v1, s[24:27], s65 offen sc1\n"
"v_mfma_f32_16x16x32_bf16 v[116:119], v[196:199], a[204:207], v[116:119]\n"
"v_add_f32 v123, v123, v143\n"
"v_add_f32 v120, v120, v144\n"
"v_mfma_f32_16x16x32_bf16 v[116:119], v[200:203], a[208:211], v[116:119]\n"
"v_add_f32 v121, v121, v145\n"
"v_add_f32 v122, v122, v146\n"
"v_mfma_f32_16x16x32_bf16 v[116:119], v[204:207], a[212:215], v[116:119]\n"
"v_add_f32 v123, v123, v147\n"
"v_mfma_f32_16x16x32_bf16 v[116:119], v[208:211], a[216:219], v[116:119]\n"
"buffer_load_dwordx4 v[52:55], v1, s[24:27], s65 offen offset:1024 sc1\n"
"v_mfma_f32_16x16x32_bf16 v[116:119], v[212:215], a[220:223], v[116:119]\n"
"v_cvt_pk_bf16_f32 v150, v120, v121\n"
"v_mfma_f32_16x16x32_bf16 v[104:107], v[22:25], v[152:155], 0\n"
"v_mfma_f32_16x16x32_bf16 v[104:107], v[184:187], a[0:3], v[104:107]\n"
"v_cvt_pk_bf16_f32 v151, v122, v123\n"
"v_mfma_f32_16x16x32_bf16 v[104:107], v[188:191], a[4:7], v[104:107]\n"
"buffer_store_dwordx4 v[148:151], v1, s[24:27], s72 offen offset:2048 sc1\n"
"v_mfma_f32_16x16x32_bf16 v[104:107], v[192:195], a[8:11], v[104:107]\n"
"buffer_load_dwordx4 v[56:59], v1, s[24:27], s65 offen offset:2048 sc1\n"
"v_mfma_f32_16x16x32_bf16 v[104:107], v[196:199], a[12:15], v[104:107]\n"
"v_mfma_f32_16x16x32_bf16 v[104:107], v[200:203], a[16:19], v[104:107]\n"
"v_mfma_f32_16x16x32_bf16 v[104:107], v[204:207], a[20:23], v[104:107]\n"
"s_xor_b32 s64, s75, 0x2000\n"
"v_mfma_f32_16x16x32_bf16 v[104:107], v[208:211], a[24:27], v[104:107]\n"
"buffer_load_dwordx4 v[60:63], v1, s[24:27], s65 offen offset:3072 sc1\n"
"v_mfma_f32_16x16x32_bf16 v[104:107], v[212:215], a[28:31], v[104:107]\n"
"s_add_u32 s65, s65, 0x1000\n"
"s_waitcnt vmcnt(15)\n"
"v_mfma_f32_16x16x32_bf16 v[108:111], v[216:219], a[96:99], v[108:111]\n"
"v_mfma_f32_16x16x32_bf16 v[108:111], v[220:223], a[100:103], v[108:111]\n"
"s_waitcnt vmcnt(13)\n"
"v_mfma_f32_16x16x32_bf16 v[108:111], v[224:227], a[104:107], v[108:111]\n"
"buffer_load_dwordx4 v[64:67], v1, s[24:27], s65 offen sc1\n"
"v_mfma_f32_16x16x32_bf16 v[108:111], v[228:231], a[108:111], v[108:111]\n"
"s_waitcnt vmcnt(12)\n"
"v_mfma_f32_16x16x32_bf16 v[108:111], v[232:235], a[112:115], v[108:111]\n"
"v_mfma_f32_16x16x32_bf16 v[108:111], v[236:239], a[116:119], v[108:111]\n"
"s_waitcnt vmcnt(10)\n"
"v_mfma_f32_16x16x32_bf16 v[108:111], v[240:243], a[120:123], v[108:111]\n"
"buffer_load_dwordx4 v[68:71], v1, s[24:27], s65 offen offset:1024 sc1\n"
"v_mfma_f32_16x16x32_bf16 v[108:111], v[244:247], a[124:127], v[108:111]\n"
"v_mfma_f32_16x16x32_bf16 v[112:115], v[216:219], a[160:163], v[112:115]\n"
"v_mfma_f32_16x16x32_bf16 v[112:115], v[220:223], a[164:167], v[112:115]\n"
"v_mfma_f32_16x16x32_bf16 v[112:115], v[224:227], a[168:171], v[112:115]\n"
"buffer_load_dwordx4 v[72:75], v1, s[24:27], s65 offen offset:2048 sc1\n"
"v_mfma_f32_16x16x32_bf16 v[112:115], v[228:231], a[172:175], v[112:115]\n"
"v_mfma_f32_16x16x32_bf16 v[112:115], v[232:235], a[176:179], v[112:115]\n"
"v_mfma_f32_16x16x32_bf16 v[112:115], v[236:239], a[180:183], v[112:115]\n"
"v_mfma_f32_16x16x32_bf16 v[112:115], v[240:243], a[184:187], v[112:115]\n"
"buffer_load_dwordx4 v[76:79], v1, s[24:27], s65 offen offset:3072 sc1\n"
"v_mfma_f32_16x16x32_bf16 v[112:115], v[244:247], a[188:191], v[112:115]\n"
"ds_write_b128 v170, v[108:111]\n"
"s_add_u32 s65, s65, 0x1000\n"
"v_mfma_f32_16x16x32_bf16 v[116:119], v[216:219], a[224:227], v[116:119]\n"
"v_mfma_f32_16x16x32_bf16 v[116:119], v[220:223], a[228:231], v[116:119]\n"
"v_mfma_f32_16x16x32_bf16 v[116:119], v[224:227], a[232:235], v[116:119]\n"
"buffer_load_dwordx4 v[80:83], v1, s[24:27], s65 offen sc1\n"
"v_mfma_f32_16x16x32_bf16 v[116:119], v[228:231], a[236:239], v[116:119]\n"
"v_mfma_f32_16x16x32_bf16 v[116:119], v[232:235], a[240:243], v[116:119]\n"
"v_mfma_f32_16x16x32_bf16 v[116:119], v[236:239], a[244:247], v[116:119]\n"
"v_mfma_f32_16x16x32_bf16 v[116:119], v[240:243], a[248:251], v[116:119]\n"
"buffer_load_dwordx4 v[84:87], v1, s[24:27], s65 offen offset:1024 sc1\n"
"v_mfma_f32_16x16x32_bf16 v[116:119], v[244:247], a[252:255], v[116:119]\n"
"ds_write_b128 v171, v[112:115] offset:1024\n"
"v_mfma_f32_16x16x32_bf16 v[104:107], v[216:219], a[32:35], v[104:107]\n"
"v_mfma_f32_16x16x32_bf16 v[104:107], v[220:223], a[36:39], v[104:107]\n"
"v_mfma_f32_16x16x32_bf16 v[104:107], v[224:227], a[40:43], v[104:107]\n"
"buffer_load_dwordx4 v[88:91], v1, s[24:27], s65 offen offset:2048 sc1\n"
"v_mfma_f32_16x16x32_bf16 v[104:107], v[228:231], a[44:47], v[104:107]\n"
"v_mfma_f32_16x16x32_bf16 v[104:107], v[232:235], a[48:51], v[104:107]\n"
"v_mfma_f32_16x16x32_bf16 v[104:107], v[236:239], a[52:55], v[104:107]\n"
"v_mfma_f32_16x16x32_bf16 v[104:107], v[240:243], a[56:59], v[104:107]\n"
"ds_write_b128 v172, v[116:119] offset:2048\n"
"buffer_load_dwordx4 v[92:95], v1, s[24:27], s65 offen offset:3072 sc1\n"
"v_mfma_f32_16x16x32_bf16 v[120:123], v[244:247], a[60:63], v[104:107]\n"
"s_mov_b32 s65, s74\n"
"s_waitcnt vmcnt(15)\n"
"v_mfma_f32_16x16x32_bf16 v[108:111], v[32:35], a[64:67], 0\n"
"v_mfma_f32_16x16x32_bf16 v[108:111], v[36:39], a[68:71], v[108:111]\n"
"s_waitcnt vmcnt(13)\n"
"v_mfma_f32_16x16x32_bf16 v[108:111], v[40:43], a[72:75], v[108:111]\n"
"buffer_load_dwordx4 v[184:187], v1, s[24:27], s65 offen sc1\n"
"v_mfma_f32_16x16x32_bf16 v[108:111], v[44:47], a[76:79], v[108:111]\n"
"s_waitcnt lgkmcnt(0)\n"
"s_barrier\n"
"s_waitcnt vmcnt(12)\n"
"v_mfma_f32_16x16x32_bf16 v[108:111], v[48:51], a[80:83], v[108:111]\n"
"ds_read_b128 v[136:139], v10\n"
"v_mfma_f32_16x16x32_bf16 v[108:111], v[52:55], a[84:87], v[108:111]\n"
"ds_read_b128 v[140:143], v10 offset:1024\n"
"s_waitcnt vmcnt(9)\n"
"v_mfma_f32_16x16x32_bf16 v[108:111], v[56:59], a[88:91], v[108:111]\n"
"buffer_load_dwordx4 v[188:191], v1, s[24:27], s65 offen offset:1024 sc1\n"
"v_mfma_f32_16x16x32_bf16 v[108:111], v[60:63], a[92:95], v[108:111]\n"
"ds_read_b128 v[144:147], v10 offset:2048\n"
"v_mfma_f32_16x16x32_bf16 v[112:115], v[32:35], a[128:131], 0\n"
"v_mfma_f32_16x16x32_bf16 v[112:115], v[36:39], a[132:135], v[112:115]\n"
"s_bfm_b64 exec, 1, 0\n"
"buffer_atomic_add v27, off, s[24:27], s64 offset:516 sc1\n"
"s_mov_b64 exec, -1\n"
"v_mfma_f32_16x16x32_bf16 v[112:115], v[40:43], a[136:139], v[112:115]\n"
"buffer_load_dwordx4 v[192:195], v1, s[24:27], s65 offen offset:2048 sc1\n"
"v_mfma_f32_16x16x32_bf16 v[112:115], v[44:47], a[140:143], v[112:115]\n"
"v_mfma_f32_16x16x32_bf16 v[112:115], v[48:51], a[144:147], v[112:115]\n"
"v_mfma_f32_16x16x32_bf16 v[112:115], v[52:55], a[148:151], v[112:115]\n"
"s_waitcnt lgkmcnt(0)\n"
"v_add_f32 v120, v120, v136\n"
"v_mfma_f32_16x16x32_bf16 v[112:115], v[56:59], a[152:155], v[112:115]\n"
"buffer_load_dwordx4 v[196:199], v1, s[24:27], s65 offen offset:3072 sc1\n"
"v_mfma_f32_16x16x32_bf16 v[112:115], v[60:63], a[156:159], v[112:115]\n"
"v_add_f32 v121, v121, v137\n"
"v_add_f32 v122, v122, v138\n"
"s_add_u32 s65, s65, 0x1000\n"
"v_mfma_f32_16x16x32_bf16 v[116:119], v[32:35], a[192:195], 0\n"
"v_add_f32 v123, v123, v139\n"
"v_add_f32 v120, v120, v140\n"
"v_mfma_f32_16x16x32_bf16 v[116:119], v[36:39], a[196:199], v[116:119]\n"
"v_add_f32 v121, v121, v141\n"
"v_add_f32 v122, v122, v142\n"
"v_mfma_f32_16x16x32_bf16 v[116:119], v[40:43], a[200:203], v[116:119]\n"
"buffer_load_dwordx4 v[200:203], v1, s[24:27], s65 offen sc1\n"
"v_mfma_f32_16x16x32_bf16 v[116:119], v[44:47], a[204:207], v[116:119]\n"
"v_add_f32 v123, v123, v143\n"
"v_add_f32 v124, v120, v144\n"
"v_mfma_f32_16x16x32_bf16 v[116:119], v[48:51], a[208:211], v[116:119]\n"
"v_add_f32 v125, v121, v145\n"
"v_add_f32 v126, v122, v146\n"
"v_mfma_f32_16x16x32_bf16 v[116:119], v[52:55], a[212:215], v[116:119]\n"
"v_add_f32 v127, v123, v147\n"
"v_mfma_f32_16x16x32_bf16 v[116:119], v[56:59], a[216:219], v[116:119]\n"
"buffer_load_dwordx4 v[204:207], v1, s[24:27], s65 offen offset:1024 sc1\n"
"v_mfma_f32_16x16x32_bf16 v[116:119], v[60:63], a[220:223], v[116:119]\n"
"v_mfma_f32_16x16x32_bf16 v[104:107], v[20:23], v[152:155], 0\n"
"v_mfma_f32_16x16x32_bf16 v[104:107], v[32:35], a[0:3], v[104:107]\n"
"v_mfma_f32_16x16x32_bf16 v[104:107], v[36:39], a[4:7], v[104:107]\n"
"v_mfma_f32_16x16x32_bf16 v[104:107], v[40:43], a[8:11], v[104:107]\n"
"buffer_load_dwordx4 v[208:211], v1, s[24:27], s65 offen offset:2048 sc1\n"
"v_mfma_f32_16x16x32_bf16 v[104:107], v[44:47], a[12:15], v[104:107]\n"
"v_mfma_f32_16x16x32_bf16 v[104:107], v[48:51], a[16:19], v[104:107]\n"
"v_mfma_f32_16x16x32_bf16 v[104:107], v[52:55], a[20:23], v[104:107]\n"
"ds_read_b128 v[152:155], v11\n"
"v_mfma_f32_16x16x32_bf16 v[104:107], v[56:59], a[24:27], v[104:107]\n"
"buffer_load_dwordx4 v[212:215], v1, s[24:27], s65 offen offset:3072 sc1\n"
"v_mfma_f32_16x16x32_bf16 v[104:107], v[60:63], a[28:31], v[104:107]\n"
"s_add_u32 s65, s65, 0x1000\n"
"s_waitcnt vmcnt(15)\n"
"v_mfma_f32_16x16x32_bf16 v[108:111], v[64:67], a[96:99], v[108:111]\n"
"v_mfma_f32_16x16x32_bf16 v[108:111], v[68:71], a[100:103], v[108:111]\n"
"s_waitcnt vmcnt(13)\n"
"v_mfma_f32_16x16x32_bf16 v[108:111], v[72:75], a[104:107], v[108:111]\n"
"buffer_load_dwordx4 v[216:219], v1, s[24:27], s65 offen sc1\n"
"v_mfma_f32_16x16x32_bf16 v[108:111], v[76:79], a[108:111], v[108:111]\n"
"s_waitcnt vmcnt(12)\n"
"v_mfma_f32_16x16x32_bf16 v[108:111], v[80:83], a[112:115], v[108:111]\n"
"v_mfma_f32_16x16x32_bf16 v[108:111], v[84:87], a[116:119], v[108:111]\n"
"s_waitcnt vmcnt(10)\n"
"v_mfma_f32_16x16x32_bf16 v[108:111], v[88:91], a[120:123], v[108:111]\n"
"buffer_load_dwordx4 v[220:223], v1, s[24:27], s65 offen offset:1024 sc1\n"
"v_mfma_f32_16x16x32_bf16 v[108:111], v[92:95], a[124:127], v[108:111]\n"
"v_mfma_f32_16x16x32_bf16 v[112:115], v[64:67], a[160:163], v[112:115]\n"
"v_mfma_f32_16x16x32_bf16 v[112:115], v[68:71], a[164:167], v[112:115]\n"
"v_mfma_f32_16x16x32_bf16 v[112:115], v[72:75], a[168:171], v[112:115]\n"
"buffer_load_dwordx4 v[224:227], v1, s[24:27], s65 offen offset:2048 sc1\n"
"v_mfma_f32_16x16x32_bf16 v[112:115], v[76:79], a[172:175], v[112:115]\n"
"v_mfma_f32_16x16x32_bf16 v[112:115], v[80:83], a[176:179], v[112:115]\n"
"v_mfma_f32_16x16x32_bf16 v[112:115], v[84:87], a[180:183], v[112:115]\n"
"v_mfma_f32_16x16x32_bf16 v[112:115], v[88:91], a[184:187], v[112:115]\n"
"buffer_load_dwordx4 v[228:231], v1, s[24:27], s65 offen offset:3072 sc1\n"
"v_mfma_f32_16x16x32_bf16 v[112:115], v[92:95], a[188:191], v[112:115]\n"
"ds_write_b128 v170, v[108:111] offset:12288\n"
"s_add_u32 s65, s65, 0x1000\n"
"v_mfma_f32_16x16x32_bf16 v[116:119], v[64:67], a[224:227], v[116:119]\n"
"v_mfma_f32_16x16x32_bf16 v[116:119], v[68:71], a[228:231], v[116:119]\n"
"v_mfma_f32_16x16x32_bf16 v[116:119], v[72:75], a[232:235], v[116:119]\n"
"buffer_load_dwordx4 v[232:235], v1, s[24:27], s65 offen sc1\n"
"v_mfma_f32_16x16x32_bf16 v[116:119], v[76:79], a[236:239], v[116:119]\n"
"v_mfma_f32_16x16x32_bf16 v[116:119], v[80:83], a[240:243], v[116:119]\n"
"v_mfma_f32_16x16x32_bf16 v[116:119], v[84:87], a[244:247], v[116:119]\n"
"v_mfma_f32_16x16x32_bf16 v[116:119], v[88:91], a[248:251], v[116:119]\n"
"buffer_load_dwordx4 v[236:239], v1, s[24:27], s65 offen offset:1024 sc1\n"
"v_mfma_f32_16x16x32_bf16 v[116:119], v[92:95], a[252:255], v[116:119]\n"
"ds_write_b128 v171, v[112:115] offset:13312\n"
"v_mfma_f32_16x16x32_bf16 v[104:107], v[64:67], a[32:35], v[104:107]\n"
"v_mfma_f32_16x16x32_bf16 v[104:107], v[68:71], a[36:39], v[104:107]\n"
"s_mov_b64 exec, 1\n"
"v_mov_b32 v16, 0\n"
"buffer_atomic_add v16, off, s[24:27], s75 offset:516 sc0 sc1\n"
"s_mov_b64 exec, -1\n"
"v_mfma_f32_16x16x32_bf16 v[104:107], v[72:75], a[40:43], v[104:107]\n"
"buffer_load_dwordx4 v[240:243], v1, s[24:27], s65 offen offset:2048 sc1\n"
"v_mfma_f32_16x16x32_bf16 v[104:107], v[76:79], a[44:47], v[104:107]\n"
"v_mfma_f32_16x16x32_bf16 v[104:107], v[80:83], a[48:51], v[104:107]\n"
"v_mfma_f32_16x16x32_bf16 v[104:107], v[84:87], a[52:55], v[104:107]\n"
"v_mfma_f32_16x16x32_bf16 v[104:107], v[88:91], a[56:59], v[104:107]\n"
"ds_write_b128 v172, v[116:119] offset:14336\n"
"buffer_load_dwordx4 v[244:247], v1, s[24:27], s65 offen offset:3072 sc1\n"
"v_mfma_f32_16x16x32_bf16 v[120:123], v[92:95], a[60:63], v[104:107]\n"
"s_add_u32 s65, s84, 0x40000\n"
"s_waitcnt vmcnt(16)\n"
"v_mfma_f32_16x16x32_bf16 v[108:111], v[184:187], a[64:67], 0\n"
"v_mfma_f32_16x16x32_bf16 v[108:111], v[188:191], a[68:71], v[108:111]\n"
"s_waitcnt vmcnt(13)\n"
"v_mfma_f32_16x16x32_bf16 v[108:111], v[192:195], a[72:75], v[108:111]\n"
"buffer_load_dwordx4 v[32:35], v1, s[24:27], s65 offen sc1\n"
"v_mfma_f32_16x16x32_bf16 v[108:111], v[196:199], a[76:79], v[108:111]\n"
"s_waitcnt lgkmcnt(0)\n"
"s_barrier\n"
"s_waitcnt vmcnt(12)\n"
"v_mfma_f32_16x16x32_bf16 v[108:111], v[200:203], a[80:83], v[108:111]\n"
"ds_read_b128 v[136:139], v10 offset:12288\n"
"v_mfma_f32_16x16x32_bf16 v[108:111], v[204:207], a[84:87], v[108:111]\n"
"ds_read_b128 v[140:143], v10 offset:13312\n"
"s_waitcnt vmcnt(10)\n"
"v_mfma_f32_16x16x32_bf16 v[108:111], v[208:211], a[88:91], v[108:111]\n"
"buffer_load_dwordx4 v[36:39], v1, s[24:27], s65 offen offset:1024 sc1\n"
"v_mfma_f32_16x16x32_bf16 v[108:111], v[212:215], a[92:95], v[108:111]\n"
"ds_read_b128 v[144:147], v10 offset:14336\n"
"v_mfma_f32_16x16x32_bf16 v[112:115], v[184:187], a[128:131], 0\n"
"v_mfma_f32_16x16x32_bf16 v[112:115], v[188:191], a[132:135], v[112:115]\n"
"v_mfma_f32_16x16x32_bf16 v[112:115], v[192:195], a[136:139], v[112:115]\n"
"buffer_load_dwordx4 v[40:43], v1, s[24:27], s65 offen offset:2048 sc1\n"
"v_mfma_f32_16x16x32_bf16 v[112:115], v[196:199], a[140:143], v[112:115]\n"
"v_mfma_f32_16x16x32_bf16 v[112:115], v[200:203], a[144:147], v[112:115]\n"
"v_mfma_f32_16x16x32_bf16 v[112:115], v[204:207], a[148:151], v[112:115]\n"
"s_waitcnt lgkmcnt(0)\n"
"v_add_f32 v120, v120, v136\n"
"v_mfma_f32_16x16x32_bf16 v[112:115], v[208:211], a[152:155], v[112:115]\n"
"buffer_load_dwordx4 v[44:47], v1, s[24:27], s65 offen offset:3072 sc1\n"
"v_mfma_f32_16x16x32_bf16 v[112:115], v[212:215], a[156:159], v[112:115]\n"
"v_add_f32 v121, v121, v137\n"
"v_add_f32 v122, v122, v138\n"
"s_add_u32 s65, s65, 0x1000\n"
"v_mfma_f32_16x16x32_bf16 v[116:119], v[184:187], a[192:195], 0\n"
"v_add_f32 v123, v123, v139\n"
"v_add_f32 v120, v120, v140\n"
"v_mfma_f32_16x16x32_bf16 v[116:119], v[188:191], a[196:199], v[116:119]\n"
"v_add_f32 v121, v121, v141\n"
"v_add_f32 v122, v122, v142\n"
"v_mfma_f32_16x16x32_bf16 v[116:119], v[192:195], a[200:203], v[116:119]\n"
"buffer_load_dwordx4 v[48:51], v1, s[24:27], s65 offen sc1\n"
"v_mfma_f32_16x16x32_bf16 v[116:119], v[196:199], a[204:207], v[116:119]\n"
"v_add_f32 v123, v123, v143\n"
"v_add_f32 v128, v120, v144\n"
"v_mfma_f32_16x16x32_bf16 v[116:119], v[200:203], a[208:211], v[116:119]\n"
"v_add_f32 v129, v121, v145\n"
"v_add_f32 v130, v122, v146\n"
"v_mfma_f32_16x16x32_bf16 v[116:119], v[204:207], a[212:215], v[116:119]\n"
"v_add_f32 v131, v123, v147\n"
"v_mfma_f32_16x16x32_bf16 v[116:119], v[208:211], a[216:219], v[116:119]\n"
"buffer_load_dwordx4 v[52:55], v1, s[24:27], s65 offen offset:1024 sc1\n"
"v_mfma_f32_16x16x32_bf16 v[116:119], v[212:215], a[220:223], v[116:119]\n"
"v_mfma_f32_16x16x32_bf16 v[104:107], v[166:169], v[152:155], 0\n"
"v_mfma_f32_16x16x32_bf16 v[104:107], v[184:187], a[0:3], v[104:107]\n"
"v_mfma_f32_16x16x32_bf16 v[104:107], v[188:191], a[4:7], v[104:107]\n"
"s_add_u32 s64, s73, 0x12000\n"
"v_mfma_f32_16x16x32_bf16 v[104:107], v[192:195], a[8:11], v[104:107]\n"
"buffer_load_dwordx4 v[56:59], v1, s[24:27], s65 offen offset:2048 sc1\n"
"v_mfma_f32_16x16x32_bf16 v[104:107], v[196:199], a[12:15], v[104:107]\n"
"v_mfma_f32_16x16x32_bf16 v[104:107], v[200:203], a[16:19], v[104:107]\n"
"s_waitcnt vmcnt(9)\n"
"v_readfirstlane_b32 vcc_lo, v16\n"
"s_nop 0\n"
"s_cmp_ge_u32 vcc_lo, 1\n"
"s_cbranch_scc1 Lbm_d%=\n"
"Lbm%=:\n"
"s_mov_b64 exec, 1\n"
"v_mov_b32 v16, 0\n"
"buffer_atomic_add v16, off, s[24:27], s75 offset:516 sc0 sc1\n"
"s_mov_b64 exec, -1\n"
"s_waitcnt vmcnt(0)\n"
"v_readfirstlane_b32 vcc_lo, v16\n"
"s_nop 0\n"
"s_cmp_ge_u32 vcc_lo, 1\n"
"s_cbranch_scc1 Lbm_d%=\n"
"s_sleep 2\n"
"s_sub_u32 s60, s60, 1\n"
"s_cbranch_scc0 Lbm%=\n"
"s_mov_b32 s60, 0\n"
"Lbm_d%=:\n"
"buffer_load_dwordx4 v[156:159], v1, s[24:27], s64 offen sc1\n"
"v_mfma_f32_16x16x32_bf16 v[104:107], v[204:207], a[20:23], v[104:107]\n"
"ds_read_b128 v[152:155], v12 offset:2048\n"
"v_mfma_f32_16x16x32_bf16 v[104:107], v[208:211], a[24:27], v[104:107]\n"
"buffer_load_dwordx4 v[60:63], v1, s[24:27], s65 offen offset:3072 sc1\n"
"v_mfma_f32_16x16x32_bf16 v[104:107], v[212:215], a[28:31], v[104:107]\n"
"s_add_u32 s65, s65, 0x1000\n"
"v_mfma_f32_16x16x32_bf16 v[108:111], v[216:219], a[96:99], v[108:111]\n"
"v_mfma_f32_16x16x32_bf16 v[108:111], v[220:223], a[100:103], v[108:111]\n"
"v_mfma_f32_16x16x32_bf16 v[108:111], v[224:227], a[104:107], v[108:111]\n"
"buffer_load_dwordx4 v[64:67], v1, s[24:27], s65 offen sc1\n"
"v_mfma_f32_16x16x32_bf16 v[108:111], v[228:231], a[108:111], v[108:111]\n"
"v_mfma_f32_16x16x32_bf16 v[108:111], v[232:235], a[112:115], v[108:111]\n"
"buffer_load_dwordx4 v[160:163], v1, s[24:27], s64 offen offset:2048 sc1\n"
"v_mfma_f32_16x16x32_bf16 v[108:111], v[236:239], a[116:119], v[108:111]\n"
"s_waitcnt vmcnt(11)\n"
"v_mfma_f32_16x16x32_bf16 v[108:111], v[240:243], a[120:123], v[108:111]\n"
"buffer_load_dwordx4 v[68:71], v1, s[24:27], s65 offen offset:1024 sc1\n"
"v_mfma_f32_16x16x32_bf16 v[108:111], v[244:247], a[124:127], v[108:111]\n"
"v_mfma_f32_16x16x32_bf16 v[112:115], v[216:219], a[160:163], v[112:115]\n"
"v_mfma_f32_16x16x32_bf16 v[112:115], v[220:223], a[164:167], v[112:115]\n"
"v_mfma_f32_16x16x32_bf16 v[112:115], v[224:227], a[168:171], v[112:115]\n"
"buffer_load_dwordx4 v[72:75], v1, s[24:27], s65 offen offset:2048 sc1\n"
"v_mfma_f32_16x16x32_bf16 v[112:115], v[228:231], a[172:175], v[112:115]\n"
"v_mfma_f32_16x16x32_bf16 v[112:115], v[232:235], a[176:179], v[112:115]\n"
"v_mfma_f32_16x16x32_bf16 v[112:115], v[236:239], a[180:183], v[112:115]\n"
"v_mfma_f32_16x16x32_bf16 v[112:115], v[240:243], a[184:187], v[112:115]\n"
"buffer_load_dwordx4 v[76:79], v1, s[24:27], s65 offen offset:3072 sc1\n"
"v_mfma_f32_16x16x32_bf16 v[112:115], v[244:247], a[188:191], v[112:115]\n"
"ds_write_b128 v170, v[108:111]\n"
"s_add_u32 s65, s65, 0x1000\n"
"v_mfma_f32_16x16x32_bf16 v[116:119], v[216:219], a[224:227], v[116:119]\n"
"v_mfma_f32_16x16x32_bf16 v[116:119], v[220:223], a[228:231], v[116:119]\n"
"v_mfma_f32_16x16x32_bf16 v[116:119], v[224:227], a[232:235], v[116:119]\n"
"buffer_load_dwordx4 v[80:83], v1, s[24:27], s65 offen sc1\n"
"v_mfma_f32_16x16x32_bf16 v[116:119], v[228:231], a[236:239], v[116:119]\n"
"v_mfma_f32_16x16x32_bf16 v[116:119], v[232:235], a[240:243], v[116:119]\n"
"v_mfma_f32_16x16x32_bf16 v[116:119], v[236:239], a[244:247], v[116:119]\n"
"v_mfma_f32_16x16x32_bf16 v[116:119], v[240:243], a[248:251], v[116:119]\n"
"buffer_load_dwordx4 v[84:87], v1, s[24:27], s65 offen offset:1024 sc1\n"
"v_mfma_f32_16x16x32_bf16 v[116:119], v[244:247], a[252:255], v[116:119]\n"
"ds_write_b128 v171, v[112:115] offset:1024\n"
"v_mfma_f32_16x16x32_bf16 v[104:107], v[216:219], a[32:35], v[104:107]\n"
"v_mfma_f32_16x16x32_bf16 v[104:107], v[220:223], a[36:39], v[104:107]\n"
"v_mfma_f32_16x16x32_bf16 v[104:107], v[224:227], a[40:43], v[104:107]\n"
"buffer_load_dwordx4 v[88:91], v1, s[24:27], s65 offen offset:2048 sc1\n"
"v_mfma_f32_16x16x32_bf16 v[104:107], v[228:231], a[44:47], v[104:107]\n"
"v_mfma_f32_16x16x32_bf16 v[104:107], v[232:235], a[48:51], v[104:107]\n"
"v_mfma_f32_16x16x32_bf16 v[104:107], v[236:239], a[52:55], v[104:107]\n"
"v_mfma_f32_16x16x32_bf16 v[104:107], v[240:243], a[56:59], v[104:107]\n"
"ds_write_b128 v172, v[116:119] offset:2048\n"
"buffer_load_dwordx4 v[92:95], v1, s[24:27], s65 offen offset:3072 sc1\n"
"v_mfma_f32_16x16x32_bf16 v[120:123], v[244:247], a[60:63], v[104:107]\n"
"s_add_u32 s65, s65, 0xd000\n"
"s_waitcnt vmcnt(16)\n"
"v_mfma_f32_16x16x32_bf16 v[108:111], v[32:35], a[64:67], 0\n"
"v_mfma_f32_16x16x32_bf16 v[108:111], v[36:39], a[68:71], v[108:111]\n"
"s_waitcnt vmcnt(14)\n"
"v_mfma_f32_16x16x32_bf16 v[108:111], v[40:43], a[72:75], v[108:111]\n"
"buffer_load_dwordx4 v[184:187], v1, s[24:27], s65 offen sc1\n"
"v_mfma_f32_16x16x32_bf16 v[108:111], v[44:47], a[76:79], v[108:111]\n"
"s_waitcnt lgkmcnt(0)\n"
"s_barrier\n"
"s_waitcnt vmcnt(13)\n"
"v_mfma_f32_16x16x32_bf16 v[108:111], v[48:51], a[80:83], v[108:111]\n"
"ds_read_b128 v[136:139], v10\n"
"v_mfma_f32_16x16x32_bf16 v[108:111], v[52:55], a[84:87], v[108:111]\n"
"ds_read_b128 v[140:143], v10 offset:1024\n"
"s_waitcnt vmcnt(10)\n"
"v_mfma_f32_16x16x32_bf16 v[108:111], v[56:59], a[88:91], v[108:111]\n"
"buffer_load_dwordx4 v[188:191], v1, s[24:27], s65 offen offset:1024 sc1\n"
"v_mfma_f32_16x16x32_bf16 v[108:111], v[60:63], a[92:95], v[108:111]\n"
"ds_read_b128 v[144:147], v10 offset:2048\n"
"v_mfma_f32_16x16x32_bf16 v[124:127], v[22:25], v[156:159], v[124:127]\n"
"v_mfma_f32_16x16x32_bf16 v[128:131], v[20:23], v[156:159], v[128:131]\n"
"v_mfma_f32_16x16x32_bf16 v[112:115], v[32:35], a[128:131], 0\n"
"v_mfma_f32_16x16x32_bf16 v[112:115], v[36:39], a[132:135], v[112:115]\n"
"s_mov_b64 exec, 1\n"
"v_mov_b32 v17, 0\n"
"buffer_atomic_add v17, off, s[24:27], s69 offset:384 sc0 sc1\n"
"s_mov_b64 exec, -1\n"
"v_mfma_f32_16x16x32_bf16 v[112:115], v[40:43], a[136:139], v[112:115]\n"
"buffer_load_dwordx4 v[192:195], v1, s[24:27], s65 offen offset:2048 sc1\n"
"v_mfma_f32_16x16x32_bf16 v[112:115], v[44:47], a[140:143], v[112:115]\n"
"v_lshrrev_b32 v18, 1, v1\n"
"v_and_b32 v18, 0x100, v18\n"
"v_mfma_f32_16x16x32_bf16 v[112:115], v[48:51], a[144:147], v[112:115]\n"
"v_and_b32 v19, 0xff, v1\n"
"v_add_u32 v18, v18, v19\n"
"v_mfma_f32_16x16x32_bf16 v[112:115], v[52:55], a[148:151], v[112:115]\n"
"s_waitcnt lgkmcnt(0)\n"
"v_add_f32 v120, v120, v136\n"
"v_cvt_pk_bf16_f32 v132, v124, v125\n"
"v_mfma_f32_16x16x32_bf16 v[112:115], v[56:59], a[152:155], v[112:115]\n"
"buffer_load_dwordx4 v[196:199], v1, s[24:27], s65 offen offset:3072 sc1\n"
"v_mfma_f32_16x16x32_bf16 v[112:115], v[60:63], a[156:159], v[112:115]\n"
"v_add_f32 v121, v121, v137\n"
"v_add_f32 v122, v122, v138\n"
"v_cvt_pk_bf16_f32 v133, v126, v127\n"
"s_add_u32 s65, s65, 0x1000\n"
"v_mfma_f32_16x16x32_bf16 v[116:119], v[32:35], a[192:195], 0\n"
"v_add_f32 v123, v123, v139\n"
"v_add_f32 v120, v120, v140\n"
"v_cvt_pk_bf16_f32 v134, v128, v129\n"
"v_mfma_f32_16x16x32_bf16 v[116:119], v[36:39], a[196:199], v[116:119]\n"
"v_add_f32 v121, v121, v141\n"
"v_add_f32 v122, v122, v142\n"
"v_cvt_pk_bf16_f32 v135, v130, v131\n"
"v_mfma_f32_16x16x32_bf16 v[116:119], v[40:43], a[200:203], v[116:119]\n"
"buffer_load_dwordx4 v[200:203], v1, s[24:27], s65 offen sc1\n"
"v_mfma_f32_16x16x32_bf16 v[116:119], v[44:47], a[204:207], v[116:119]\n"
"v_add_f32 v123, v123, v143\n"
"v_add_f32 v120, v120, v144\n"
"buffer_store_dwordx4 v[132:135], v1, s[24:27], s73 offen sc1\n"
"v_mfma_f32_16x16x32_bf16 v[116:119], v[48:51], a[208:211], v[116:119]\n"
"v_add_f32 v121, v121, v145\n"
"v_add_f32 v122, v122, v146\n"
"v_mfma_f32_16x16x32_bf16 v[116:119], v[52:55], a[212:215], v[116:119]\n"
"v_add_f32 v123, v123, v147\n"
"v_mfma_f32_16x16x32_bf16 v[116:119], v[56:59], a[216:219], v[116:119]\n"
"s_waitcnt vmcnt(14)\n"
"buffer_load_dwordx4 v[204:207], v1, s[24:27], s65 offen offset:1024 sc1\n"
"v_mfma_f32_16x16x32_bf16 v[116:119], v[60:63], a[220:223], v[116:119]\n"
"v_cvt_pk_bf16_f32 v148, v120, v121\n"
"v_mfma_f32_16x16x32_bf16 v[104:107], v[22:25], v[152:155], 0\n"
"v_mfma_f32_16x16x32_bf16 v[104:107], v[22:25], v[160:163], v[104:107]\n"
"v_mfma_f32_16x16x32_bf16 v[104:107], v[32:35], a[0:3], v[104:107]\n"
"v_cvt_pk_bf16_f32 v149, v122, v123\n"
"v_mfma_f32_16x16x32_bf16 v[104:107], v[36:39], a[4:7], v[104:107]\n"
"v_mov_b32 v150, v148\n"
"v_mov_b32 v151, v149\n"
"v_mfma_f32_16x16x32_bf16 v[104:107], v[40:43], a[8:11], v[104:107]\n"
"buffer_load_dwordx4 v[208:211], v1, s[24:27], s65 offen offset:2048 sc1\n"
"v_mfma_f32_16x16x32_bf16 v[104:107], v[44:47], a[12:15], v[104:107]\n"
"v_permlane16_swap_b32 v148, v150\n"
"v_permlane16_swap_b32 v149, v151\n"
"v_mfma_f32_16x16x32_bf16 v[104:107], v[48:51], a[16:19], v[104:107]\n"
"s_mov_b32 exec_lo, 0xffff\n"
"s_mov_b32 exec_hi, 0xffff\n"
"buffer_store_dwordx4 v[148:151], v18, s[24:27], s66 offen sc1\n"
"s_mov_b64 exec, -1\n"
"v_mfma_f32_16x16x32_bf16 v[104:107], v[52:55], a[20:23], v[104:107]\n"
"v_mfma_f32_16x16x32_bf16 v[104:107], v[56:59], a[24:27], v[104:107]\n"
"buffer_load_dwordx4 v[212:215], v1, s[24:27], s65 offen offset:3072 sc1\n"
"v_mfma_f32_16x16x32_bf16 v[104:107], v[60:63], a[28:31], v[104:107]\n"
"s_add_u32 s65, s65, 0x1000\n"
"s_waitcnt vmcnt(17)\n"
"v_mfma_f32_16x16x32_bf16 v[108:111], v[64:67], a[96:99], v[108:111]\n"
"v_mfma_f32_16x16x32_bf16 v[108:111], v[68:71], a[100:103], v[108:111]\n"
"s_waitcnt vmcnt(15)\n"
"v_mfma_f32_16x16x32_bf16 v[108:111], v[72:75], a[104:107], v[108:111]\n"
"buffer_load_dwordx4 v[216:219], v1, s[24:27], s65 offen sc1\n"
"v_mfma_f32_16x16x32_bf16 v[108:111], v[76:79], a[108:111], v[108:111]\n"
"s_waitcnt vmcnt(14)\n"
"v_mfma_f32_16x16x32_bf16 v[108:111], v[80:83], a[112:115], v[108:111]\n"
"v_mfma_f32_16x16x32_bf16 v[108:111], v[84:87], a[116:119], v[108:111]\n"
"s_waitcnt vmcnt(12)\n"
"v_mfma_f32_16x16x32_bf16 v[108:111], v[88:91], a[120:123], v[108:111]\n"
"buffer_load_dwordx4 v[220:223], v1, s[24:27], s65 offen offset:1024 sc1\n"
"v_mfma_f32_16x16x32_bf16 v[108:111], v[92:95], a[124:127], v[108:111]\n"
"v_mfma_f32_16x16x32_bf16 v[112:115], v[64:67], a[160:163], v[112:115]\n"
"v_mfma_f32_16x16x32_bf16 v[112:115], v[68:71], a[164:167], v[112:115]\n"
"v_mfma_f32_16x16x32_bf16 v[112:115], v[72:75], a[168:171], v[112:115]\n"
"buffer_load_dwordx4 v[224:227], v1, s[24:27], s65 offen offset:2048 sc1\n"
"v_mfma_f32_16x16x32_bf16 v[112:115], v[76:79], a[172:175], v[112:115]\n"
"v_mfma_f32_16x16x32_bf16 v[112:115], v[80:83], a[176:179], v[112:115]\n"
"v_mfma_f32_16x16x32_bf16 v[112:115], v[84:87], a[180:183], v[112:115]\n"
"v_mfma_f32_16x16x32_bf16 v[112:115], v[88:91], a[184:187], v[112:115]\n"
"buffer_load_dwordx4 v[228:231], v1, s[24:27], s65 offen offset:3072 sc1\n"
"v_mfma_f32_16x16x32_bf16 v[112:115], v[92:95], a[188:191], v[112:115]\n"
"ds_write_b128 v170, v[108:111] offset:12288\n"
"s_add_u32 s65, s65, 0x1000\n"
"v_mfma_f32_16x16x32_bf16 v[116:119], v[64:67], a[224:227], v[116:119]\n"
"v_mfma_f32_16x16x32_bf16 v[116:119], v[68:71], a[228:231], v[116:119]\n"
"v_mfma_f32_16x16x32_bf16 v[116:119], v[72:75], a[232:235], v[116:119]\n"
"buffer_load_dwordx4 v[232:235], v1, s[24:27], s65 offen sc1\n"
"v_mfma_f32_16x16x32_bf16 v[116:119], v[76:79], a[236:239], v[116:119]\n"
"v_mfma_f32_16x16x32_bf16 v[116:119], v[80:83], a[240:243], v[116:119]\n"
"v_mfma_f32_16x16x32_bf16 v[116:119], v[84:87], a[244:247], v[116:119]\n"
"v_mfma_f32_16x16x32_bf16 v[116:119], v[88:91], a[248:251], v[116:119]\n"
"buffer_load_dwordx4 v[236:239], v1, s[24:27], s65 offen offset:1024 sc1\n"
"v_mfma_f32_16x16x32_bf16 v[116:119], v[92:95], a[252:255], v[116:119]\n"
"ds_write_b128 v171, v[112:115] offset:13312\n"
"v_mfma_f32_16x16x32_bf16 v[104:107], v[64:67], a[32:35], v[104:107]\n"
"v_mfma_f32_16x16x32_bf16 v[104:107], v[68:71], a[36:39], v[104:107]\n"
"v_mfma_f32_16x16x32_bf16 v[104:107], v[72:75], a[40:43], v[104:107]\n"
"buffer_load_dwordx4 v[240:243], v1, s[24:27], s65 offen offset:2048 sc1\n"
"v_mfma_f32_16x16x32_bf16 v[104:107], v[76:79], a[44:47], v[104:107]\n"
"v_mfma_f32_16x16x32_bf16 v[104:107], v[80:83], a[48:51], v[104:107]\n"
"v_mfma_f32_16x16x32_bf16 v[104:107], v[84:87], a[52:55], v[104:107]\n"
"v_mfma_f32_16x16x32_bf16 v[104:107], v[88:91], a[56:59], v[104:107]\n"
"ds_write_b128 v172, v[116:119] offset:14336\n"
"buffer_load_dwordx4 v[244:247], v1, s[24:27], s65 offen offset:3072 sc1\n"
"v_mfma_f32_16x16x32_bf16 v[120:123], v[92:95], a[60:63], v[104:107]\n"
"s_add_u32 s65, s65, 0xd000\n"
"s_waitcnt vmcnt(16)\n"
"v_readfirstlane_b32 vcc_lo, v17\n"
"s_nop 0\n"
"s_cmp_ge_u32 vcc_lo, 32\n"
"s_cbranch_scc1 Lpt_d%=\n"
"Lpt%=:\n"
"s_mov_b64 exec, 1\n"
"v_mov_b32 v17, 0\n"
"buffer_atomic_add v17, off, s[24:27], s69 offset:384 sc0 sc1\n"
"s_mov_b64 exec, -1\n"
"s_waitcnt vmcnt(0)\n"
"v_readfirstlane_b32 vcc_lo, v17\n"
"s_nop 0\n"
"s_cmp_ge_u32 vcc_lo, 32\n"
"s_cbranch_scc1 Lpt_d%=\n"
"s_sleep 2\n"
"s_sub_u32 s60, s60, 1\n"
"s_cbranch_scc0 Lpt%=\n"
"s_mov_b32 s60, 0\n"
"Lpt_d%=:\n"
"s_cmp_lg_u32 s59, 0\n"
"s_cbranch_scc1 Lcpf%=\n"
"s_and_b32 s62, s57, 7\n"
"s_and_b32 s63, s58, 3\n"
"s_lshl2_add_u32 s62, s62, s63\n"
"s_lshl2_add_u32 s62, s62, s56\n"
"s_lshl_b32 s62, s62, 9\n"
"v_lshl_add_u32 v18, v0, 3, s62\n"
"buffer_load_dwordx2 v[174:175], v18, s[40:43], 0 offen\n"
"Lcpf%=:\n"
"v_mfma_f32_16x16x32_bf16 v[108:111], v[184:187], a[64:67], 0\n"
"v_mfma_f32_16x16x32_bf16 v[108:111], v[188:191], a[68:71], v[108:111]\n"
"s_waitcnt vmcnt(14)\n"
"v_mfma_f32_16x16x32_bf16 v[108:111], v[192:195], a[72:75], v[108:111]\n"
"buffer_load_dwordx4 v[32:35], v1, s[24:27], s76 offen sc1\n"
"v_mfma_f32_16x16x32_bf16 v[108:111], v[196:199], a[76:79], v[108:111]\n"
"s_waitcnt lgkmcnt(0)\n"
"s_barrier\n"
"s_waitcnt vmcnt(12)\n"
"v_mfma_f32_16x16x32_bf16 v[108:111], v[200:203], a[80:83], v[108:111]\n"
"ds_read_b128 v[136:139], v10 offset:12288\n"
"v_mfma_f32_16x16x32_bf16 v[108:111], v[204:207], a[84:87], v[108:111]\n"
"ds_read_b128 v[140:143], v10 offset:13312\n"
"s_waitcnt vmcnt(10)\n"
"s_bfm_b64 exec, 8, 0\n"
"buffer_atomic_add v27, v31, s[24:27], s21 offen offset:512 sc1\n"
"s_mov_b64 exec, -1\n"
"s_waitcnt vmcnt(10)\n"
"v_mfma_f32_16x16x32_bf16 v[108:111], v[208:211], a[88:91], v[108:111]\n"
"buffer_load_dwordx4 v[36:39], v1, s[24:27], s76 offen offset:1024 sc1\n"
"v_mfma_f32_16x16x32_bf16 v[108:111], v[212:215], a[92:95], v[108:111]\n"
"ds_read_b128 v[144:147], v10 offset:14336\n"
"v_mfma_f32_16x16x32_bf16 v[112:115], v[184:187], a[128:131], 0\n"
"v_mfma_f32_16x16x32_bf16 v[112:115], v[188:191], a[132:135], v[112:115]\n"
"v_mfma_f32_16x16x32_bf16 v[112:115], v[192:195], a[136:139], v[112:115]\n"
"buffer_load_dwordx4 v[40:43], v1, s[24:27], s76 offen offset:2048 sc1\n"
"v_mfma_f32_16x16x32_bf16 v[112:115], v[196:199], a[140:143], v[112:115]\n"
"s_add_u32 s76, s76, 0x3000\n"
"v_mfma_f32_16x16x32_bf16 v[112:115], v[200:203], a[144:147], v[112:115]\n"
"v_mfma_f32_16x16x32_bf16 v[112:115], v[204:207], a[148:151], v[112:115]\n"
"s_waitcnt lgkmcnt(0)\n"
"v_add_f32 v120, v120, v136\n"
"v_mfma_f32_16x16x32_bf16 v[112:115], v[208:211], a[152:155], v[112:115]\n"
"buffer_load_dwordx4 v[44:47], v1, s[24:27], s76 offen sc1\n"
"v_mfma_f32_16x16x32_bf16 v[112:115], v[212:215], a[156:159], v[112:115]\n"
"v_add_f32 v121, v121, v137\n"
"v_add_f32 v122, v122, v138\n"
"v_mfma_f32_16x16x32_bf16 v[116:119], v[184:187], a[192:195], 0\n"
"v_add_f32 v123, v123, v139\n"
"v_add_f32 v120, v120, v140\n"
"v_mfma_f32_16x16x32_bf16 v[116:119], v[188:191], a[196:199], v[116:119]\n"
"v_add_f32 v121, v121, v141\n"
"v_add_f32 v122, v122, v142\n"
"v_mfma_f32_16x16x32_bf16 v[116:119], v[192:195], a[200:203], v[116:119]\n"
"buffer_load_dwordx4 v[48:51], v1, s[24:27], s76 offen offset:1024 sc1\n"
"v_mfma_f32_16x16x32_bf16 v[116:119], v[196:199], a[204:207], v[116:119]\n"
"v_add_f32 v123, v123, v143\n"
"v_add_f32 v120, v120, v144\n"
"v_mfma_f32_16x16x32_bf16 v[116:119], v[200:203], a[208:211], v[116:119]\n"
"v_add_f32 v121, v121, v145\n"
"v_add_f32 v122, v122, v146\n"
"v_mfma_f32_16x16x32_bf16 v[116:119], v[204:207], a[212:215], v[116:119]\n"
"v_add_f32 v123, v123, v147\n"
"v_mfma_f32_16x16x32_bf16 v[116:119], v[208:211], a[216:219], v[116:119]\n"
"buffer_load_dwordx4 v[52:55], v1, s[24:27], s76 offen offset:2048 sc1\n"
"v_mfma_f32_16x16x32_bf16 v[116:119], v[212:215], a[220:223], v[116:119]\n"
"v_cvt_pk_bf16_f32 v148, v120, v121\n"
"v_mfma_f32_16x16x32_bf16 v[104:107], v[20:23], v[152:155], 0\n"
"v_mfma_f32_16x16x32_bf16 v[104:107], v[20:23], v[160:163], v[104:107]\n"
"s_add_u32 s76, s76, 0x3000\n"
"v_mfma_f32_16x16x32_bf16 v[104:107], v[184:187], a[0:3], v[104:107]\n"
"v_cvt_pk_bf16_f32 v149, v122, v123\n"
"v_mfma_f32_16x16x32_bf16 v[104:107], v[188:191], a[4:7], v[104:107]\n"
"v_mfma_f32_16x16x32_bf16 v[104:107], v[192:195], a[8:11], v[104:107]\n"
"buffer_load_dwordx4 v[56:59], v1, s[24:27], s76 offen sc1\n"
"v_mfma_f32_16x16x32_bf16 v[104:107], v[196:199], a[12:15], v[104:107]\n"
"v_mfma_f32_16x16x32_bf16 v[104:107], v[200:203], a[16:19], v[104:107]\n"
"v_mfma_f32_16x16x32_bf16 v[104:107], v[204:207], a[20:23], v[104:107]\n"
"v_mfma_f32_16x16x32_bf16 v[104:107], v[208:211], a[24:27], v[104:107]\n"
"buffer_load_dwordx4 v[60:63], v1, s[24:27], s76 offen offset:1024 sc1\n"
"v_mfma_f32_16x16x32_bf16 v[104:107], v[212:215], a[28:31], v[104:107]\n"
"s_waitcnt vmcnt(15)\n"
"v_mfma_f32_16x16x32_bf16 v[108:111], v[216:219], a[96:99], v[108:111]\n"
"v_mfma_f32_16x16x32_bf16 v[108:111], v[220:223], a[100:103], v[108:111]\n"
"s_waitcnt vmcnt(13)\n"
"v_mfma_f32_16x16x32_bf16 v[108:111], v[224:227], a[104:107], v[108:111]\n"
"buffer_load_dwordx4 v[64:67], v1, s[24:27], s76 offen offset:2048 sc1\n"
"v_mfma_f32_16x16x32_bf16 v[108:111], v[228:231], a[108:111], v[108:111]\n"
"s_add_u32 s76, s76, 0xc000\n"
"s_waitcnt vmcnt(12)\n"
"v_mfma_f32_16x16x32_bf16 v[108:111], v[232:235], a[112:115], v[108:111]\n"
"v_mfma_f32_16x16x32_bf16 v[108:111], v[236:239], a[116:119], v[108:111]\n"
"s_waitcnt vmcnt(10)\n"
"v_mfma_f32_16x16x32_bf16 v[108:111], v[240:243], a[120:123], v[108:111]\n"
"buffer_load_dwordx4 v[68:71], v1, s[24:27], s76 offen sc1\n"
"v_mfma_f32_16x16x32_bf16 v[108:111], v[244:247], a[124:127], v[108:111]\n"
"v_mfma_f32_16x16x32_bf16 v[112:115], v[216:219], a[160:163], v[112:115]\n"
"v_mfma_f32_16x16x32_bf16 v[112:115], v[220:223], a[164:167], v[112:115]\n"
"v_mfma_f32_16x16x32_bf16 v[112:115], v[224:227], a[168:171], v[112:115]\n"
"buffer_load_dwordx4 v[72:75], v1, s[24:27], s76 offen offset:1024 sc1\n"
"v_mfma_f32_16x16x32_bf16 v[112:115], v[228:231], a[172:175], v[112:115]\n"
"v_mfma_f32_16x16x32_bf16 v[112:115], v[232:235], a[176:179], v[112:115]\n"
"v_mfma_f32_16x16x32_bf16 v[112:115], v[236:239], a[180:183], v[112:115]\n"
"v_mfma_f32_16x16x32_bf16 v[112:115], v[240:243], a[184:187], v[112:115]\n"
"buffer_load_dwordx4 v[76:79], v1, s[24:27], s76 offen offset:2048 sc1\n"
"v_mfma_f32_16x16x32_bf16 v[112:115], v[244:247], a[188:191], v[112:115]\n"
"ds_write_b128 v170, v[108:111]\n"
"s_add_u32 s76, s76, 0x3000\n"
"v_mfma_f32_16x16x32_bf16 v[116:119], v[216:219], a[224:227], v[116:119]\n"
"v_mfma_f32_16x16x32_bf16 v[116:119], v[220:223], a[228:231], v[116:119]\n"
"s_mov_b64 exec, 1\n"
"v_mov_b32 v16, 0\n"
"buffer_atomic_add v16, off, s[24:27], s75 offset:512 sc0 sc1\n"
"s_mov_b64 exec, -1\n"
"v_mfma_f32_16x16x32_bf16 v[116:119], v[224:227], a[232:235], v[116:119]\n"
"buffer_load_dwordx4 v[80:83], v1, s[24:27], s76 offen sc1\n"
"v_mfma_f32_16x16x32_bf16 v[116:119], v[228:231], a[236:239], v[116:119]\n"
"v_mfma_f32_16x16x32_bf16 v[116:119], v[232:235], a[240:243], v[116:119]\n"
"v_mfma_f32_16x16x32_bf16 v[116:119], v[236:239], a[244:247], v[116:119]\n"
"v_mfma_f32_16x16x32_bf16 v[116:119], v[240:243], a[248:251], v[116:119]\n"
"buffer_load_dwordx4 v[84:87], v1, s[24:27], s76 offen offset:1024 sc1\n"
"v_mfma_f32_16x16x32_bf16 v[116:119], v[244:247], a[252:255], v[116:119]\n"
"ds_write_b128 v171, v[112:115] offset:1024\n"
"v_mfma_f32_16x16x32_bf16 v[104:107], v[216:219], a[32:35], v[104:107]\n"
"v_mfma_f32_16x16x32_bf16 v[104:107], v[220:223], a[36:39], v[104:107]\n"
"v_mfma_f32_16x16x32_bf16 v[104:107], v[224:227], a[40:43], v[104:107]\n"
"buffer_load_dwordx4 v[88:91], v1, s[24:27], s76 offen offset:2048 sc1\n"
"v_mfma_f32_16x16x32_bf16 v[104:107], v[228:231], a[44:47], v[104:107]\n"
"s_add_u32 s76, s76, 0x3000\n"
"v_mfma_f32_16x16x32_bf16 v[104:107], v[232:235], a[48:51], v[104:107]\n"
"v_mfma_f32_16x16x32_bf16 v[104:107], v[236:239], a[52:55], v[104:107]\n"
"v_mfma_f32_16x16x32_bf16 v[104:107], v[240:243], a[56:59], v[104:107]\n"
"ds_write_b128 v172, v[116:119] offset:2048\n"
"v_mfma_f32_16x16x32_bf16 v[120:123], v[244:247], a[60:63], v[104:107]\n"
"s_waitcnt lgkmcnt(0)\n"
"s_barrier\n"
"ds_read_b128 v[136:139], v10\n"
"ds_read_b128 v[140:143], v10 offset:1024\n"
"ds_read_b128 v[144:147], v10 offset:2048\n"
"s_waitcnt lgkmcnt(0)\n"
"s_nop 1\n"
"v_add_f32 v120, v120, v136\n"
"v_add_f32 v121, v121, v137\n"
"v_add_f32 v122, v122, v138\n"
"v_add_f32 v123, v123, v139\n"
"v_add_f32 v120, v120, v140\n"
"v_add_f32 v121, v121, v141\n"
"v_add_f32 v122, v122, v142\n"
"v_add_f32 v123, v123, v143\n"
"v_add_f32 v120, v120, v144\n"
"v_add_f32 v121, v121, v145\n"
"v_add_f32 v122, v122, v146\n"
"v_add_f32 v123, v123, v147\n"
"v_cvt_pk_bf16_f32 v150, v120, v121\n"
"v_cvt_pk_bf16_f32 v151, v122, v123\n"
"buffer_store_dwordx4 v[148:151], v1, s[24:27], s73 offen offset:2048 sc1\n"
"v_mov_b32 v156, 0x3f803f80\n"
"v_mov_b32 v157, 0x3f803f80\n"
"v_mov_b32 v158, 0x3f803f80\n"
"v_mov_b32 v159, 0x3f803f80\n"
"s_waitcnt vmcnt(4)\n"
"v_readfirstlane_b32 vcc_lo, v16\n"
"s_nop 0\n"
"s_cmp_ge_u32 vcc_lo, 8\n"
"s_cbranch_scc1 Lbe_d%=\n"
"Lbe%=:\n"
"s_mov_b64 exec, 1\n"
"v_mov_b32 v16, 0\n"
"buffer_atomic_add v16, off, s[24:27], s75 offset:512 sc0 sc1\n"
"s_mov_b64 exec, -1\n"
"s_waitcnt vmcnt(0)\n"
"v_readfirstlane_b32 vcc_lo, v16\n"
"s_nop 0\n"
"s_cmp_ge_u32 vcc_lo, 8\n"
"s_cbranch_scc1 Lbe_d%=\n"
"s_sleep 2\n"
"s_sub_u32 s60, s60, 1\n"
"s_cbranch_scc0 Lbe%=\n"
"s_mov_b32 s60, 0\n"
"Lbe_d%=:\n"
"s_add_u32 s92, s81, 0x1000\n"
"s_add_u32 s93, s92, 0x12000\n"
"buffer_load_dwordx4 v[224:227], v1, s[24:27], s92 offen sc1\n"
"buffer_load_dwordx4 v[228:231], v1, s[24:27], s93 offen sc1\n"
"s_add_u32 s92, s81, 0x3400\n"
"s_add_u32 s93, s92, 0x12000\n"
"buffer_load_dwordx4 v[232:235], v1, s[24:27], s92 offen sc1\n"
"buffer_load_dwordx4 v[236:239], v1, s[24:27], s93 offen sc1\n"
"s_add_u32 m0, s61, 0\n"
"s_add_u32 s92, s81, 0\n"
"buffer_load_dwordx4 v1, s[24:27], s92 offen sc1 lds\n"
"s_add_u32 m0, s61, 0x400\n"
"s_add_u32 s92, s81, 0x400\n"
"buffer_load_dwordx4 v1, s[24:27], s92 offen sc1 lds\n"
"s_add_u32 m0, s61, 0x1400\n"
"s_add_u32 s92, s81, 0x1400\n"
"buffer_load_dwordx4 v1, s[24:27], s92 offen sc1 lds\n"
"s_add_u32 m0, s61, 0x1800\n"
"s_add_u32 s92, s81, 0x1800\n"
"buffer_load_dwordx4 v1, s[24:27], s92 offen sc1 lds\n"
"s_add_u32 m0, s61, 0x2400\n"
"s_add_u32 s92, s81, 0x2400\n"
"buffer_load_dwordx4 v1, s[24:27], s92 offen sc1 lds\n"
"s_add_u32 m0, s61, 0x2800\n"
"s_add_u32 s92, s81, 0x2800\n"
"buffer_load_dwordx4 v1, s[24:27], s92 offen sc1 lds\n"
"s_add_u32 m0, s61, 0x3800\n"
"s_add_u32 s92, s81, 0x3800\n"
"buffer_load_dwordx4 v1, s[24:27], s92 offen sc1 lds\n"
"s_add_u32 m0, s61, 0x3c00\n"
"s_add_u32 s92, s81, 0x3c00\n"
"buffer_load_dwordx4 v1, s[24:27], s92 offen sc1 lds\n"
"s_cmp_lg_u32 s59, 0\n"
"s_cbranch_scc1 Lcs0%=\n"
"v_lshrrev_b32 v18, 2, v0\n"
"v_and_b32 v19, 48, v1\n"
"v_lshl_add_u32 v18, v18, 8, v19\n"
"s_lshl_b32 s62, s58, 9\n"
"s_add_u32 s62, s62, s56\n"
"s_lshl_b32 s62, s62, 6\n"
"s_add_u32 s62, s62, 0x4660000\n"
"s_bfm_b64 exec, 36, 0\n"
"buffer_load_dwordx4 v[160:163], v18, s[24:27], s62 offen sc1\n"
"s_add_u32 s62, s62, 0x1200\n"
"buffer_load_dwordx4 v[164:167], v18, s[24:27], s62 offen sc1\n"
"s_add_u32 s62, s62, 0x1200\n"
"buffer_load_dwordx4 v[168:171], v18, s[24:27], s62 offen sc1\n"
"s_add_u32 s62, s62, 0x1200\n"
"buffer_load_dwordx4 v[172:175], v18, s[24:27], s62 offen sc1\n"
"s_add_u32 s62, s62, 0x1200\n"
"buffer_load_dwordx4 v[176:179], v18, s[24:27], s62 offen sc1\n"
"s_add_u32 s62, s62, 0x1200\n"
"buffer_load_dwordx4 v[180:183], v18, s[24:27], s62 offen sc1\n"
"s_mov_b64 exec, -1\n"
"Lcs0%=:\n"
"s_and_b32 s62, s56, 1\n"
"s_lshl_b32 s62, s62, 3\n"
"s_lshr_b32 s63, s56, 1\n"
"s_lshl_b32 s63, s63, 10\n"
"s_add_u32 s62, s62, s63\n"
"v_add_u32 v10, s62, v3\n"
"s_mul_i32 s62, s56, 0x240\n"
"s_add_u32 s62, s62, 0x27200\n"
"v_and_b32 v11, 48, v0\n"
"v_add_u32 v11, s62, v11\n"
"v_mov_b32 v248, 0\n"
"v_mov_b32 v249, 0\n"
"v_mov_b32 v250, 0\n"
"v_mov_b32 v251, 0\n"
"v_mov_b32 v252, 0\n"
"v_mov_b32 v253, 0\n"
"v_mov_b32 v254, 0\n"
"v_mov_b32 v255, 0\n"
"v_and_b32 v18, 1, v0\n"
"v_lshlrev_b32 v18, 4, v18\n"
"v_mov_b32 v19, 0x3f80\n"
"v_lshlrev_b32 v18, v18, v19\n"
"s_mov_b32 exec_lo, 0x3000003\n"
"s_mov_b32 exec_hi, 0\n"
"v_mov_b32 v248, v18\n"
"s_mov_b32 exec_lo, 0xc00000c\n"
"v_mov_b32 v249, v18\n"
"s_mov_b32 exec_lo, 0x30000030\n"
"v_mov_b32 v250, v18\n"
"s_mov_b32 exec_lo, 0xc00000c0\n"
"v_mov_b32 v251, v18\n"
"s_mov_b32 exec_hi, 0x3000003\n"
"s_mov_b32 exec_lo, 0\n"
"v_mov_b32 v252, v18\n"
"s_mov_b32 exec_hi, 0xc00000c\n"
"v_mov_b32 v253, v18\n"
"s_mov_b32 exec_hi, 0x30000030\n"
"v_mov_b32 v254, v18\n"
"s_mov_b32 exec_hi, 0xc00000c0\n"
"v_mov_b32 v255, v18\n"
"s_mov_b64 exec, -1\n"
"s_waitcnt vmcnt(12)\n"
"s_bfm_b64 exec, 8, 0\n"
"buffer_atomic_add v27, v31, s[24:27], s21 offen offset:520 sc1\n"
"s_mov_b64 exec, -1\n"
"s_waitcnt vmcnt(9)\n"
"v_mfma_f32_16x16x32_bf16 v[120:123], v[248:251], v[224:227], 0\n"
"v_mfma_f32_16x16x32_bf16 v[124:127], v[252:255], v[224:227], 0\n"
"v_mfma_f32_16x16x32_bf16 v[128:131], v[248:251], v[232:235], 0\n"
"v_mfma_f32_16x16x32_bf16 v[132:135], v[252:255], v[232:235], 0\n"
"v_mfma_f32_16x16x32_bf16 v[120:123], v[248:251], v[228:231], v[120:123]\n"
"v_mfma_f32_16x16x32_bf16 v[124:127], v[252:255], v[228:231], v[124:127]\n"
"v_mfma_f32_16x16x32_bf16 v[128:131], v[248:251], v[236:239], v[128:131]\n"
"v_mfma_f32_16x16x32_bf16 v[132:135], v[252:255], v[236:239], v[132:135]\n"
"s_nop 4\n"
"v_cvt_pk_bf16_f32 v240, v120, v121\n"
"v_cvt_pk_bf16_f32 v241, v122, v123\n"
"v_cvt_pk_bf16_f32 v242, v124, v125\n"
"v_cvt_pk_bf16_f32 v243, v126, v127\n"
"ds_write_b128 v5, v[240:243] offset:4096\n"
"v_cvt_pk_bf16_f32 v244, v128, v129\n"
"v_cvt_pk_bf16_f32 v245, v130, v131\n"
"v_cvt_pk_bf16_f32 v246, v132, v133\n"
"v_cvt_pk_bf16_f32 v247, v134, v135\n"
"ds_write_b128 v5, v[244:247] offset:13312\n"
"v_mfma_f32_16x16x32_bf16 v[148:151], v[156:159], v[240:243], 0\n"
"v_mfma_f32_16x16x32_bf16 v[152:155], v[240:243], v[240:243], 0\n"
"v_mfma_f32_16x16x32_bf16 v[148:151], v[156:159], v[244:247], v[148:151]\n"
"v_mfma_f32_16x16x32_bf16 v[152:155], v[244:247], v[244:247], v[152:155]\n"
"s_waitcnt vmcnt(1) lgkmcnt(0)\n"
"s_barrier\n"
"ds_read_b128 a[0:3], v1\n"
"ds_read_b128 a[16:19], v1 offset:1024\n"
"ds_read_b128 a[32:35], v1 offset:5120\n"
"ds_read_b128 a[4:7], v1 offset:18432\n"
"ds_read_b128 a[20:23], v1 offset:19456\n"
"ds_read_b128 a[36:39], v1 offset:23552\n"
"ds_read_b128 a[8:11], v2\n"
"ds_read_b128 a[24:27], v2 offset:1024\n"
"ds_read_b128 a[40:43], v2 offset:5120\n"
"s_waitcnt lgkmcnt(6)\n"
"v_mfma_f32_16x16x32_bf16 v[184:187], v[32:35], a[0:3], 0\n"
"ds_read_b128 a[12:15], v2 offset:18432\n"
"v_mfma_f32_16x16x32_bf16 v[184:187], v[36:39], a[16:19], v[184:187]\n"
"ds_read_b128 a[28:31], v2 offset:19456\n"
"buffer_load_dwordx4 v[92:95], v1, s[24:27], s76 offen sc1\n"
"v_mfma_f32_16x16x32_bf16 v[184:187], v[40:43], a[32:35], v[184:187]\n"
"ds_read_b128 a[44:47], v2 offset:23552\n"
"s_mov_b64 exec, 1\n"
"v_mov_b32 v16, 0\n"
"buffer_atomic_add v16, off, s[24:27], s75 offset:520 sc0 sc1\n"
"s_mov_b64 exec, -1\n"
"s_waitcnt lgkmcnt(6)\n"
"v_mfma_f32_16x16x32_bf16 v[188:191], v[32:35], a[4:7], 0\n"
"ds_read_b128 a[48:51], v1 offset:6144\n"
"v_mfma_f32_16x16x32_bf16 v[188:191], v[36:39], a[20:23], v[188:191]\n"
"ds_read_b128 a[64:67], v1 offset:9216\n"
"v_mfma_f32_16x16x32_bf16 v[188:191], v[40:43], a[36:39], v[188:191]\n"
"ds_read_b128 a[80:83], v1 offset:10240\n"
"buffer_load_dwordx4 v[96:99], v1, s[24:27], s76 offen offset:1024 sc1\n"
"s_waitcnt lgkmcnt(6)\n"
"v_mfma_f32_16x16x32_bf16 v[192:195], v[32:35], a[8:11], 0\n"
"ds_read_b128 a[52:55], v1 offset:24576\n"
"v_mfma_f32_16x16x32_bf16 v[192:195], v[36:39], a[24:27], v[192:195]\n"
"ds_read_b128 a[68:71], v1 offset:27648\n"
"v_mfma_f32_16x16x32_bf16 v[192:195], v[40:43], a[40:43], v[192:195]\n"
"ds_read_b128 a[84:87], v1 offset:28672\n"
"s_waitcnt lgkmcnt(6)\n"
"v_mfma_f32_16x16x32_bf16 v[196:199], v[32:35], a[12:15], 0\n"
"ds_read_b128 a[56:59], v2 offset:6144\n"
"buffer_load_dwordx4 v[100:103], v1, s[24:27], s76 offen offset:2048 sc1\n"
"v_mfma_f32_16x16x32_bf16 v[196:199], v[36:39], a[28:31], v[196:199]\n"
"ds_read_b128 a[72:75], v2 offset:9216\n"
"s_add_u32 s76, s76, 0xc000\n"
"v_mfma_f32_16x16x32_bf16 v[196:199], v[40:43], a[44:47], v[196:199]\n"
"ds_read_b128 a[88:91], v2 offset:10240\n"
"s_waitcnt lgkmcnt(6)\n"
"v_mfma_f32_16x16x32_bf16 v[184:187], v[44:47], a[48:51], v[184:187]\n"
"ds_read_b128 a[60:63], v2 offset:24576\n"
"v_mfma_f32_16x16x32_bf16 v[184:187], v[48:51], a[64:67], v[184:187]\n"
"ds_read_b128 a[76:79], v2 offset:27648\n"
"buffer_load_dwordx4 v[32:35], v1, s[24:27], s76 offen sc1\n"
"v_mfma_f32_16x16x32_bf16 v[184:187], v[52:55], a[80:83], v[184:187]\n"
"ds_read_b128 a[92:95], v2 offset:28672\n"
"s_waitcnt lgkmcnt(6)\n"
"v_mfma_f32_16x16x32_bf16 v[188:191], v[44:47], a[52:55], v[188:191]\n"
"ds_read_b128 a[96:99], v1 offset:14336\n"
"v_mfma_f32_16x16x32_bf16 v[188:191], v[48:51], a[68:71], v[188:191]\n"
"ds_read_b128 a[112:115], v1 offset:15360\n"
"v_mfma_f32_16x16x32_bf16 v[188:191], v[52:55], a[84:87], v[188:191]\n"
"ds_read_b128 a[128:131], v1 offset:4096\n"
"buffer_load_dwordx4 v[36:39], v1, s[24:27], s76 offen offset:1024 sc1\n"
"s_waitcnt lgkmcnt(6)\n"
"v_mfma_f32_16x16x32_bf16 v[192:195], v[44:47], a[56:59], v[192:195]\n"
"ds_read_b128 a[100:103], v1 offset:32768\n"
"v_mfma_f32_16x16x32_bf16 v[192:195], v[48:51], a[72:75], v[192:195]\n"
"ds_read_b128 a[116:119], v1 offset:33792\n"
"v_mfma_f32_16x16x32_bf16 v[192:195], v[52:55], a[88:91], v[192:195]\n"
"ds_read_b128 a[132:135], v1 offset:22528\n"
"s_waitcnt lgkmcnt(6)\n"
"v_mfma_f32_16x16x32_bf16 v[196:199], v[44:47], a[60:63], v[196:199]\n"
"ds_read_b128 a[104:107], v2 offset:14336\n"
"buffer_load_dwordx4 v[40:43], v1, s[24:27], s76 offen offset:2048 sc1\n"
"v_mfma_f32_16x16x32_bf16 v[196:199], v[48:51], a[76:79], v[196:199]\n"
"ds_read_b128 a[120:123], v2 offset:15360\n"
"s_add_u32 s76, s76, 0x3000\n"
"v_mfma_f32_16x16x32_bf16 v[196:199], v[52:55], a[92:95], v[196:199]\n"
"ds_read_b128 a[136:139], v2 offset:4096\n"
"s_waitcnt lgkmcnt(6)\n"
"v_mfma_f32_16x16x32_bf16 v[184:187], v[56:59], a[96:99], v[184:187]\n"
"ds_read_b128 a[108:111], v2 offset:32768\n"
"v_mfma_f32_16x16x32_bf16 v[184:187], v[60:63], a[112:115], v[184:187]\n"
"ds_read_b128 a[124:127], v2 offset:33792\n"
"buffer_load_dwordx4 v[44:47], v1, s[24:27], s76 offen sc1\n"
"v_mfma_f32_16x16x32_bf16 v[184:187], v[64:67], a[128:131], v[184:187]\n"
"ds_read_b128 a[140:143], v2 offset:22528\n"
"s_waitcnt lgkmcnt(6)\n"
"v_mfma_f32_16x16x32_bf16 v[188:191], v[56:59], a[100:103], v[188:191]\n"
"ds_read_b128 v[224:227], v5\n"
"v_mfma_f32_16x16x32_bf16 v[188:191], v[60:63], a[116:119], v[188:191]\n"
"ds_read_b128 v[228:231], v5 offset:1024\n"
"v_mfma_f32_16x16x32_bf16 v[188:191], v[64:67], a[132:135], v[188:191]\n"
"ds_read_b128 v[232:235], v5 offset:5120\n"
"buffer_load_dwordx4 v[48:51], v1, s[24:27], s76 offen offset:1024 sc1\n"
"s_waitcnt lgkmcnt(6)\n"
"v_mfma_f32_16x16x32_bf16 v[192:195], v[56:59], a[104:107], v[192:195]\n"
"s_waitcnt vmcnt(7)\n"
"v_readfirstlane_b32 vcc_lo, v16\n"
"s_nop 0\n"
"s_cmp_ge_u32 vcc_lo, 8\n"
"s_cbranch_scc1 Lbp_d%=\n"
"Lbp%=:\n"
"s_mov_b64 exec, 1\n"
"v_mov_b32 v16, 0\n"
"buffer_atomic_add v16, off, s[24:27], s75 offset:520 sc0 sc1\n"
"s_mov_b64 exec, -1\n"
"s_waitcnt vmcnt(0)\n"
"v_readfirstlane_b32 vcc_lo, v16\n"
"s_nop 0\n"
"s_cmp_ge_u32 vcc_lo, 8\n"
"s_cbranch_scc1 Lbp_d%=\n"
"s_sleep 2\n"
"s_sub_u32 s60, s60, 1\n"
"s_cbranch_scc0 Lbp%=\n"
"s_mov_b32 s60, 0\n"
"Lbp_d%=:\n"
"s_add_u32 m0, s61, 0x800\n"
"s_add_u32 s92, s81, 0x800\n"
"buffer_load_dwordx4 v1, s[24:27], s92 offen sc1 lds\n"
"ds_read_b128 v[236:239], v5 offset:6144\n"
"v_mfma_f32_16x16x32_bf16 v[192:195], v[60:63], a[120:123], v[192:195]\n"
"ds_read_b128 v[240:243], v5 offset:9216\n"
"v_mfma_f32_16x16x32_bf16 v[192:195], v[64:67], a[136:139], v[192:195]\n"
"ds_read_b128 v[244:247], v5 offset:10240\n"
"s_waitcnt lgkmcnt(6)\n"
"v_mfma_f32_16x16x32_bf16 v[196:199], v[56:59], a[108:111], v[196:199]\n"
"s_add_u32 m0, s61, 0xc00\n"
"s_add_u32 s92, s81, 0xc00\n"
"buffer_load_dwordx4 v1, s[24:27], s92 offen sc1 lds\n"
"ds_read_b128 v[248:251], v5 offset:14336\n"
"buffer_load_dwordx4 v[52:55], v1, s[24:27], s76 offen offset:2048 sc1\n"
"v_mfma_f32_16x16x32_bf16 v[196:199], v[60:63], a[124:127], v[196:199]\n"
"s_add_u32 m0, s61, 0x1c00\n"
"s_add_u32 s92, s81, 0x1c00\n"
"buffer_load_dwordx4 v1, s[24:27], s92 offen sc1 lds\n"
"ds_read_b128 v[252:255], v5 offset:15360\n"
"s_add_u32 s76, s76, 0x3000\n"
"v_mfma_f32_16x16x32_bf16 v[196:199], v[64:67], a[140:143], v[196:199]\n"
"s_waitcnt lgkmcnt(7)\n"
"v_mfma_f32_16x16x32_bf16 v[148:151], v[156:159], v[224:227], v[148:151]\n"
"v_mfma_f32_16x16x32_bf16 v[152:155], v[224:227], v[224:227], v[152:155]\n"
"v_mfma_f32_16x16x32_bf16 v[200:203], v[68:71], a[0:3], 0\n"
"v_mfma_f32_16x16x32_bf16 v[200:203], v[72:75], a[16:19], v[200:203]\n"
"s_add_u32 m0, s61, 0x2000\n"
"s_add_u32 s92, s81, 0x2000\n"
"buffer_load_dwordx4 v1, s[24:27], s92 offen sc1 lds\n"
"buffer_load_dwordx4 v[56:59], v1, s[24:27], s76 offen sc1\n"
"v_mfma_f32_16x16x32_bf16 v[200:203], v[76:79], a[32:35], v[200:203]\n"
"s_waitcnt lgkmcnt(6)\n"
"v_mfma_f32_16x16x32_bf16 v[148:151], v[156:159], v[228:231], v[148:151]\n"
"v_mfma_f32_16x16x32_bf16 v[152:155], v[228:231], v[228:231], v[152:155]\n"
"s_add_u32 m0, s61, 0x2c00\n"
"s_add_u32 s92, s81, 0x2c00\n"
"buffer_load_dwordx4 v1, s[24:27], s92 offen sc1 lds\n"
"v_mfma_f32_16x16x32_bf16 v[204:207], v[68:71], a[4:7], 0\n"
"v_mfma_f32_16x16x32_bf16 v[204:207], v[72:75], a[20:23], v[204:207]\n"
"v_mfma_f32_16x16x32_bf16 v[204:207], v[76:79], a[36:39], v[204:207]\n"
"s_waitcnt lgkmcnt(5)\n"
"v_mfma_f32_16x16x32_bf16 v[148:151], v[156:159], v[232:235], v[148:151]\n"
"v_mfma_f32_16x16x32_bf16 v[152:155], v[232:235], v[232:235], v[152:155]\n"
"s_add_u32 m0, s61, 0x3000\n"
"s_add_u32 s92, s81, 0x3000\n"
"buffer_load_dwordx4 v1, s[24:27], s92 offen sc1 lds\n"
"buffer_load_dwordx4 v[60:63], v1, s[24:27], s76 offen offset:1024 sc1\n"
"v_mfma_f32_16x16x32_bf16 v[208:211], v[68:71], a[8:11], 0\n"
"s_add_u32 m0, s61, 0x4000\n"
"s_add_u32 s92, s81, 0x4000\n"
"buffer_load_dwordx4 v1, s[24:27], s92 offen sc1 lds\n"
"v_mfma_f32_16x16x32_bf16 v[208:211], v[72:75], a[24:27], v[208:211]\n"
"v_mfma_f32_16x16x32_bf16 v[208:211], v[76:79], a[40:43], v[208:211]\n"
"s_waitcnt lgkmcnt(4)\n"
"v_mfma_f32_16x16x32_bf16 v[148:151], v[156:159], v[236:239], v[148:151]\n"
"v_mfma_f32_16x16x32_bf16 v[152:155], v[236:239], v[236:239], v[152:155]\n"
"v_mfma_f32_16x16x32_bf16 v[212:215], v[68:71], a[12:15], 0\n"
"s_add_u32 m0, s61, 0x4400\n"
"s_add_u32 s92, s81, 0x4400\n"
"buffer_load_dwordx4 v1, s[24:27], s92 offen sc1 lds\n"
"buffer_load_dwordx4 v[64:67], v1, s[24:27], s76 offen offset:2048 sc1\n"
"v_mfma_f32_16x16x32_bf16 v[212:215], v[72:75], a[28:31], v[212:215]\n"
"s_add_u32 s76, s76, 0xfffdf000\n"
"v_mfma_f32_16x16x32_bf16 v[212:215], v[76:79], a[44:47], v[212:215]\n"
"s_waitcnt lgkmcnt(3)\n"
"v_mfma_f32_16x16x32_bf16 v[148:151], v[156:159], v[240:243], v[148:151]\n"
"v_mfma_f32_16x16x32_bf16 v[152:155], v[240:243], v[240:243], v[152:155]\n"
"v_mfma_f32_16x16x32_bf16 v[200:203], v[80:83], a[48:51], v[200:203]\n"
"v_mfma_f32_16x16x32_bf16 v[200:203], v[84:87], a[64:67], v[200:203]\n"
"buffer_load_dwordx4 v[68:71], v1, s[24:27], s76 offen sc1\n"
"v_mfma_f32_16x16x32_bf16 v[200:203], v[88:91], a[80:83], v[200:203]\n"
"s_waitcnt lgkmcnt(2)\n"
"v_mfma_f32_16x16x32_bf16 v[148:151], v[156:159], v[244:247], v[148:151]\n"
"v_mfma_f32_16x16x32_bf16 v[152:155], v[244:247], v[244:247], v[152:155]\n"
"v_mfma_f32_16x16x32_bf16 v[204:207], v[80:83], a[52:55], v[204:207]\n"
"v_mfma_f32_16x16x32_bf16 v[204:207], v[84:87], a[68:71], v[204:207]\n"
"v_mfma_f32_16x16x32_bf16 v[204:207], v[88:91], a[84:87], v[204:207]\n"
"s_waitcnt lgkmcnt(1)\n"
"v_mfma_f32_16x16x32_bf16 v[148:151], v[156:159], v[248:251], v[148:151]\n"
"v_mfma_f32_16x16x32_bf16 v[152:155], v[248:251], v[248:251], v[152:155]\n"
"buffer_load_dwordx4 v[72:75], v1, s[24:27], s76 offen offset:1024 sc1\n"
"v_mfma_f32_16x16x32_bf16 v[208:211], v[80:83], a[56:59], v[208:211]\n"
"v_mfma_f32_16x16x32_bf16 v[208:211], v[84:87], a[72:75], v[208:211]\n"
"v_mfma_f32_16x16x32_bf16 v[208:211], v[88:91], a[88:91], v[208:211]\n"
"s_waitcnt lgkmcnt(0)\n"
"v_mfma_f32_16x16x32_bf16 v[148:151], v[156:159], v[252:255], v[148:151]\n"
"v_mfma_f32_16x16x32_bf16 v[152:155], v[252:255], v[252:255], v[152:155]\n"
"v_mfma_f32_16x16x32_bf16 v[212:215], v[80:83], a[60:63], v[212:215]\n"
"buffer_load_dwordx4 v[76:79], v1, s[24:27], s76 offen offset:2048 sc1\n"
"v_mfma_f32_16x16x32_bf16 v[212:215], v[84:87], a[76:79], v[212:215]\n"
"s_add_u32 s76, s76, 0x3000\n"
"v_mfma_f32_16x16x32_bf16 v[212:215], v[88:91], a[92:95], v[212:215]\n"
"s_cmp_lg_u32 s59, 0\n"
"s_cbranch_scc1 Lcs1%=\n"
"v_add_f32 v160, v160, v164\n"
"v_add_f32 v161, v161, v165\n"
"v_add_f32 v162, v162, v166\n"
"v_add_f32 v163, v163, v167\n"
"v_add_f32 v160, v160, v168\n"
"v_add_f32 v161, v161, v169\n"
"v_add_f32 v162, v162, v170\n"
"v_add_f32 v163, v163, v171\n"
"v_add_f32 v160, v160, v172\n"
"v_add_f32 v161, v161, v173\n"
"v_add_f32 v162, v162, v174\n"
"v_add_f32 v163, v163, v175\n"
"v_add_f32 v160, v160, v176\n"
"v_add_f32 v161, v161, v177\n"
"v_add_f32 v162, v162, v178\n"
"v_add_f32 v163, v163, v179\n"
"v_add_f32 v160, v160, v180\n"
"v_add_f32 v161, v161, v181\n"
"v_add_f32 v162, v162, v182\n"
"v_add_f32 v163, v163, v183\n"
"s_mul_i32 s62, s56, 0x240\n"
"s_add_u32 s62, s62, 0x27200\n"
"v_add_u32 v18, s62, v1\n"
"s_bfm_b64 exec, 36, 0\n"
"ds_write_b128 v18, v[160:163]\n"
"s_mov_b64 exec, -1\n"
"Lcs1%=:\n"
"s_waitcnt vmcnt(20)\n"
"v_mfma_f32_16x16x32_bf16 v[200:203], v[92:95], a[96:99], v[200:203]\n"
"v_mfma_f32_16x16x32_bf16 v[200:203], v[96:99], a[112:115], v[200:203]\n"
"buffer_load_dwordx4 v[80:83], v1, s[24:27], s76 offen sc1\n"
"v_mfma_f32_16x16x32_bf16 v[200:203], v[100:103], a[128:131], v[200:203]\n"
"v_mfma_f32_16x16x32_bf16 v[204:207], v[92:95], a[100:103], v[204:207]\n"
"v_mfma_f32_16x16x32_bf16 v[204:207], v[96:99], a[116:119], v[204:207]\n"
"v_mfma_f32_16x16x32_bf16 v[204:207], v[100:103], a[132:135], v[204:207]\n"
"buffer_load_dwordx4 v[84:87], v1, s[24:27], s76 offen offset:1024 sc1\n"
"v_mfma_f32_16x16x32_bf16 v[208:211], v[92:95], a[104:107], v[208:211]\n"
"v_mfma_f32_16x16x32_bf16 v[208:211], v[96:99], a[120:123], v[208:211]\n"
"v_mfma_f32_16x16x32_bf16 v[208:211], v[100:103], a[136:139], v[208:211]\n"
"v_mfma_f32_16x16x32_bf16 v[212:215], v[92:95], a[108:111], v[212:215]\n"
"buffer_load_dwordx4 v[88:91], v1, s[24:27], s76 offen offset:2048 sc1\n"
"v_mfma_f32_16x16x32_bf16 v[212:215], v[96:99], a[124:127], v[212:215]\n"
"s_add_u32 s76, s76, 0x3000\n"
"v_mfma_f32_16x16x32_bf16 v[212:215], v[100:103], a[140:143], v[212:215]\n"
"s_waitcnt vmcnt(20)\n"
"v_mfma_f32_16x16x32_bf16 v[104:107], v[32:35], a[0:3], 0\n"
"v_mfma_f32_16x16x32_bf16 v[104:107], v[36:39], a[16:19], v[104:107]\n"
"buffer_load_dwordx4 v[92:95], v1, s[24:27], s76 offen sc1\n"
"v_mfma_f32_16x16x32_bf16 v[104:107], v[40:43], a[32:35], v[104:107]\n"
"v_mfma_f32_16x16x32_bf16 v[108:111], v[32:35], a[4:7], 0\n"
"v_mfma_f32_16x16x32_bf16 v[108:111], v[36:39], a[20:23], v[108:111]\n"
"v_mfma_f32_16x16x32_bf16 v[108:111], v[40:43], a[36:39], v[108:111]\n"
"buffer_load_dwordx4 v[96:99], v1, s[24:27], s76 offen offset:1024 sc1\n"
"v_mfma_f32_16x16x32_bf16 v[112:115], v[32:35], a[8:11], 0\n"
"v_mfma_f32_16x16x32_bf16 v[112:115], v[36:39], a[24:27], v[112:115]\n"
"v_mfma_f32_16x16x32_bf16 v[112:115], v[40:43], a[40:43], v[112:115]\n"
"s_waitcnt vmcnt(19)\n"
"ds_read_b128 v[232:235], v5 offset:2048\n"
"v_mfma_f32_16x16x32_bf16 v[116:119], v[32:35], a[12:15], 0\n"
"buffer_load_dwordx4 v[100:103], v1, s[24:27], s76 offen offset:2048 sc1\n"
"v_mfma_f32_16x16x32_bf16 v[116:119], v[36:39], a[28:31], v[116:119]\n"
"s_add_u32 s76, s76, 0xc000\n"
"v_mfma_f32_16x16x32_bf16 v[116:119], v[40:43], a[44:47], v[116:119]\n"
"s_waitcnt vmcnt(19)\n"
"ds_read_b128 v[236:239], v5 offset:3072\n"
"s_waitcnt lgkmcnt(1)\n"
"v_mfma_f32_16x16x32_bf16 v[148:151], v[156:159], v[232:235], v[148:151]\n"
"v_mfma_f32_16x16x32_bf16 v[152:155], v[232:235], v[232:235], v[152:155]\n"
"s_waitcnt vmcnt(18)\n"
"v_mfma_f32_16x16x32_bf16 v[104:107], v[44:47], a[48:51], v[104:107]\n"
"v_mfma_f32_16x16x32_bf16 v[104:107], v[48:51], a[64:67], v[104:107]\n"
"buffer_load_dwordx4 v[32:35], v1, s[24:27], s76 offen sc1\n"
"v_mfma_f32_16x16x32_bf16 v[104:107], v[52:55], a[80:83], v[104:107]\n"
"s_waitcnt vmcnt(18)\n"
"ds_read_b128 v[240:243], v5 offset:7168\n"
"s_waitcnt lgkmcnt(1)\n"
"v_mfma_f32_16x16x32_bf16 v[148:151], v[156:159], v[236:239], v[148:151]\n"
"v_mfma_f32_16x16x32_bf16 v[152:155], v[236:239], v[236:239], v[152:155]\n"
"v_mfma_f32_16x16x32_bf16 v[108:111], v[44:47], a[52:55], v[108:111]\n"
"v_mfma_f32_16x16x32_bf16 v[108:111], v[48:51], a[68:71], v[108:111]\n"
"v_mfma_f32_16x16x32_bf16 v[108:111], v[52:55], a[84:87], v[108:111]\n"
"s_waitcnt vmcnt(17)\n"
"ds_read_b128 v[244:247], v5 offset:8192\n"
"s_waitcnt lgkmcnt(1)\n"
"v_mfma_f32_16x16x32_bf16 v[148:151], v[156:159], v[240:243], v[148:151]\n"
"v_mfma_f32_16x16x32_bf16 v[152:155], v[240:243], v[240:243], v[152:155]\n"
"buffer_load_dwordx4 v[36:39], v1, s[24:27], s76 offen offset:1024 sc1\n"
"v_mfma_f32_16x16x32_bf16 v[112:115], v[44:47], a[56:59], v[112:115]\n"
"v_mfma_f32_16x16x32_bf16 v[112:115], v[48:51], a[72:75], v[112:115]\n"
"v_mfma_f32_16x16x32_bf16 v[112:115], v[52:55], a[88:91], v[112:115]\n"
"s_waitcnt vmcnt(16)\n"
"ds_read_b128 v[248:251], v5 offset:11264\n"
"s_waitcnt lgkmcnt(1)\n"
"v_mfma_f32_16x16x32_bf16 v[148:151], v[156:159], v[244:247], v[148:151]\n"
"v_mfma_f32_16x16x32_bf16 v[152:155], v[244:247], v[244:247], v[152:155]\n"
"v_mfma_f32_16x16x32_bf16 v[116:119], v[44:47], a[60:63], v[116:119]\n"
"buffer_load_dwordx4 v[40:43], v1, s[24:27], s76 offen offset:2048 sc1\n"
"v_mfma_f32_16x16x32_bf16 v[116:119], v[48:51], a[76:79], v[116:119]\n"
"s_add_u32 s76, s76, 0x3000\n"
"v_mfma_f32_16x16x32_bf16 v[116:119], v[52:55], a[92:95], v[116:119]\n"
"s_waitcnt vmcnt(16)\n"
"ds_read_b128 v[252:255], v5 offset:12288\n"
"s_waitcnt lgkmcnt(1)\n"
"v_mfma_f32_16x16x32_bf16 v[148:151], v[156:159], v[248:251], v[148:151]\n"
"v_mfma_f32_16x16x32_bf16 v[152:155], v[248:251], v[248:251], v[152:155]\n"
"s_waitcnt vmcnt(12)\n"
"v_mfma_f32_16x16x32_bf16 v[104:107], v[56:59], a[96:99], v[104:107]\n"
"v_mfma_f32_16x16x32_bf16 v[104:107], v[60:63], a[112:115], v[104:107]\n"
"buffer_load_dwordx4 v[44:47], v1, s[24:27], s76 offen sc1\n"
"v_mfma_f32_16x16x32_bf16 v[104:107], v[64:67], a[128:131], v[104:107]\n"
"ds_read_b128 v[224:227], v5 offset:16384\n"
"s_waitcnt lgkmcnt(1)\n"
"v_mfma_f32_16x16x32_bf16 v[148:151], v[156:159], v[252:255], v[148:151]\n"
"v_mfma_f32_16x16x32_bf16 v[152:155], v[252:255], v[252:255], v[152:155]\n"
"v_mfma_f32_16x16x32_bf16 v[108:111], v[56:59], a[100:103], v[108:111]\n"
"v_mfma_f32_16x16x32_bf16 v[108:111], v[60:63], a[116:119], v[108:111]\n"
"v_mfma_f32_16x16x32_bf16 v[108:111], v[64:67], a[132:135], v[108:111]\n"
"ds_read_b128 v[228:231], v5 offset:17408\n"
"s_waitcnt lgkmcnt(1)\n"
"v_mfma_f32_16x16x32_bf16 v[148:151], v[156:159], v[224:227], v[148:151]\n"
"v_mfma_f32_16x16x32_bf16 v[152:155], v[224:227], v[224:227], v[152:155]\n"
"buffer_load_dwordx4 v[48:51], v1, s[24:27], s76 offen offset:1024 sc1\n"
"v_mfma_f32_16x16x32_bf16 v[112:115], v[56:59], a[104:107], v[112:115]\n"
"v_mfma_f32_16x16x32_bf16 v[112:115], v[60:63], a[120:123], v[112:115]\n"
"v_mfma_f32_16x16x32_bf16 v[112:115], v[64:67], a[136:139], v[112:115]\n"
"s_waitcnt lgkmcnt(0)\n"
"v_mfma_f32_16x16x32_bf16 v[148:151], v[156:159], v[228:231], v[148:151]\n"
"v_mfma_f32_16x16x32_bf16 v[152:155], v[228:231], v[228:231], v[152:155]\n"
"v_mfma_f32_16x16x32_bf16 v[116:119], v[56:59], a[108:111], v[116:119]\n"
"buffer_load_dwordx4 v[52:55], v1, s[24:27], s76 offen offset:2048 sc1\n"
"v_mfma_f32_16x16x32_bf16 v[116:119], v[60:63], a[124:127], v[116:119]\n"
"s_add_u32 s76, s76, 0x3000\n"
"v_mfma_f32_16x16x32_bf16 v[116:119], v[64:67], a[140:143], v[116:119]\n"
"v_and_b32 v18, 15, v0\n"
"s_lshl_b32 s62, s56, 6\n"
"s_add_u32 s62, s62, 0x27000\n"
"v_lshl_add_u32 v18, v18, 2, s62\n"
"s_mov_b32 vcc_lo, 0xaaaaaaaa\n"
"s_mov_b32 vcc_hi, 0xaaaaaaaa\n"
"v_mul_f32 v30, 0xbae38e39, v148\n"
"v_cndmask_b32 v152, v152, v153, vcc\n"
"v_cndmask_b32 v154, v154, v155, vcc\n"
"s_mov_b32 vcc_lo, 0xcccccccc\n"
"s_mov_b32 vcc_hi, 0xcccccccc\n"
"v_mul_f32 v19, v30, v30\n"
"v_cndmask_b32 v152, v152, v154, vcc\n"
"v_mul_f32 v152, 0x3ae38e39, v152\n"
"v_sub_f32 v152, v152, v19\n"
"v_max_f32 v152, 0, v152\n"
"v_cvt_pk_bf16_f32 v19, v30, v152\n"
"v_and_b32 v152, 0xffff0000, v19\n"
"v_lshlrev_b32 v30, 16, v19\n"
"v_add_f32 v152, 0x3727c5ac, v152\n"
"v_cvt_pk_bf16_f32 v152, 0, v152\n"
"v_sqrt_f32 v152, v152\n"
"s_nop 0\n"
"v_cvt_pk_bf16_f32 v152, 0, v152\n"
"v_rcp_f32 v152, v152\n"
"s_mov_b64 exec, s[94:95]\n"
"ds_write_b32 v18, v30 offset:256\n"
"ds_write_b32 v18, v152\n"
"s_mov_b64 exec, -1\n"
"v_and_b32 v18, 15, v0\n"
"v_lshlrev_b32 v18, 2, v18\n"
"v_add_u32 v18, 0x27000, v18\n"
"s_waitcnt lgkmcnt(0)\n"
"s_barrier\n"
"ds_read_b128 a[144:147], v1 offset:13312\n"
"ds_read_b128 a[160:163], v1 offset:2048\n"
"ds_read_b128 a[176:179], v1 offset:3072\n"
"ds_read_b128 a[148:151], v1 offset:31744\n"
"ds_read_b128 a[164:167], v1 offset:20480\n"
"ds_read_b128 a[180:183], v1 offset:21504\n"
"ds_read_b128 a[152:155], v2 offset:13312\n"
"ds_read_b128 a[168:171], v2 offset:2048\n"
"ds_read_b128 a[184:187], v2 offset:3072\n"
"s_waitcnt vmcnt(12) lgkmcnt(6)\n"
"v_mfma_f32_16x16x32_bf16 v[184:187], v[68:71], a[144:147], v[184:187]\n"
"ds_read_b128 a[156:159], v2 offset:31744\n"
"v_mfma_f32_16x16x32_bf16 v[184:187], v[72:75], a[160:163], v[184:187]\n"
"ds_read_b128 a[172:175], v2 offset:20480\n"
"buffer_load_dwordx4 v[56:59], v1, s[24:27], s76 offen sc1\n"
"v_mfma_f32_16x16x32_bf16 v[184:187], v[76:79], a[176:179], v[184:187]\n"
"ds_read_b128 a[188:191], v2 offset:21504\n"
"s_waitcnt lgkmcnt(6)\n"
"v_mfma_f32_16x16x32_bf16 v[188:191], v[68:71], a[148:151], v[188:191]\n"
"ds_read_b128 a[192:195], v1 offset:7168\n"
"v_mfma_f32_16x16x32_bf16 v[188:191], v[72:75], a[164:167], v[188:191]\n"
"ds_read_b128 a[208:211], v1 offset:8192\n"
"v_mfma_f32_16x16x32_bf16 v[188:191], v[76:79], a[180:183], v[188:191]\n"
"ds_read_b128 a[224:227], v1 offset:11264\n"
"buffer_load_dwordx4 v[60:63], v1, s[24:27], s76 offen offset:1024 sc1\n"
"s_waitcnt lgkmcnt(6)\n"
"v_mfma_f32_16x16x32_bf16 v[192:195], v[68:71], a[152:155], v[192:195]\n"
"ds_read_b128 a[196:199], v1 offset:25600\n"
"v_mfma_f32_16x16x32_bf16 v[192:195], v[72:75], a[168:171], v[192:195]\n"
"ds_read_b128 a[212:215], v1 offset:26624\n"
"v_mfma_f32_16x16x32_bf16 v[192:195], v[76:79], a[184:187], v[192:195]\n"
"ds_read_b128 a[228:231], v1 offset:29696\n"
"s_waitcnt lgkmcnt(6)\n"
"v_mfma_f32_16x16x32_bf16 v[196:199], v[68:71], a[156:159], v[196:199]\n"
"ds_read_b128 a[200:203], v2 offset:7168\n"
"buffer_load_dwordx4 v[64:67], v1, s[24:27], s76 offen offset:2048 sc1\n"
"v_mfma_f32_16x16x32_bf16 v[196:199], v[72:75], a[172:175], v[196:199]\n"
"ds_read_b128 a[216:219], v2 offset:8192\n"
"s_add_u32 s76, s76, 0xc000\n"
"v_mfma_f32_16x16x32_bf16 v[196:199], v[76:79], a[188:191], v[196:199]\n"
"ds_read_b128 a[232:235], v2 offset:11264\n"
"s_waitcnt vmcnt(12) lgkmcnt(6)\n"
"v_mfma_f32_16x16x32_bf16 v[184:187], v[80:83], a[192:195], v[184:187]\n"
"ds_read_b128 a[204:207], v2 offset:25600\n"
"v_mfma_f32_16x16x32_bf16 v[184:187], v[84:87], a[208:211], v[184:187]\n"
"ds_read_b128 a[220:223], v2 offset:26624\n"
"buffer_load_dwordx4 v[68:71], v1, s[24:27], s76 offen sc1\n"
"v_mfma_f32_16x16x32_bf16 v[184:187], v[88:91], a[224:227], v[184:187]\n"
"ds_read_b128 a[236:239], v2 offset:29696\n"
"s_waitcnt lgkmcnt(6)\n"
"v_mfma_f32_16x16x32_bf16 v[188:191], v[80:83], a[196:199], v[188:191]\n"
"ds_read_b128 a[240:243], v1 offset:12288\n"
"v_mfma_f32_16x16x32_bf16 v[188:191], v[84:87], a[212:215], v[188:191]\n"
"ds_read_b128 v[224:227], v1 offset:16384\n"
"v_mfma_f32_16x16x32_bf16 v[188:191], v[88:91], a[228:231], v[188:191]\n"
"ds_read_b128 v[240:243], v1 offset:17408\n"
"buffer_load_dwordx4 v[72:75], v1, s[24:27], s76 offen offset:1024 sc1\n"
"s_waitcnt lgkmcnt(6)\n"
"v_mfma_f32_16x16x32_bf16 v[192:195], v[80:83], a[200:203], v[192:195]\n"
"ds_read_b128 a[244:247], v1 offset:30720\n"
"v_mfma_f32_16x16x32_bf16 v[192:195], v[84:87], a[216:219], v[192:195]\n"
"ds_read_b128 v[228:231], v1 offset:34816\n"
"v_mfma_f32_16x16x32_bf16 v[192:195], v[88:91], a[232:235], v[192:195]\n"
"ds_read_b128 v[244:247], v1 offset:35840\n"
"s_waitcnt lgkmcnt(6)\n"
"v_mfma_f32_16x16x32_bf16 v[196:199], v[80:83], a[204:207], v[196:199]\n"
"ds_read_b128 a[248:251], v2 offset:12288\n"
"buffer_load_dwordx4 v[76:79], v1, s[24:27], s76 offen offset:2048 sc1\n"
"v_mfma_f32_16x16x32_bf16 v[196:199], v[84:87], a[220:223], v[196:199]\n"
"ds_read_b128 v[232:235], v2 offset:16384\n"
"s_add_u32 s76, s76, 0x3000\n"
"v_mfma_f32_16x16x32_bf16 v[196:199], v[88:91], a[236:239], v[196:199]\n"
"ds_read_b128 v[248:251], v2 offset:17408\n"
"s_waitcnt vmcnt(12) lgkmcnt(6)\n"
"v_mfma_f32_16x16x32_bf16 v[184:187], v[92:95], a[240:243], v[184:187]\n"
"ds_read_b128 a[252:255], v2 offset:30720\n"
"v_mfma_f32_16x16x32_bf16 v[184:187], v[96:99], v[224:227], v[184:187]\n"
"ds_read_b128 v[236:239], v2 offset:34816\n"
"buffer_load_dwordx4 v[80:83], v1, s[24:27], s76 offen sc1\n"
"v_mfma_f32_16x16x32_bf16 v[184:187], v[100:103], v[240:243], v[184:187]\n"
"ds_read_b128 v[252:255], v2 offset:35840\n"
"s_waitcnt lgkmcnt(6)\n"
"v_mfma_f32_16x16x32_bf16 v[188:191], v[92:95], a[244:247], v[188:191]\n"
"ds_read_b32 v144, v18 offset:256\n"
"v_mfma_f32_16x16x32_bf16 v[188:191], v[96:99], v[228:231], v[188:191]\n"
"ds_read_b32 v145, v18 offset:320\n"
"v_mfma_f32_16x16x32_bf16 v[188:191], v[100:103], v[244:247], v[188:191]\n"
"ds_read_b32 v146, v18 offset:384\n"
"buffer_load_dwordx4 v[84:87], v1, s[24:27], s76 offen offset:1024 sc1\n"
"s_waitcnt lgkmcnt(6)\n"
"v_mfma_f32_16x16x32_bf16 v[192:195], v[92:95], a[248:251], v[192:195]\n"
"ds_read_b32 v147, v18 offset:448\n"
"v_mfma_f32_16x16x32_bf16 v[192:195], v[96:99], v[232:235], v[192:195]\n"
"ds_read_b128 v[160:163], v11\n"
"v_mfma_f32_16x16x32_bf16 v[192:195], v[100:103], v[248:251], v[192:195]\n"
"v_add_u32 v11, 64, v11\n"
"s_waitcnt lgkmcnt(5)\n"
"v_mfma_f32_16x16x32_bf16 v[196:199], v[92:95], a[252:255], v[196:199]\n"
"ds_read_b128 v[164:167], v11\n"
"buffer_load_dwordx4 v[88:91], v1, s[24:27], s76 offen offset:2048 sc1\n"
"v_mfma_f32_16x16x32_bf16 v[196:199], v[96:99], v[236:239], v[196:199]\n"
"v_add_u32 v11, 64, v11\n"
"s_add_u32 s76, s76, 0x3000\n"
"v_mfma_f32_16x16x32_bf16 v[196:199], v[100:103], v[252:255], v[196:199]\n"
"s_waitcnt vmcnt(12)\n"
"v_mfma_f32_16x16x32_bf16 v[200:203], v[32:35], a[144:147], v[200:203]\n"
"v_mfma_f32_16x16x32_bf16 v[200:203], v[36:39], a[160:163], v[200:203]\n"
"buffer_load_dwordx4 v[92:95], v1, s[24:27], s76 offen sc1\n"
"v_mfma_f32_16x16x32_bf16 v[200:203], v[40:43], a[176:179], v[200:203]\n"
"s_waitcnt lgkmcnt(0)\n"
"v_fmac_f32 v184, v144, v160\n"
"v_fmac_f32 v185, v144, v161\n"
"v_mfma_f32_16x16x32_bf16 v[204:207], v[32:35], a[148:151], v[204:207]\n"
"v_fmac_f32 v186, v144, v162\n"
"v_fmac_f32 v187, v144, v163\n"
"v_mfma_f32_16x16x32_bf16 v[204:207], v[36:39], a[164:167], v[204:207]\n"
"v_cvt_pk_bf16_f32 v136, v184, v185\n"
"v_cvt_pk_bf16_f32 v137, v186, v187\n"
"v_mfma_f32_16x16x32_bf16 v[204:207], v[40:43], a[180:183], v[204:207]\n"
"buffer_load_dwordx4 v[96:99], v1, s[24:27], s76 offen offset:1024 sc1\n"
"v_mfma_f32_16x16x32_bf16 v[208:211], v[32:35], a[152:155], v[208:211]\n"
"v_pk_max_i16 v136, v136, 0\n"
"v_pk_max_i16 v137, v137, 0\n"
"v_mfma_f32_16x16x32_bf16 v[208:211], v[36:39], a[168:171], v[208:211]\n"
"ds_write_b64 v10, v[136:137]\n"
"v_fmac_f32 v188, v145, v160\n"
"v_mfma_f32_16x16x32_bf16 v[208:211], v[40:43], a[184:187], v[208:211]\n"
"v_fmac_f32 v189, v145, v161\n"
"v_fmac_f32 v190, v145, v162\n"
"v_mfma_f32_16x16x32_bf16 v[212:215], v[32:35], a[156:159], v[212:215]\n"
"buffer_load_dwordx4 v[100:103], v1, s[24:27], s76 offen offset:2048 sc1\n"
"v_mfma_f32_16x16x32_bf16 v[212:215], v[36:39], a[172:175], v[212:215]\n"
"v_fmac_f32 v191, v145, v163\n"
"v_cvt_pk_bf16_f32 v138, v188, v189\n"
"s_add_u32 s76, s76, 0x3000\n"
"v_mfma_f32_16x16x32_bf16 v[212:215], v[40:43], a[188:191], v[212:215]\n"
"v_cvt_pk_bf16_f32 v139, v190, v191\n"
"v_pk_max_i16 v138, v138, 0\n"
"s_waitcnt vmcnt(12)\n"
"v_mfma_f32_16x16x32_bf16 v[200:203], v[44:47], a[192:195], v[200:203]\n"
"v_pk_max_i16 v139, v139, 0\n"
"ds_write_b64 v10, v[138:139] offset:18432\n"
"v_mfma_f32_16x16x32_bf16 v[200:203], v[48:51], a[208:211], v[200:203]\n"
"buffer_load_dwordx4 v[32:35], v1, s[24:27], s76 offen sc1\n"
"v_mfma_f32_16x16x32_bf16 v[200:203], v[52:55], a[224:227], v[200:203]\n"
"v_fmac_f32 v192, v146, v160\n"
"v_fmac_f32 v193, v146, v161\n"
"v_mfma_f32_16x16x32_bf16 v[204:207], v[44:47], a[196:199], v[204:207]\n"
"v_fmac_f32 v194, v146, v162\n"
"v_fmac_f32 v195, v146, v163\n"
"v_mfma_f32_16x16x32_bf16 v[204:207], v[48:51], a[212:215], v[204:207]\n"
"v_cvt_pk_bf16_f32 v140, v192, v193\n"
"v_cvt_pk_bf16_f32 v141, v194, v195\n"
"v_mfma_f32_16x16x32_bf16 v[204:207], v[52:55], a[228:231], v[204:207]\n"
"buffer_load_dwordx4 v[36:39], v1, s[24:27], s76 offen offset:1024 sc1\n"
"v_mfma_f32_16x16x32_bf16 v[208:211], v[44:47], a[200:203], v[208:211]\n"
"v_pk_max_i16 v140, v140, 0\n"
"v_pk_max_i16 v141, v141, 0\n"
"v_mfma_f32_16x16x32_bf16 v[208:211], v[48:51], a[216:219], v[208:211]\n"
"ds_write_b64 v10, v[140:141] offset:36864\n"
"v_fmac_f32 v196, v147, v160\n"
"v_mfma_f32_16x16x32_bf16 v[208:211], v[52:55], a[232:235], v[208:211]\n"
"v_fmac_f32 v197, v147, v161\n"
"v_fmac_f32 v198, v147, v162\n"
"v_mfma_f32_16x16x32_bf16 v[212:215], v[44:47], a[204:207], v[212:215]\n"
"buffer_load_dwordx4 v[40:43], v1, s[24:27], s76 offen offset:2048 sc1\n"
"v_mfma_f32_16x16x32_bf16 v[212:215], v[48:51], a[220:223], v[212:215]\n"
"v_fmac_f32 v199, v147, v163\n"
"v_cvt_pk_bf16_f32 v142, v196, v197\n"
"s_add_u32 s76, s76, 0x3000\n"
"v_mfma_f32_16x16x32_bf16 v[212:215], v[52:55], a[236:239], v[212:215]\n"
"v_cvt_pk_bf16_f32 v143, v198, v199\n"
"v_pk_max_i16 v142, v142, 0\n"
"s_waitcnt vmcnt(12)\n"
"v_mfma_f32_16x16x32_bf16 v[200:203], v[56:59], a[240:243], v[200:203]\n"
"v_pk_max_i16 v143, v143, 0\n"
"ds_write_b64 v10, v[142:143] offset:55296\n"
"v_add_u32 v10, 0x800, v10\n"
"v_mfma_f32_16x16x32_bf16 v[200:203], v[60:63], v[224:227], v[200:203]\n"
"buffer_load_dwordx4 v[44:47], v1, s[24:27], s76 offen sc1\n"
"v_mfma_f32_16x16x32_bf16 v[200:203], v[64:67], v[240:243], v[200:203]\n"
"v_mfma_f32_16x16x32_bf16 v[204:207], v[56:59], a[244:247], v[204:207]\n"
"v_mfma_f32_16x16x32_bf16 v[204:207], v[60:63], v[228:231], v[204:207]\n"
"ds_read_b128 v[148:151], v11\n"
"v_mfma_f32_16x16x32_bf16 v[204:207], v[64:67], v[244:247], v[204:207]\n"
"v_add_u32 v11, 64, v11\n"
"buffer_load_dwordx4 v[48:51], v1, s[24:27], s76 offen offset:1024 sc1\n"
"v_mfma_f32_16x16x32_bf16 v[208:211], v[56:59], a[248:251], v[208:211]\n"
"v_mfma_f32_16x16x32_bf16 v[208:211], v[60:63], v[232:235], v[208:211]\n"
"v_mfma_f32_16x16x32_bf16 v[208:211], v[64:67], v[248:251], v[208:211]\n"
"v_mfma_f32_16x16x32_bf16 v[212:215], v[56:59], a[252:255], v[212:215]\n"
"buffer_load_dwordx4 v[52:55], v1, s[24:27], s76 offen offset:2048 sc1\n"
"v_mfma_f32_16x16x32_bf16 v[212:215], v[60:63], v[236:239], v[212:215]\n"
"s_add_u32 s76, s76, 0x3000\n"
"v_mfma_f32_16x16x32_bf16 v[212:215], v[64:67], v[252:255], v[212:215]\n"
"s_waitcnt vmcnt(12)\n"
"v_mfma_f32_16x16x32_bf16 v[104:107], v[68:71], a[144:147], v[104:107]\n"
"v_mfma_f32_16x16x32_bf16 v[104:107], v[72:75], a[160:163], v[104:107]\n"
"buffer_load_dwordx4 v[56:59], v1, s[24:27], s76 offen sc1\n"
"v_mfma_f32_16x16x32_bf16 v[104:107], v[76:79], a[176:179], v[104:107]\n"
"s_waitcnt lgkmcnt(0)\n"
"v_fmac_f32 v200, v144, v164\n"
"v_fmac_f32 v201, v144, v165\n"
"v_mfma_f32_16x16x32_bf16 v[108:111], v[68:71], a[148:151], v[108:111]\n"
"v_fmac_f32 v202, v144, v166\n"
"v_fmac_f32 v203, v144, v167\n"
"v_mfma_f32_16x16x32_bf16 v[108:111], v[72:75], a[164:167], v[108:111]\n"
"v_cvt_pk_bf16_f32 v136, v200, v201\n"
"v_cvt_pk_bf16_f32 v137, v202, v203\n"
"v_mfma_f32_16x16x32_bf16 v[108:111], v[76:79], a[180:183], v[108:111]\n"
"buffer_load_dwordx4 v[60:63], v1, s[24:27], s76 offen offset:1024 sc1\n"
"v_mfma_f32_16x16x32_bf16 v[112:115], v[68:71], a[152:155], v[112:115]\n"
"v_pk_max_i16 v136, v136, 0\n"
"v_pk_max_i16 v137, v137, 0\n"
"v_mfma_f32_16x16x32_bf16 v[112:115], v[72:75], a[168:171], v[112:115]\n"
"ds_write_b64 v10, v[136:137]\n"
"v_fmac_f32 v204, v145, v164\n"
"v_mfma_f32_16x16x32_bf16 v[112:115], v[76:79], a[184:187], v[112:115]\n"
"v_fmac_f32 v205, v145, v165\n"
"v_fmac_f32 v206, v145, v166\n"
"v_mfma_f32_16x16x32_bf16 v[116:119], v[68:71], a[156:159], v[116:119]\n"
"buffer_load_dwordx4 v[64:67], v1, s[24:27], s76 offen offset:2048 sc1\n"
"v_mfma_f32_16x16x32_bf16 v[116:119], v[72:75], a[172:175], v[116:119]\n"
"v_fmac_f32 v207, v145, v167\n"
"v_cvt_pk_bf16_f32 v138, v204, v205\n"
"s_add_u32 s76, s76, 0x3000\n"
"v_mfma_f32_16x16x32_bf16 v[116:119], v[76:79], a[188:191], v[116:119]\n"
"v_cvt_pk_bf16_f32 v139, v206, v207\n"
"v_pk_max_i16 v138, v138, 0\n"
"s_waitcnt vmcnt(12)\n"
"v_mfma_f32_16x16x32_bf16 v[104:107], v[80:83], a[192:195], v[104:107]\n"
"v_pk_max_i16 v139, v139, 0\n"
"ds_write_b64 v10, v[138:139] offset:18432\n"
"v_mfma_f32_16x16x32_bf16 v[104:107], v[84:87], a[208:211], v[104:107]\n"
"buffer_load_dwordx4 v[68:71], v1, s[24:27], s76 offen sc1\n"
"v_mfma_f32_16x16x32_bf16 v[104:107], v[88:91], a[224:227], v[104:107]\n"
"v_fmac_f32 v208, v146, v164\n"
"v_fmac_f32 v209, v146, v165\n"
"v_mfma_f32_16x16x32_bf16 v[108:111], v[80:83], a[196:199], v[108:111]\n"
"v_fmac_f32 v210, v146, v166\n"
"v_fmac_f32 v211, v146, v167\n"
"v_mfma_f32_16x16x32_bf16 v[108:111], v[84:87], a[212:215], v[108:111]\n"
"v_cvt_pk_bf16_f32 v140, v208, v209\n"
"v_cvt_pk_bf16_f32 v141, v210, v211\n"
"v_mfma_f32_16x16x32_bf16 v[108:111], v[88:91], a[228:231], v[108:111]\n"
"buffer_load_dwordx4 v[72:75], v1, s[24:27], s76 offen offset:1024 sc1\n"
"v_mfma_f32_16x16x32_bf16 v[112:115], v[80:83], a[200:203], v[112:115]\n"
"v_pk_max_i16 v140, v140, 0\n"
"v_pk_max_i16 v141, v141, 0\n"
"v_mfma_f32_16x16x32_bf16 v[112:115], v[84:87], a[216:219], v[112:115]\n"
"ds_write_b64 v10, v[140:141] offset:36864\n"
"v_fmac_f32 v212, v147, v164\n"
"v_mfma_f32_16x16x32_bf16 v[112:115], v[88:91], a[232:235], v[112:115]\n"
"v_fmac_f32 v213, v147, v165\n"
"v_fmac_f32 v214, v147, v166\n"
"v_mfma_f32_16x16x32_bf16 v[116:119], v[80:83], a[204:207], v[116:119]\n"
"buffer_load_dwordx4 v[76:79], v1, s[24:27], s76 offen offset:2048 sc1\n"
"v_mfma_f32_16x16x32_bf16 v[116:119], v[84:87], a[220:223], v[116:119]\n"
"v_fmac_f32 v215, v147, v167\n"
"v_cvt_pk_bf16_f32 v142, v212, v213\n"
"s_add_u32 s76, s76, 0x3000\n"
"v_mfma_f32_16x16x32_bf16 v[116:119], v[88:91], a[236:239], v[116:119]\n"
"v_cvt_pk_bf16_f32 v143, v214, v215\n"
"v_pk_max_i16 v142, v142, 0\n"
"s_waitcnt vmcnt(12)\n"
"v_mfma_f32_16x16x32_bf16 v[104:107], v[92:95], a[240:243], v[104:107]\n"
"v_pk_max_i16 v143, v143, 0\n"
"ds_write_b64 v10, v[142:143] offset:55296\n"
"v_add_u32 v10, 0x800, v10\n"
"v_mfma_f32_16x16x32_bf16 v[104:107], v[96:99], v[224:227], v[104:107]\n"
"buffer_load_dwordx4 v[80:83], v1, s[24:27], s76 offen sc1\n"
"v_mfma_f32_16x16x32_bf16 v[120:123], v[100:103], v[240:243], v[104:107]\n"
"v_mfma_f32_16x16x32_bf16 v[108:111], v[92:95], a[244:247], v[108:111]\n"
"v_mfma_f32_16x16x32_bf16 v[108:111], v[96:99], v[228:231], v[108:111]\n"
"v_mfma_f32_16x16x32_bf16 v[124:127], v[100:103], v[244:247], v[108:111]\n"
"buffer_load_dwordx4 v[84:87], v1, s[24:27], s76 offen offset:1024 sc1\n"
"v_mfma_f32_16x16x32_bf16 v[112:115], v[92:95], a[248:251], v[112:115]\n"
"v_mfma_f32_16x16x32_bf16 v[112:115], v[96:99], v[232:235], v[112:115]\n"
"v_mfma_f32_16x16x32_bf16 v[128:131], v[100:103], v[248:251], v[112:115]\n"
"v_mfma_f32_16x16x32_bf16 v[116:119], v[92:95], a[252:255], v[116:119]\n"
"buffer_load_dwordx4 v[88:91], v1, s[24:27], s76 offen offset:2048 sc1\n"
"v_mfma_f32_16x16x32_bf16 v[116:119], v[96:99], v[236:239], v[116:119]\n"
"s_add_u32 s76, s76, 0x3000\n"
"v_mfma_f32_16x16x32_bf16 v[132:135], v[100:103], v[252:255], v[116:119]\n"
"s_mov_b32 s61, 5\n"
"Lf1%=:\n"
"s_waitcnt vmcnt(12)\n"
"v_mfma_f32_16x16x32_bf16 v[104:107], v[32:35], a[0:3], 0\n"
"v_mfma_f32_16x16x32_bf16 v[104:107], v[36:39], a[16:19], v[104:107]\n"
"buffer_load_dwordx4 v[92:95], v1, s[24:27], s76 offen sc1\n"
"v_mfma_f32_16x16x32_bf16 v[104:107], v[40:43], a[32:35], v[104:107]\n"
"s_waitcnt lgkmcnt(0)\n"
"v_fmac_f32 v120, v144, v148\n"
"v_fmac_f32 v121, v144, v149\n"
"v_mfma_f32_16x16x32_bf16 v[108:111], v[32:35], a[4:7], 0\n"
"v_fmac_f32 v122, v144, v150\n"
"v_fmac_f32 v123, v144, v151\n"
"v_mfma_f32_16x16x32_bf16 v[108:111], v[36:39], a[20:23], v[108:111]\n"
"v_cvt_pk_bf16_f32 v136, v120, v121\n"
"v_cvt_pk_bf16_f32 v137, v122, v123\n"
"v_mfma_f32_16x16x32_bf16 v[108:111], v[40:43], a[36:39], v[108:111]\n"
"buffer_load_dwordx4 v[96:99], v1, s[24:27], s76 offen offset:1024 sc1\n"
"v_mfma_f32_16x16x32_bf16 v[112:115], v[32:35], a[8:11], 0\n"
"v_pk_max_i16 v136, v136, 0\n"
"v_pk_max_i16 v137, v137, 0\n"
"v_mfma_f32_16x16x32_bf16 v[112:115], v[36:39], a[24:27], v[112:115]\n"
"ds_write_b64 v10, v[136:137]\n"
"v_mfma_f32_16x16x32_bf16 v[112:115], v[40:43], a[40:43], v[112:115]\n"
"v_fmac_f32 v124, v145, v148\n"
"v_fmac_f32 v125, v145, v149\n"
"v_mfma_f32_16x16x32_bf16 v[116:119], v[32:35], a[12:15], 0\n"
"buffer_load_dwordx4 v[100:103], v1, s[24:27], s76 offen offset:2048 sc1\n"
"v_mfma_f32_16x16x32_bf16 v[116:119], v[36:39], a[28:31], v[116:119]\n"
"v_fmac_f32 v126, v145, v150\n"
"v_fmac_f32 v127, v145, v151\n"
"s_add_u32 s76, s76, 0x3000\n"
"v_mfma_f32_16x16x32_bf16 v[116:119], v[40:43], a[44:47], v[116:119]\n"
"v_cvt_pk_bf16_f32 v138, v124, v125\n"
"v_cvt_pk_bf16_f32 v139, v126, v127\n"
"s_waitcnt vmcnt(12)\n"
"v_mfma_f32_16x16x32_bf16 v[104:107], v[44:47], a[48:51], v[104:107]\n"
"v_pk_max_i16 v138, v138, 0\n"
"v_pk_max_i16 v139, v139, 0\n"
"v_mfma_f32_16x16x32_bf16 v[104:107], v[48:51], a[64:67], v[104:107]\n"
"buffer_load_dwordx4 v[32:35], v1, s[24:27], s76 offen sc1\n"
"v_mfma_f32_16x16x32_bf16 v[104:107], v[52:55], a[80:83], v[104:107]\n"
"ds_write_b64 v10, v[138:139] offset:18432\n"
"v_mfma_f32_16x16x32_bf16 v[108:111], v[44:47], a[52:55], v[108:111]\n"
"v_fmac_f32 v128, v146, v148\n"
"v_fmac_f32 v129, v146, v149\n"
"v_mfma_f32_16x16x32_bf16 v[108:111], v[48:51], a[68:71], v[108:111]\n"
"v_fmac_f32 v130, v146, v150\n"
"v_fmac_f32 v131, v146, v151\n"
"v_mfma_f32_16x16x32_bf16 v[108:111], v[52:55], a[84:87], v[108:111]\n"
"buffer_load_dwordx4 v[36:39], v1, s[24:27], s76 offen offset:1024 sc1\n"
"v_mfma_f32_16x16x32_bf16 v[112:115], v[44:47], a[56:59], v[112:115]\n"
"v_cvt_pk_bf16_f32 v140, v128, v129\n"
"v_cvt_pk_bf16_f32 v141, v130, v131\n"
"v_mfma_f32_16x16x32_bf16 v[112:115], v[48:51], a[72:75], v[112:115]\n"
"v_pk_max_i16 v140, v140, 0\n"
"v_pk_max_i16 v141, v141, 0\n"
"v_mfma_f32_16x16x32_bf16 v[112:115], v[52:55], a[88:91], v[112:115]\n"
"ds_write_b64 v10, v[140:141] offset:36864\n"
"v_mfma_f32_16x16x32_bf16 v[116:119], v[44:47], a[60:63], v[116:119]\n"
"buffer_load_dwordx4 v[40:43], v1, s[24:27], s76 offen offset:2048 sc1\n"
"v_mfma_f32_16x16x32_bf16 v[116:119], v[48:51], a[76:79], v[116:119]\n"
"v_fmac_f32 v132, v147, v148\n"
"v_fmac_f32 v133, v147, v149\n"
"s_add_u32 s76, s76, 0x3000\n"
"v_mfma_f32_16x16x32_bf16 v[116:119], v[52:55], a[92:95], v[116:119]\n"
"v_fmac_f32 v134, v147, v150\n"
"v_fmac_f32 v135, v147, v151\n"
"s_waitcnt vmcnt(12)\n"
"v_mfma_f32_16x16x32_bf16 v[104:107], v[56:59], a[96:99], v[104:107]\n"
"v_cvt_pk_bf16_f32 v142, v132, v133\n"
"v_cvt_pk_bf16_f32 v143, v134, v135\n"
"v_mfma_f32_16x16x32_bf16 v[104:107], v[60:63], a[112:115], v[104:107]\n"
"buffer_load_dwordx4 v[44:47], v1, s[24:27], s76 offen sc1\n"
"v_mfma_f32_16x16x32_bf16 v[104:107], v[64:67], a[128:131], v[104:107]\n"
"v_pk_max_i16 v142, v142, 0\n"
"v_pk_max_i16 v143, v143, 0\n"
"v_mfma_f32_16x16x32_bf16 v[108:111], v[56:59], a[100:103], v[108:111]\n"
"ds_write_b64 v10, v[142:143] offset:55296\n"
"v_mfma_f32_16x16x32_bf16 v[108:111], v[60:63], a[116:119], v[108:111]\n"
"v_add_u32 v10, 0x800, v10\n"
"v_mfma_f32_16x16x32_bf16 v[108:111], v[64:67], a[132:135], v[108:111]\n"
"buffer_load_dwordx4 v[48:51], v1, s[24:27], s76 offen offset:1024 sc1\n"
"v_mfma_f32_16x16x32_bf16 v[112:115], v[56:59], a[104:107], v[112:115]\n"
"v_mfma_f32_16x16x32_bf16 v[112:115], v[60:63], a[120:123], v[112:115]\n"
"v_mfma_f32_16x16x32_bf16 v[112:115], v[64:67], a[136:139], v[112:115]\n"
"v_mfma_f32_16x16x32_bf16 v[116:119], v[56:59], a[108:111], v[116:119]\n"
"buffer_load_dwordx4 v[52:55], v1, s[24:27], s76 offen offset:2048 sc1\n"
"v_mfma_f32_16x16x32_bf16 v[116:119], v[60:63], a[124:127], v[116:119]\n"
"s_add_u32 s76, s76, 0x3000\n"
"v_mfma_f32_16x16x32_bf16 v[116:119], v[64:67], a[140:143], v[116:119]\n"
"s_waitcnt vmcnt(12)\n"
"v_mfma_f32_16x16x32_bf16 v[104:107], v[68:71], a[144:147], v[104:107]\n"
"v_mfma_f32_16x16x32_bf16 v[104:107], v[72:75], a[160:163], v[104:107]\n"
"buffer_load_dwordx4 v[56:59], v1, s[24:27], s76 offen sc1\n"
"v_mfma_f32_16x16x32_bf16 v[104:107], v[76:79], a[176:179], v[104:107]\n"
"v_mfma_f32_16x16x32_bf16 v[108:111], v[68:71], a[148:151], v[108:111]\n"
"ds_read_b128 v[148:151], v11\n"
"v_mfma_f32_16x16x32_bf16 v[108:111], v[72:75], a[164:167], v[108:111]\n"
"v_add_u32 v11, 64, v11\n"
"v_mfma_f32_16x16x32_bf16 v[108:111], v[76:79], a[180:183], v[108:111]\n"
"buffer_load_dwordx4 v[60:63], v1, s[24:27], s76 offen offset:1024 sc1\n"
"v_mfma_f32_16x16x32_bf16 v[112:115], v[68:71], a[152:155], v[112:115]\n"
"v_mfma_f32_16x16x32_bf16 v[112:115], v[72:75], a[168:171], v[112:115]\n"
"v_mfma_f32_16x16x32_bf16 v[112:115], v[76:79], a[184:187], v[112:115]\n"
"v_mfma_f32_16x16x32_bf16 v[116:119], v[68:71], a[156:159], v[116:119]\n"
"buffer_load_dwordx4 v[64:67], v1, s[24:27], s76 offen offset:2048 sc1\n"
"v_mfma_f32_16x16x32_bf16 v[116:119], v[72:75], a[172:175], v[116:119]\n"
"s_add_u32 s76, s76, 0x3000\n"
"v_mfma_f32_16x16x32_bf16 v[116:119], v[76:79], a[188:191], v[116:119]\n"
"s_waitcnt vmcnt(12)\n"
"v_mfma_f32_16x16x32_bf16 v[104:107], v[80:83], a[192:195], v[104:107]\n"
"v_mfma_f32_16x16x32_bf16 v[104:107], v[84:87], a[208:211], v[104:107]\n"
"buffer_load_dwordx4 v[68:71], v1, s[24:27], s76 offen sc1\n"
"v_mfma_f32_16x16x32_bf16 v[104:107], v[88:91], a[224:227], v[104:107]\n"
"v_mfma_f32_16x16x32_bf16 v[108:111], v[80:83], a[196:199], v[108:111]\n"
"v_mfma_f32_16x16x32_bf16 v[108:111], v[84:87], a[212:215], v[108:111]\n"
"v_mfma_f32_16x16x32_bf16 v[108:111], v[88:91], a[228:231], v[108:111]\n"
"buffer_load_dwordx4 v[72:75], v1, s[24:27], s76 offen offset:1024 sc1\n"
"v_mfma_f32_16x16x32_bf16 v[112:115], v[80:83], a[200:203], v[112:115]\n"
"v_mfma_f32_16x16x32_bf16 v[112:115], v[84:87], a[216:219], v[112:115]\n"
"v_mfma_f32_16x16x32_bf16 v[112:115], v[88:91], a[232:235], v[112:115]\n"
"v_mfma_f32_16x16x32_bf16 v[116:119], v[80:83], a[204:207], v[116:119]\n"
"buffer_load_dwordx4 v[76:79], v1, s[24:27], s76 offen offset:2048 sc1\n"
"v_mfma_f32_16x16x32_bf16 v[116:119], v[84:87], a[220:223], v[116:119]\n"
"s_add_u32 s76, s76, 0x3000\n"
"v_mfma_f32_16x16x32_bf16 v[116:119], v[88:91], a[236:239], v[116:119]\n"
"s_waitcnt vmcnt(12)\n"
"v_mfma_f32_16x16x32_bf16 v[104:107], v[92:95], a[240:243], v[104:107]\n"
"v_mfma_f32_16x16x32_bf16 v[104:107], v[96:99], v[224:227], v[104:107]\n"
"buffer_load_dwordx4 v[80:83], v1, s[24:27], s76 offen sc1\n"
"v_mfma_f32_16x16x32_bf16 v[120:123], v[100:103], v[240:243], v[104:107]\n"
"v_mfma_f32_16x16x32_bf16 v[108:111], v[92:95], a[244:247], v[108:111]\n"
"v_mfma_f32_16x16x32_bf16 v[108:111], v[96:99], v[228:231], v[108:111]\n"
"v_mfma_f32_16x16x32_bf16 v[124:127], v[100:103], v[244:247], v[108:111]\n"
"buffer_load_dwordx4 v[84:87], v1, s[24:27], s76 offen offset:1024 sc1\n"
"v_mfma_f32_16x16x32_bf16 v[112:115], v[92:95], a[248:251], v[112:115]\n"
"v_mfma_f32_16x16x32_bf16 v[112:115], v[96:99], v[232:235], v[112:115]\n"
"v_mfma_f32_16x16x32_bf16 v[128:131], v[100:103], v[248:251], v[112:115]\n"
"v_mfma_f32_16x16x32_bf16 v[116:119], v[92:95], a[252:255], v[116:119]\n"
"buffer_load_dwordx4 v[88:91], v1, s[24:27], s76 offen offset:2048 sc1\n"
"v_mfma_f32_16x16x32_bf16 v[116:119], v[96:99], v[236:239], v[116:119]\n"
"s_add_u32 s76, s76, 0x3000\n"
"v_mfma_f32_16x16x32_bf16 v[132:135], v[100:103], v[252:255], v[116:119]\n"
"s_sub_u32 s61, s61, 1\n"
"s_cmp_lg_u32 s61, 0\n"
"s_cbranch_scc1 Lf1%=\n"
"s_waitcnt vmcnt(12)\n"
"v_mfma_f32_16x16x32_bf16 v[104:107], v[32:35], a[0:3], 0\n"
"v_mfma_f32_16x16x32_bf16 v[104:107], v[36:39], a[16:19], v[104:107]\n"
"buffer_load_dwordx4 v[92:95], v1, s[24:27], s76 offen sc1\n"
"v_mfma_f32_16x16x32_bf16 v[104:107], v[40:43], a[32:35], v[104:107]\n"
"s_waitcnt lgkmcnt(0)\n"
"v_fmac_f32 v120, v144, v148\n"
"v_fmac_f32 v121, v144, v149\n"
"v_mfma_f32_16x16x32_bf16 v[108:111], v[32:35], a[4:7], 0\n"
"v_fmac_f32 v122, v144, v150\n"
"v_fmac_f32 v123, v144, v151\n"
"v_mfma_f32_16x16x32_bf16 v[108:111], v[36:39], a[20:23], v[108:111]\n"
"v_cvt_pk_bf16_f32 v136, v120, v121\n"
"v_cvt_pk_bf16_f32 v137, v122, v123\n"
"v_mfma_f32_16x16x32_bf16 v[108:111], v[40:43], a[36:39], v[108:111]\n"
"buffer_load_dwordx4 v[96:99], v1, s[24:27], s76 offen offset:1024 sc1\n"
"v_mfma_f32_16x16x32_bf16 v[112:115], v[32:35], a[8:11], 0\n"
"v_pk_max_i16 v136, v136, 0\n"
"v_pk_max_i16 v137, v137, 0\n"
"v_mfma_f32_16x16x32_bf16 v[112:115], v[36:39], a[24:27], v[112:115]\n"
"ds_write_b64 v10, v[136:137]\n"
"v_mfma_f32_16x16x32_bf16 v[112:115], v[40:43], a[40:43], v[112:115]\n"
"v_fmac_f32 v124, v145, v148\n"
"v_fmac_f32 v125, v145, v149\n"
"v_mfma_f32_16x16x32_bf16 v[116:119], v[32:35], a[12:15], 0\n"
"buffer_load_dwordx4 v[100:103], v1, s[24:27], s76 offen offset:2048 sc1\n"
"v_mfma_f32_16x16x32_bf16 v[116:119], v[36:39], a[28:31], v[116:119]\n"
"v_fmac_f32 v126, v145, v150\n"
"v_fmac_f32 v127, v145, v151\n"
"s_add_u32 s76, s76, 0x3000\n"
"v_mfma_f32_16x16x32_bf16 v[116:119], v[40:43], a[44:47], v[116:119]\n"
"v_cvt_pk_bf16_f32 v138, v124, v125\n"
"v_cvt_pk_bf16_f32 v139, v126, v127\n"
"s_waitcnt vmcnt(12)\n"
"v_mfma_f32_16x16x32_bf16 v[104:107], v[44:47], a[48:51], v[104:107]\n"
"v_pk_max_i16 v138, v138, 0\n"
"v_pk_max_i16 v139, v139, 0\n"
"v_mfma_f32_16x16x32_bf16 v[104:107], v[48:51], a[64:67], v[104:107]\n"
"buffer_load_dwordx4 v[32:35], v1, s[24:27], s76 offen sc1\n"
"v_mfma_f32_16x16x32_bf16 v[104:107], v[52:55], a[80:83], v[104:107]\n"
"ds_write_b64 v10, v[138:139] offset:18432\n"
"v_mfma_f32_16x16x32_bf16 v[108:111], v[44:47], a[52:55], v[108:111]\n"
"v_fmac_f32 v128, v146, v148\n"
"v_fmac_f32 v129, v146, v149\n"
"v_mfma_f32_16x16x32_bf16 v[108:111], v[48:51], a[68:71], v[108:111]\n"
"v_fmac_f32 v130, v146, v150\n"
"v_fmac_f32 v131, v146, v151\n"
"v_mfma_f32_16x16x32_bf16 v[108:111], v[52:55], a[84:87], v[108:111]\n"
"buffer_load_dwordx4 v[36:39], v1, s[24:27], s76 offen offset:1024 sc1\n"
"v_mfma_f32_16x16x32_bf16 v[112:115], v[44:47], a[56:59], v[112:115]\n"
"v_cvt_pk_bf16_f32 v140, v128, v129\n"
"v_cvt_pk_bf16_f32 v141, v130, v131\n"
"v_mfma_f32_16x16x32_bf16 v[112:115], v[48:51], a[72:75], v[112:115]\n"
"v_pk_max_i16 v140, v140, 0\n"
"v_pk_max_i16 v141, v141, 0\n"
"v_mfma_f32_16x16x32_bf16 v[112:115], v[52:55], a[88:91], v[112:115]\n"
"ds_write_b64 v10, v[140:141] offset:36864\n"
"v_mfma_f32_16x16x32_bf16 v[116:119], v[44:47], a[60:63], v[116:119]\n"
"buffer_load_dwordx4 v[40:43], v1, s[24:27], s76 offen offset:2048 sc1\n"
"v_mfma_f32_16x16x32_bf16 v[116:119], v[48:51], a[76:79], v[116:119]\n"
"v_fmac_f32 v132, v147, v148\n"
"v_fmac_f32 v133, v147, v149\n"
"s_add_u32 s76, s76, 0x3000\n"
"v_mfma_f32_16x16x32_bf16 v[116:119], v[52:55], a[92:95], v[116:119]\n"
"v_fmac_f32 v134, v147, v150\n"
"v_fmac_f32 v135, v147, v151\n"
"s_waitcnt vmcnt(12)\n"
"v_mfma_f32_16x16x32_bf16 v[104:107], v[56:59], a[96:99], v[104:107]\n"
"v_cvt_pk_bf16_f32 v142, v132, v133\n"
"v_cvt_pk_bf16_f32 v143, v134, v135\n"
"v_mfma_f32_16x16x32_bf16 v[104:107], v[60:63], a[112:115], v[104:107]\n"
"buffer_load_dwordx4 v[44:47], v1, s[24:27], s76 offen sc1\n"
"v_mfma_f32_16x16x32_bf16 v[104:107], v[64:67], a[128:131], v[104:107]\n"
"v_pk_max_i16 v142, v142, 0\n"
"v_pk_max_i16 v143, v143, 0\n"
"v_mfma_f32_16x16x32_bf16 v[108:111], v[56:59], a[100:103], v[108:111]\n"
"ds_write_b64 v10, v[142:143] offset:55296\n"
"v_mfma_f32_16x16x32_bf16 v[108:111], v[60:63], a[116:119], v[108:111]\n"
"v_add_u32 v10, 0x800, v10\n"
"v_mfma_f32_16x16x32_bf16 v[108:111], v[64:67], a[132:135], v[108:111]\n"
"buffer_load_dwordx4 v[48:51], v1, s[24:27], s76 offen offset:1024 sc1\n"
"v_mfma_f32_16x16x32_bf16 v[112:115], v[56:59], a[104:107], v[112:115]\n"
"s_waitcnt lgkmcnt(0)\n"
"s_barrier\n"
"ds_read_b128 a[0:3], v3\n"
"ds_read_b128 a[16:19], v3 offset:1024\n"
"v_mfma_f32_16x16x32_bf16 v[112:115], v[60:63], a[120:123], v[112:115]\n"
"ds_read_b128 a[32:35], v3 offset:2048\n"
"v_mfma_f32_16x16x32_bf16 v[112:115], v[64:67], a[136:139], v[112:115]\n"
"ds_read_b128 a[4:7], v3 offset:18432\n"
"v_mfma_f32_16x16x32_bf16 v[116:119], v[56:59], a[108:111], v[116:119]\n"
"ds_read_b128 a[20:23], v3 offset:19456\n"
"buffer_load_dwordx4 v[52:55], v1, s[24:27], s76 offen offset:2048 sc1\n"
"v_mfma_f32_16x16x32_bf16 v[116:119], v[60:63], a[124:127], v[116:119]\n"
"ds_read_b128 a[36:39], v3 offset:20480\n"
"s_add_u32 s76, s76, 0x3000\n"
"v_mfma_f32_16x16x32_bf16 v[116:119], v[64:67], a[140:143], v[116:119]\n"
"ds_read_b128 a[8:11], v4\n"
"s_waitcnt vmcnt(12)\n"
"v_mfma_f32_16x16x32_bf16 v[104:107], v[68:71], a[144:147], v[104:107]\n"
"ds_read_b128 a[24:27], v4 offset:1024\n"
"v_mfma_f32_16x16x32_bf16 v[104:107], v[72:75], a[160:163], v[104:107]\n"
"ds_read_b128 a[40:43], v4 offset:2048\n"
"ds_read_b128 a[12:15], v4 offset:18432\n"
"buffer_load_dwordx4 v[56:59], v1, s[24:27], s76 offen sc1\n"
"v_mfma_f32_16x16x32_bf16 v[104:107], v[76:79], a[176:179], v[104:107]\n"
"ds_read_b128 a[28:31], v4 offset:19456\n"
"v_mfma_f32_16x16x32_bf16 v[108:111], v[68:71], a[148:151], v[108:111]\n"
"ds_read_b128 v[148:151], v11\n"
"ds_read_b128 a[44:47], v4 offset:20480\n"
"v_mfma_f32_16x16x32_bf16 v[108:111], v[72:75], a[164:167], v[108:111]\n"
"v_add_u32 v11, 64, v11\n"
"ds_read_b128 a[48:51], v3 offset:3072\n"
"v_mfma_f32_16x16x32_bf16 v[108:111], v[76:79], a[180:183], v[108:111]\n"
"ds_read_b128 a[64:67], v3 offset:4096\n"
"buffer_load_dwordx4 v[60:63], v1, s[24:27], s76 offen offset:1024 sc1\n"
"v_mfma_f32_16x16x32_bf16 v[112:115], v[68:71], a[152:155], v[112:115]\n"
"ds_read_b128 a[80:83], v3 offset:5120\n"
"v_mfma_f32_16x16x32_bf16 v[112:115], v[72:75], a[168:171], v[112:115]\n"
"ds_read_b128 a[52:55], v3 offset:21504\n"
"v_mfma_f32_16x16x32_bf16 v[112:115], v[76:79], a[184:187], v[112:115]\n"
"ds_read_b128 a[68:71], v3 offset:22528\n"
"ds_read_b128 a[84:87], v3 offset:23552\n"
"v_mfma_f32_16x16x32_bf16 v[116:119], v[68:71], a[156:159], v[116:119]\n"
"ds_read_b128 a[56:59], v4 offset:3072\n"
"buffer_load_dwordx4 v[64:67], v1, s[24:27], s76 offen offset:2048 sc1\n"
"v_mfma_f32_16x16x32_bf16 v[116:119], v[72:75], a[172:175], v[116:119]\n"
"ds_read_b128 a[72:75], v4 offset:4096\n"
"s_add_u32 s76, s76, 0x3000\n"
"v_mfma_f32_16x16x32_bf16 v[116:119], v[76:79], a[188:191], v[116:119]\n"
"ds_read_b128 a[88:91], v4 offset:5120\n"
"s_waitcnt vmcnt(12)\n"
"v_mfma_f32_16x16x32_bf16 v[104:107], v[80:83], a[192:195], v[104:107]\n"
"ds_read_b128 a[60:63], v4 offset:21504\n"
"v_mfma_f32_16x16x32_bf16 v[104:107], v[84:87], a[208:211], v[104:107]\n"
"ds_read_b128 a[76:79], v4 offset:22528\n"
"buffer_load_dwordx4 v[68:71], v1, s[24:27], s76 offen sc1\n"
"v_mfma_f32_16x16x32_bf16 v[104:107], v[88:91], a[224:227], v[104:107]\n"
"ds_read_b128 a[92:95], v4 offset:23552\n"
"v_mfma_f32_16x16x32_bf16 v[108:111], v[80:83], a[196:199], v[108:111]\n"
"ds_read_b128 a[96:99], v3 offset:6144\n"
"ds_read_b128 a[112:115], v3 offset:7168\n"
"v_mfma_f32_16x16x32_bf16 v[108:111], v[84:87], a[212:215], v[108:111]\n"
"ds_read_b128 a[128:131], v3 offset:8192\n"
"v_mfma_f32_16x16x32_bf16 v[108:111], v[88:91], a[228:231], v[108:111]\n"
"ds_read_b128 a[100:103], v3 offset:24576\n"
"buffer_load_dwordx4 v[72:75], v1, s[24:27], s76 offen offset:1024 sc1\n"
"v_mfma_f32_16x16x32_bf16 v[112:115], v[80:83], a[200:203], v[112:115]\n"
"ds_read_b128 a[116:119], v3 offset:25600\n"
"v_mfma_f32_16x16x32_bf16 v[112:115], v[84:87], a[216:219], v[112:115]\n"
"ds_read_b128 a[132:135], v3 offset:26624\n"
"v_mfma_f32_16x16x32_bf16 v[112:115], v[88:91], a[232:235], v[112:115]\n"
"ds_read_b128 a[104:107], v4 offset:6144\n"
"v_mfma_f32_16x16x32_bf16 v[116:119], v[80:83], a[204:207], v[116:119]\n"
"ds_read_b128 a[120:123], v4 offset:7168\n"
"buffer_load_dwordx4 v[76:79], v1, s[24:27], s76 offen offset:2048 sc1\n"
"v_mfma_f32_16x16x32_bf16 v[116:119], v[84:87], a[220:223], v[116:119]\n"
"ds_read_b128 a[136:139], v4 offset:8192\n"
"ds_read_b128 a[108:111], v4 offset:24576\n"
"s_add_u32 s76, s76, 0x3000\n"
"v_mfma_f32_16x16x32_bf16 v[116:119], v[88:91], a[236:239], v[116:119]\n"
"ds_read_b128 a[124:127], v4 offset:25600\n"
"s_waitcnt vmcnt(12)\n"
"v_mfma_f32_16x16x32_bf16 v[104:107], v[92:95], a[240:243], v[104:107]\n"
"ds_read_b128 a[140:143], v4 offset:26624\n"
"v_mfma_f32_16x16x32_bf16 v[104:107], v[96:99], v[224:227], v[104:107]\n"
"ds_read_b128 a[144:147], v3 offset:9216\n"
"buffer_load_dwordx4 v[80:83], v1, s[24:27], s76 offen sc1\n"
"v_mfma_f32_16x16x32_bf16 v[120:123], v[100:103], v[240:243], v[104:107]\n"
"ds_read_b128 a[160:163], v3 offset:10240\n"
"v_mfma_f32_16x16x32_bf16 v[108:111], v[92:95], a[244:247], v[108:111]\n"
"ds_read_b128 a[176:179], v3 offset:11264\n"
"v_mfma_f32_16x16x32_bf16 v[108:111], v[96:99], v[228:231], v[108:111]\n"
"ds_read_b128 a[148:151], v3 offset:27648\n"
"v_mfma_f32_16x16x32_bf16 v[124:127], v[100:103], v[244:247], v[108:111]\n"
"ds_read_b128 a[164:167], v3 offset:28672\n"
"ds_read_b128 a[180:183], v3 offset:29696\n"
"buffer_load_dwordx4 v[84:87], v1, s[24:27], s76 offen offset:1024 sc1\n"
"v_mfma_f32_16x16x32_bf16 v[112:115], v[92:95], a[248:251], v[112:115]\n"
"ds_read_b128 a[152:155], v4 offset:9216\n"
"v_mfma_f32_16x16x32_bf16 v[112:115], v[96:99], v[232:235], v[112:115]\n"
"ds_read_b128 a[168:171], v4 offset:10240\n"
"v_mfma_f32_16x16x32_bf16 v[128:131], v[100:103], v[248:251], v[112:115]\n"
"ds_read_b128 a[184:187], v4 offset:11264\n"
"v_mfma_f32_16x16x32_bf16 v[116:119], v[92:95], a[252:255], v[116:119]\n"
"ds_read_b128 a[156:159], v4 offset:27648\n"
"buffer_load_dwordx4 v[88:91], v1, s[24:27], s76 offen offset:2048 sc1\n"
"v_mfma_f32_16x16x32_bf16 v[116:119], v[96:99], v[236:239], v[116:119]\n"
"ds_read_b128 a[172:175], v4 offset:28672\n"
"s_add_u32 s76, s76, 0x3000\n"
"v_mfma_f32_16x16x32_bf16 v[132:135], v[100:103], v[252:255], v[116:119]\n"
"ds_read_b128 a[188:191], v4 offset:29696\n"
"s_waitcnt vmcnt(12) lgkmcnt(15)\n"
"v_mfma_f32_16x16x32_bf16 v[104:107], v[32:35], a[0:3], 0\n"
"v_mfma_f32_16x16x32_bf16 v[104:107], v[36:39], a[16:19], v[104:107]\n"
"buffer_load_dwordx4 v[92:95], v1, s[24:27], s76 offen sc1\n"
"v_mfma_f32_16x16x32_bf16 v[104:107], v[40:43], a[32:35], v[104:107]\n"
"v_fmac_f32 v120, v144, v148\n"
"v_fmac_f32 v121, v144, v149\n"
"v_mfma_f32_16x16x32_bf16 v[108:111], v[32:35], a[4:7], 0\n"
"v_fmac_f32 v122, v144, v150\n"
"v_fmac_f32 v123, v144, v151\n"
"v_mfma_f32_16x16x32_bf16 v[108:111], v[36:39], a[20:23], v[108:111]\n"
"v_cvt_pk_bf16_f32 v136, v120, v121\n"
"v_cvt_pk_bf16_f32 v137, v122, v123\n"
"v_mfma_f32_16x16x32_bf16 v[108:111], v[40:43], a[36:39], v[108:111]\n"
"buffer_load_dwordx4 v[96:99], v1, s[24:27], s76 offen offset:1024 sc1\n"
"v_mfma_f32_16x16x32_bf16 v[112:115], v[32:35], a[8:11], 0\n"
"v_pk_max_i16 v136, v136, 0\n"
"v_pk_max_i16 v137, v137, 0\n"
"v_mfma_f32_16x16x32_bf16 v[112:115], v[36:39], a[24:27], v[112:115]\n"
"ds_write_b64 v10, v[136:137]\n"
"v_mfma_f32_16x16x32_bf16 v[112:115], v[40:43], a[40:43], v[112:115]\n"
"v_fmac_f32 v124, v145, v148\n"
"v_fmac_f32 v125, v145, v149\n"
"v_mfma_f32_16x16x32_bf16 v[116:119], v[32:35], a[12:15], 0\n"
"buffer_load_dwordx4 v[100:103], v1, s[24:27], s76 offen offset:2048 sc1\n"
"v_mfma_f32_16x16x32_bf16 v[116:119], v[36:39], a[28:31], v[116:119]\n"
"v_fmac_f32 v126, v145, v150\n"
"v_fmac_f32 v127, v145, v151\n"
"s_add_u32 s76, s76, 0x3000\n"
"v_mfma_f32_16x16x32_bf16 v[116:119], v[40:43], a[44:47], v[116:119]\n"
"v_cvt_pk_bf16_f32 v138, v124, v125\n"
"v_cvt_pk_bf16_f32 v139, v126, v127\n"
"s_waitcnt vmcnt(12)\n"
"v_mfma_f32_16x16x32_bf16 v[104:107], v[44:47], a[48:51], v[104:107]\n"
"v_pk_max_i16 v138, v138, 0\n"
"v_pk_max_i16 v139, v139, 0\n"
"v_mfma_f32_16x16x32_bf16 v[104:107], v[48:51], a[64:67], v[104:107]\n"
"buffer_load_dwordx4 v[32:35], v1, s[24:27], s76 offen sc1\n"
"v_mfma_f32_16x16x32_bf16 v[104:107], v[52:55], a[80:83], v[104:107]\n"
"ds_write_b64 v10, v[138:139] offset:18432\n"
"v_mfma_f32_16x16x32_bf16 v[108:111], v[44:47], a[52:55], v[108:111]\n"
"v_fmac_f32 v128, v146, v148\n"
"v_fmac_f32 v129, v146, v149\n"
"v_mfma_f32_16x16x32_bf16 v[108:111], v[48:51], a[68:71], v[108:111]\n"
"v_fmac_f32 v130, v146, v150\n"
"v_fmac_f32 v131, v146, v151\n"
"v_mfma_f32_16x16x32_bf16 v[108:111], v[52:55], a[84:87], v[108:111]\n"
"buffer_load_dwordx4 v[36:39], v1, s[24:27], s76 offen offset:1024 sc1\n"
"v_mfma_f32_16x16x32_bf16 v[112:115], v[44:47], a[56:59], v[112:115]\n"
"v_cvt_pk_bf16_f32 v140, v128, v129\n"
"v_cvt_pk_bf16_f32 v141, v130, v131\n"
"v_mfma_f32_16x16x32_bf16 v[112:115], v[48:51], a[72:75], v[112:115]\n"
"v_pk_max_i16 v140, v140, 0\n"
"v_pk_max_i16 v141, v141, 0\n"
"v_mfma_f32_16x16x32_bf16 v[112:115], v[52:55], a[88:91], v[112:115]\n"
"ds_write_b64 v10, v[140:141] offset:36864\n"
"v_mfma_f32_16x16x32_bf16 v[116:119], v[44:47], a[60:63], v[116:119]\n"
"buffer_load_dwordx4 v[40:43], v1, s[24:27], s76 offen offset:2048 sc1\n"
"v_mfma_f32_16x16x32_bf16 v[116:119], v[48:51], a[76:79], v[116:119]\n"
"v_fmac_f32 v132, v147, v148\n"
"v_fmac_f32 v133, v147, v149\n"
"s_add_u32 s76, s76, 0x3000\n"
"v_mfma_f32_16x16x32_bf16 v[116:119], v[52:55], a[92:95], v[116:119]\n"
"v_fmac_f32 v134, v147, v150\n"
"v_fmac_f32 v135, v147, v151\n"
"s_waitcnt vmcnt(12) lgkmcnt(3)\n"
"v_mfma_f32_16x16x32_bf16 v[104:107], v[56:59], a[96:99], v[104:107]\n"
"v_cvt_pk_bf16_f32 v142, v132, v133\n"
"v_cvt_pk_bf16_f32 v143, v134, v135\n"
"v_mfma_f32_16x16x32_bf16 v[104:107], v[60:63], a[112:115], v[104:107]\n"
"buffer_load_dwordx4 v[44:47], v1, s[24:27], s76 offen sc1\n"
"v_mfma_f32_16x16x32_bf16 v[104:107], v[64:67], a[128:131], v[104:107]\n"
"v_pk_max_i16 v142, v142, 0\n"
"v_pk_max_i16 v143, v143, 0\n"
"v_mfma_f32_16x16x32_bf16 v[108:111], v[56:59], a[100:103], v[108:111]\n"
"ds_write_b64 v10, v[142:143] offset:55296\n"
"v_mfma_f32_16x16x32_bf16 v[108:111], v[60:63], a[116:119], v[108:111]\n"
"v_add_u32 v10, 0x800, v10\n"
"v_mfma_f32_16x16x32_bf16 v[108:111], v[64:67], a[132:135], v[108:111]\n"
"buffer_load_dwordx4 v[48:51], v1, s[24:27], s76 offen offset:1024 sc1\n"
"v_mfma_f32_16x16x32_bf16 v[112:115], v[56:59], a[104:107], v[112:115]\n"
"s_waitcnt lgkmcnt(0)\n"
"s_barrier\n"
"ds_read_b128 a[192:195], v3 offset:12288\n"
"v_mfma_f32_16x16x32_bf16 v[112:115], v[60:63], a[120:123], v[112:115]\n"
"ds_read_b128 a[208:211], v3 offset:13312\n"
"v_mfma_f32_16x16x32_bf16 v[112:115], v[64:67], a[136:139], v[112:115]\n"
"ds_read_b128 a[224:227], v3 offset:14336\n"
"v_mfma_f32_16x16x32_bf16 v[116:119], v[56:59], a[108:111], v[116:119]\n"
"ds_read_b128 a[196:199], v3 offset:30720\n"
"buffer_load_dwordx4 v[52:55], v1, s[24:27], s76 offen offset:2048 sc1\n"
"v_mfma_f32_16x16x32_bf16 v[116:119], v[60:63], a[124:127], v[116:119]\n"
"ds_read_b128 a[212:215], v3 offset:31744\n"
"s_add_u32 s76, s76, 0x3000\n"
"v_mfma_f32_16x16x32_bf16 v[116:119], v[64:67], a[140:143], v[116:119]\n"
"ds_read_b128 a[228:231], v3 offset:32768\n"
"s_waitcnt vmcnt(12)\n"
"v_mfma_f32_16x16x32_bf16 v[104:107], v[68:71], a[144:147], v[104:107]\n"
"ds_read_b128 a[200:203], v4 offset:12288\n"
"v_mfma_f32_16x16x32_bf16 v[104:107], v[72:75], a[160:163], v[104:107]\n"
"ds_read_b128 a[216:219], v4 offset:13312\n"
"buffer_load_dwordx4 v[56:59], v1, s[24:27], s76 offen sc1\n"
"v_mfma_f32_16x16x32_bf16 v[104:107], v[76:79], a[176:179], v[104:107]\n"
"ds_read_b128 a[232:235], v4 offset:14336\n"
"v_mfma_f32_16x16x32_bf16 v[108:111], v[68:71], a[148:151], v[108:111]\n"
"ds_read_b128 a[204:207], v4 offset:30720\n"
"v_mfma_f32_16x16x32_bf16 v[108:111], v[72:75], a[164:167], v[108:111]\n"
"ds_read_b128 a[220:223], v4 offset:31744\n"
"v_mfma_f32_16x16x32_bf16 v[108:111], v[76:79], a[180:183], v[108:111]\n"
"ds_read_b128 a[236:239], v4 offset:32768\n"
"buffer_load_dwordx4 v[60:63], v1, s[24:27], s76 offen offset:1024 sc1\n"
"v_mfma_f32_16x16x32_bf16 v[112:115], v[68:71], a[152:155], v[112:115]\n"
"ds_read_b128 a[240:243], v3 offset:15360\n"
"v_mfma_f32_16x16x32_bf16 v[112:115], v[72:75], a[168:171], v[112:115]\n"
"ds_read_b128 v[224:227], v3 offset:16384\n"
"v_mfma_f32_16x16x32_bf16 v[112:115], v[76:79], a[184:187], v[112:115]\n"
"ds_read_b128 v[240:243], v3 offset:17408\n"
"v_mfma_f32_16x16x32_bf16 v[116:119], v[68:71], a[156:159], v[116:119]\n"
"ds_read_b128 a[244:247], v3 offset:33792\n"
"buffer_load_dwordx4 v[64:67], v1, s[24:27], s76 offen offset:2048 sc1\n"
"v_mfma_f32_16x16x32_bf16 v[116:119], v[72:75], a[172:175], v[116:119]\n"
"ds_read_b128 v[228:231], v3 offset:34816\n"
"s_add_u32 s76, s76, 0x3000\n"
"v_mfma_f32_16x16x32_bf16 v[116:119], v[76:79], a[188:191], v[116:119]\n"
"ds_read_b128 v[244:247], v3 offset:35840\n"
"s_waitcnt vmcnt(12) lgkmcnt(6)\n"
"v_mfma_f32_16x16x32_bf16 v[104:107], v[80:83], a[192:195], v[104:107]\n"
"ds_read_b128 a[248:251], v4 offset:15360\n"
"v_mfma_f32_16x16x32_bf16 v[104:107], v[84:87], a[208:211], v[104:107]\n"
"ds_read_b128 v[232:235], v4 offset:16384\n"
"buffer_load_dwordx4 v[68:71], v1, s[24:27], s76 offen sc1\n"
"v_mfma_f32_16x16x32_bf16 v[104:107], v[88:91], a[224:227], v[104:107]\n"
"ds_read_b128 v[248:251], v4 offset:17408\n"
"v_mfma_f32_16x16x32_bf16 v[108:111], v[80:83], a[196:199], v[108:111]\n"
"ds_read_b128 a[252:255], v4 offset:33792\n"
"v_mfma_f32_16x16x32_bf16 v[108:111], v[84:87], a[212:215], v[108:111]\n"
"ds_read_b128 v[236:239], v4 offset:34816\n"
"v_mfma_f32_16x16x32_bf16 v[108:111], v[88:91], a[228:231], v[108:111]\n"
"ds_read_b128 v[252:255], v4 offset:35840\n"
"buffer_load_dwordx4 v[72:75], v1, s[24:27], s76 offen offset:1024 sc1\n"
"v_mfma_f32_16x16x32_bf16 v[112:115], v[80:83], a[200:203], v[112:115]\n"
"v_mfma_f32_16x16x32_bf16 v[112:115], v[84:87], a[216:219], v[112:115]\n"
"s_mul_i32 s82, s59, 0x1000000\n"
"s_mul_i32 s62, s57, 0x80000\n"
"v_mfma_f32_16x16x32_bf16 v[112:115], v[88:91], a[232:235], v[112:115]\n"
"s_add_u32 s82, s82, s62\n"
"s_lshl_b32 s62, s56, 11\n"
"v_mfma_f32_16x16x32_bf16 v[116:119], v[80:83], a[204:207], v[116:119]\n"
"buffer_load_dwordx4 v[76:79], v1, s[24:27], s76 offen offset:2048 sc1\n"
"v_mfma_f32_16x16x32_bf16 v[116:119], v[84:87], a[220:223], v[116:119]\n"
"s_add_u32 s76, s76, 0x3000\n"
"v_mfma_f32_16x16x32_bf16 v[116:119], v[88:91], a[236:239], v[116:119]\n"
"s_add_u32 s82, s82, s62\n"
"s_add_u32 s82, s82, 0x2260000\n"
"s_waitcnt vmcnt(12) lgkmcnt(0)\n"
"v_mfma_f32_16x16x32_bf16 v[104:107], v[92:95], a[240:243], v[104:107]\n"
"s_mul_i32 s83, s59, 0x200000\n"
"s_lshl_b32 s62, s57, 16\n"
"v_mfma_f32_16x16x32_bf16 v[104:107], v[96:99], v[224:227], v[104:107]\n"
"buffer_load_dwordx4 v[80:83], v1, s[24:27], s76 offen sc1\n"
"v_mfma_f32_16x16x32_bf16 v[120:123], v[100:103], v[240:243], v[104:107]\n"
"v_mfma_f32_16x16x32_bf16 v[108:111], v[92:95], a[244:247], v[108:111]\n"
"s_add_u32 s83, s83, s62\n"
"s_lshl_b32 s62, s56, 11\n"
"v_mfma_f32_16x16x32_bf16 v[108:111], v[96:99], v[228:231], v[108:111]\n"
"s_add_u32 s83, s83, s62\n"
"s_add_u32 s83, s83, 0x4260000\n"
"v_mfma_f32_16x16x32_bf16 v[124:127], v[100:103], v[244:247], v[108:111]\n"
"buffer_load_dwordx4 v[84:87], v1, s[24:27], s76 offen offset:1024 sc1\n"
"v_mfma_f32_16x16x32_bf16 v[112:115], v[92:95], a[248:251], v[112:115]\n"
"v_mfma_f32_16x16x32_bf16 v[112:115], v[96:99], v[232:235], v[112:115]\n"
"s_lshl_b32 s62, s58, 13\n"
"s_add_u32 s72, s83, s62\n"
"v_mfma_f32_16x16x32_bf16 v[128:131], v[100:103], v[248:251], v[112:115]\n"
"v_mfma_f32_16x16x32_bf16 v[116:119], v[92:95], a[252:255], v[116:119]\n"
"buffer_load_dwordx4 v[88:91], v1, s[24:27], s76 offen offset:2048 sc1\n"
"v_mfma_f32_16x16x32_bf16 v[116:119], v[96:99], v[236:239], v[116:119]\n"
"s_barrier\n"
"s_add_u32 s76, s76, 0x3000\n"
"v_mfma_f32_16x16x32_bf16 v[132:135], v[100:103], v[252:255], v[116:119]\n"
"s_waitcnt vmcnt(12)\n"
"v_mfma_f32_16x16x32_bf16 v[104:107], v[32:35], a[0:3], 0\n"
"v_mfma_f32_16x16x32_bf16 v[104:107], v[36:39], a[16:19], v[104:107]\n"
"buffer_load_dwordx4 v[92:95], v1, s[24:27], s76 offen sc1\n"
"v_mfma_f32_16x16x32_bf16 v[104:107], v[40:43], a[32:35], v[104:107]\n"
"v_mfma_f32_16x16x32_bf16 v[108:111], v[32:35], a[4:7], 0\n"
"v_cvt_pk_bf16_f32 v136, v120, v121\n"
"v_mfma_f32_16x16x32_bf16 v[108:111], v[36:39], a[20:23], v[108:111]\n"
"v_cvt_pk_bf16_f32 v137, v122, v123\n"
"v_mfma_f32_16x16x32_bf16 v[108:111], v[40:43], a[36:39], v[108:111]\n"
"buffer_load_dwordx4 v[96:99], v1, s[24:27], s76 offen offset:1024 sc1\n"
"v_mfma_f32_16x16x32_bf16 v[112:115], v[32:35], a[8:11], 0\n"
"v_mfma_f32_16x16x32_bf16 v[112:115], v[36:39], a[24:27], v[112:115]\n"
"v_cvt_pk_bf16_f32 v138, v124, v125\n"
"v_mfma_f32_16x16x32_bf16 v[112:115], v[40:43], a[40:43], v[112:115]\n"
"v_cvt_pk_bf16_f32 v139, v126, v127\n"
"v_mfma_f32_16x16x32_bf16 v[116:119], v[32:35], a[12:15], 0\n"
"buffer_load_dwordx4 v[100:103], v1, s[24:27], s76 offen offset:2048 sc1\n"
"v_mfma_f32_16x16x32_bf16 v[116:119], v[36:39], a[28:31], v[116:119]\n"
"s_add_u32 s76, s76, 0x3000\n"
"v_mfma_f32_16x16x32_bf16 v[116:119], v[40:43], a[44:47], v[116:119]\n"
"v_cvt_pk_bf16_f32 v140, v128, v129\n"
"s_waitcnt vmcnt(12)\n"
"v_mfma_f32_16x16x32_bf16 v[104:107], v[44:47], a[48:51], v[104:107]\n"
"v_permlane16_swap_b32 v136, v138\n"
"v_permlane16_swap_b32 v137, v139\n"
"v_mfma_f32_16x16x32_bf16 v[104:107], v[48:51], a[64:67], v[104:107]\n"
"buffer_load_dwordx4 v[32:35], v1, s[24:27], s76 offen sc1\n"
"v_mfma_f32_16x16x32_bf16 v[104:107], v[52:55], a[80:83], v[104:107]\n"
"v_mfma_f32_16x16x32_bf16 v[108:111], v[44:47], a[52:55], v[108:111]\n"
"v_cvt_pk_bf16_f32 v141, v130, v131\n"
"v_cvt_pk_bf16_f32 v142, v132, v133\n"
"v_mfma_f32_16x16x32_bf16 v[108:111], v[48:51], a[68:71], v[108:111]\n"
"v_cvt_pk_bf16_f32 v143, v134, v135\n"
"v_mfma_f32_16x16x32_bf16 v[108:111], v[52:55], a[84:87], v[108:111]\n"
"buffer_load_dwordx4 v[36:39], v1, s[24:27], s76 offen offset:1024 sc1\n"
"v_mfma_f32_16x16x32_bf16 v[112:115], v[44:47], a[56:59], v[112:115]\n"
"v_mfma_f32_16x16x32_bf16 v[112:115], v[48:51], a[72:75], v[112:115]\n"
"v_permlane16_swap_b32 v140, v142\n"
"v_permlane16_swap_b32 v141, v143\n"
"v_mfma_f32_16x16x32_bf16 v[112:115], v[52:55], a[88:91], v[112:115]\n"
"buffer_store_dwordx4 v[136:139], v1, s[24:27], s72 offen sc1\n"
"v_mfma_f32_16x16x32_bf16 v[116:119], v[44:47], a[60:63], v[116:119]\n"
"buffer_load_dwordx4 v[40:43], v1, s[24:27], s76 offen offset:2048 sc1\n"
"v_mfma_f32_16x16x32_bf16 v[116:119], v[48:51], a[76:79], v[116:119]\n"
"s_add_u32 s76, s76, 0x3000\n"
"v_mfma_f32_16x16x32_bf16 v[116:119], v[52:55], a[92:95], v[116:119]\n"
"s_waitcnt vmcnt(13)\n"
"v_mfma_f32_16x16x32_bf16 v[104:107], v[56:59], a[96:99], v[104:107]\n"
"v_mfma_f32_16x16x32_bf16 v[104:107], v[60:63], a[112:115], v[104:107]\n"
"buffer_load_dwordx4 v[44:47], v1, s[24:27], s76 offen sc1\n"
"v_mfma_f32_16x16x32_bf16 v[104:107], v[64:67], a[128:131], v[104:107]\n"
"v_mfma_f32_16x16x32_bf16 v[108:111], v[56:59], a[100:103], v[108:111]\n"
"v_mfma_f32_16x16x32_bf16 v[108:111], v[60:63], a[116:119], v[108:111]\n"
"v_mfma_f32_16x16x32_bf16 v[108:111], v[64:67], a[132:135], v[108:111]\n"
"buffer_load_dwordx4 v[48:51], v1, s[24:27], s76 offen offset:1024 sc1\n"
"v_mfma_f32_16x16x32_bf16 v[112:115], v[56:59], a[104:107], v[112:115]\n"
"v_mfma_f32_16x16x32_bf16 v[112:115], v[60:63], a[120:123], v[112:115]\n"
"v_mfma_f32_16x16x32_bf16 v[112:115], v[64:67], a[136:139], v[112:115]\n"
"v_mfma_f32_16x16x32_bf16 v[116:119], v[56:59], a[108:111], v[116:119]\n"
"buffer_load_dwordx4 v[52:55], v1, s[24:27], s76 offen offset:2048 sc1\n"
"v_mfma_f32_16x16x32_bf16 v[116:119], v[60:63], a[124:127], v[116:119]\n"
"s_add_u32 s76, s76, 0x3000\n"
"v_mfma_f32_16x16x32_bf16 v[116:119], v[64:67], a[140:143], v[116:119]\n"
"s_waitcnt vmcnt(13)\n"
"v_mfma_f32_16x16x32_bf16 v[104:107], v[68:71], a[144:147], v[104:107]\n"
"v_and_b32 v18, 15, v0\n"
"v_lshrrev_b32 v19, 5, v0\n"
"v_mfma_f32_16x16x32_bf16 v[104:107], v[72:75], a[160:163], v[104:107]\n"
"buffer_load_dwordx4 v[56:59], v1, s[24:27], s76 offen sc1\n"
"v_mfma_f32_16x16x32_bf16 v[104:107], v[76:79], a[176:179], v[104:107]\n"
"v_mfma_f32_16x16x32_bf16 v[108:111], v[68:71], a[148:151], v[108:111]\n"
"v_lshl_add_u32 v12, v19, 4, v18\n"
"v_bfe_u32 v19, v0, 4, 1\n"
"v_mfma_f32_16x16x32_bf16 v[108:111], v[72:75], a[164:167], v[108:111]\n"
"v_lshl_add_u32 v12, v12, 1, v19\n"
"s_lshl2_add_u32 s63, s58, s56\n"
"v_mfma_f32_16x16x32_bf16 v[108:111], v[76:79], a[180:183], v[108:111]\n"
"buffer_load_dwordx4 v[60:63], v1, s[24:27], s76 offen offset:1024 sc1\n"
"v_mfma_f32_16x16x32_bf16 v[112:115], v[68:71], a[152:155], v[112:115]\n"
"v_mfma_f32_16x16x32_bf16 v[112:115], v[72:75], a[168:171], v[112:115]\n"
"s_lshl_b32 s63, s63, 9\n"
"v_lshl_add_u32 v12, v12, 3, s63\n"
"v_mfma_f32_16x16x32_bf16 v[112:115], v[76:79], a[184:187], v[112:115]\n"
"v_lshlrev_b32 v11, 2, v18\n"
"v_add_u32 v11, 0x27000, v11\n"
"v_mfma_f32_16x16x32_bf16 v[116:119], v[68:71], a[156:159], v[116:119]\n"
"buffer_load_dwordx4 v[64:67], v1, s[24:27], s76 offen offset:2048 sc1\n"
"v_mfma_f32_16x16x32_bf16 v[116:119], v[72:75], a[172:175], v[116:119]\n"
"s_add_u32 s76, s76, 0x3000\n"
"v_mfma_f32_16x16x32_bf16 v[116:119], v[76:79], a[188:191], v[116:119]\n"
"v_lshl_add_u32 v222, v18, 1, v19\n"
"v_and_b32 v18, 8, v0\n"
"s_waitcnt vmcnt(13)\n"
"v_mfma_f32_16x16x32_bf16 v[104:107], v[80:83], a[192:195], v[104:107]\n"
"buffer_store_dwordx4 v[140:143], v1, s[24:27], s72 offen offset:1024 sc1\n"
"v_lshl_add_u32 v222, v18, 1, v222\n"
"v_mfma_f32_16x16x32_bf16 v[104:107], v[84:87], a[208:211], v[104:107]\n"
"buffer_load_dwordx4 v[68:71], v1, s[24:27], s76 offen sc1\n"
"v_mfma_f32_16x16x32_bf16 v[104:107], v[88:91], a[224:227], v[104:107]\n"
"v_mfma_f32_16x16x32_bf16 v[108:111], v[80:83], a[196:199], v[108:111]\n"
"s_add_u32 s77, s58, 1\n"
"s_and_b32 s77, s77, 7\n"
"v_mfma_f32_16x16x32_bf16 v[108:111], v[84:87], a[212:215], v[108:111]\n"
"s_lshl3_add_u32 s62, s77, s58\n"
"s_lshl_b32 s62, s62, 13\n"
"ds_read_b64 v[200:201], v12\n"
"v_mfma_f32_16x16x32_bf16 v[108:111], v[88:91], a[228:231], v[108:111]\n"
"ds_read_b64 v[202:203], v12 offset:18432\n"
"buffer_load_dwordx4 v[72:75], v1, s[24:27], s76 offen offset:1024 sc1\n"
"v_mfma_f32_16x16x32_bf16 v[112:115], v[80:83], a[200:203], v[112:115]\n"
"ds_read_b64 v[204:205], v12 offset:36864\n"
"v_mfma_f32_16x16x32_bf16 v[112:115], v[84:87], a[216:219], v[112:115]\n"
"s_add_u32 s72, s82, s62\n"
"v_mfma_f32_16x16x32_bf16 v[112:115], v[88:91], a[232:235], v[112:115]\n"
"ds_read_b64 v[206:207], v12 offset:55296\n"
"v_mfma_f32_16x16x32_bf16 v[116:119], v[80:83], a[204:207], v[116:119]\n"
"ds_read_b32 v192, v11\n"
"buffer_load_dwordx4 v[76:79], v1, s[24:27], s76 offen offset:2048 sc1\n"
"v_mfma_f32_16x16x32_bf16 v[116:119], v[84:87], a[220:223], v[116:119]\n"
"ds_read_b32 v193, v11 offset:64\n"
"s_add_u32 s76, s76, 0x3000\n"
"v_mfma_f32_16x16x32_bf16 v[116:119], v[88:91], a[236:239], v[116:119]\n"
"s_waitcnt vmcnt(14)\n"
"v_mfma_f32_16x16x32_bf16 v[104:107], v[92:95], a[240:243], v[104:107]\n"
"ds_read_b32 v194, v11 offset:128\n"
"v_mfma_f32_16x16x32_bf16 v[104:107], v[96:99], v[224:227], v[104:107]\n"
"ds_read_b32 v195, v11 offset:192\n"
"buffer_load_dwordx4 v[80:83], v1, s[24:27], s76 offen sc1\n"
"v_mfma_f32_16x16x32_bf16 v[120:123], v[100:103], v[240:243], v[104:107]\n"
"v_mfma_f32_16x16x32_bf16 v[108:111], v[92:95], a[244:247], v[108:111]\n"
"v_mfma_f32_16x16x32_bf16 v[108:111], v[96:99], v[228:231], v[108:111]\n"
"v_mfma_f32_16x16x32_bf16 v[124:127], v[100:103], v[244:247], v[108:111]\n"
"buffer_load_dwordx4 v[84:87], v1, s[24:27], s76 offen offset:1024 sc1\n"
"v_mfma_f32_16x16x32_bf16 v[112:115], v[92:95], a[248:251], v[112:115]\n"
"v_mfma_f32_16x16x32_bf16 v[112:115], v[96:99], v[232:235], v[112:115]\n"
"v_mfma_f32_16x16x32_bf16 v[128:131], v[100:103], v[248:251], v[112:115]\n"
"v_mfma_f32_16x16x32_bf16 v[116:119], v[92:95], a[252:255], v[116:119]\n"
"buffer_load_dwordx4 v[88:91], v1, s[24:27], s76 offen offset:2048 sc1\n"
"v_mfma_f32_16x16x32_bf16 v[116:119], v[96:99], v[236:239], v[116:119]\n"
"s_add_u32 s76, s76, 0x3000\n"
"v_mfma_f32_16x16x32_bf16 v[132:135], v[100:103], v[252:255], v[116:119]\n"
"s_waitcnt vmcnt(13)\n"
"v_mfma_f32_16x16x32_bf16 v[104:107], v[32:35], a[0:3], 0\n"
"v_mfma_f32_16x16x32_bf16 v[104:107], v[36:39], a[16:19], v[104:107]\n"
"buffer_load_dwordx4 v[92:95], v1, s[24:27], s76 offen sc1\n"
"v_mfma_f32_16x16x32_bf16 v[104:107], v[40:43], a[32:35], v[104:107]\n"
"v_mfma_f32_16x16x32_bf16 v[108:111], v[32:35], a[4:7], 0\n"
"v_cvt_pk_bf16_f32 v136, v120, v121\n"
"v_mfma_f32_16x16x32_bf16 v[108:111], v[36:39], a[20:23], v[108:111]\n"
"v_cvt_pk_bf16_f32 v137, v122, v123\n"
"v_mfma_f32_16x16x32_bf16 v[108:111], v[40:43], a[36:39], v[108:111]\n"
"buffer_load_dwordx4 v[96:99], v1, s[24:27], s76 offen offset:1024 sc1\n"
"v_mfma_f32_16x16x32_bf16 v[112:115], v[32:35], a[8:11], 0\n"
"v_mfma_f32_16x16x32_bf16 v[112:115], v[36:39], a[24:27], v[112:115]\n"
"v_cvt_pk_bf16_f32 v138, v124, v125\n"
"v_mfma_f32_16x16x32_bf16 v[112:115], v[40:43], a[40:43], v[112:115]\n"
"v_cvt_pk_bf16_f32 v139, v126, v127\n"
"v_mfma_f32_16x16x32_bf16 v[116:119], v[32:35], a[12:15], 0\n"
"buffer_load_dwordx4 v[100:103], v1, s[24:27], s76 offen offset:2048 sc1\n"
"v_mfma_f32_16x16x32_bf16 v[116:119], v[36:39], a[28:31], v[116:119]\n"
"s_add_u32 s76, s76, 0x3000\n"
"v_mfma_f32_16x16x32_bf16 v[116:119], v[40:43], a[44:47], v[116:119]\n"
"v_cvt_pk_bf16_f32 v140, v128, v129\n"
"s_waitcnt vmcnt(13)\n"
"v_mfma_f32_16x16x32_bf16 v[104:107], v[44:47], a[48:51], v[104:107]\n"
"buffer_store_dwordx4 v[136:139], v1, s[24:27], s72 offen sc1\n"
"v_mfma_f32_16x16x32_bf16 v[104:107], v[48:51], a[64:67], v[104:107]\n"
"buffer_load_dwordx4 v[32:35], v1, s[24:27], s76 offen sc1\n"
"v_mfma_f32_16x16x32_bf16 v[104:107], v[52:55], a[80:83], v[104:107]\n"
"v_mfma_f32_16x16x32_bf16 v[108:111], v[44:47], a[52:55], v[108:111]\n"
"v_cvt_pk_bf16_f32 v141, v130, v131\n"
"v_cvt_pk_bf16_f32 v142, v132, v133\n"
"v_mfma_f32_16x16x32_bf16 v[108:111], v[48:51], a[68:71], v[108:111]\n"
"v_cvt_pk_bf16_f32 v143, v134, v135\n"
"v_mfma_f32_16x16x32_bf16 v[108:111], v[52:55], a[84:87], v[108:111]\n"
"buffer_load_dwordx4 v[36:39], v1, s[24:27], s76 offen offset:1024 sc1\n"
"v_mfma_f32_16x16x32_bf16 v[112:115], v[44:47], a[56:59], v[112:115]\n"
"v_mfma_f32_16x16x32_bf16 v[112:115], v[48:51], a[72:75], v[112:115]\n"
"v_mfma_f32_16x16x32_bf16 v[112:115], v[52:55], a[88:91], v[112:115]\n"
"v_mfma_f32_16x16x32_bf16 v[116:119], v[44:47], a[60:63], v[116:119]\n"
"buffer_load_dwordx4 v[40:43], v1, s[24:27], s76 offen offset:2048 sc1\n"
"v_mfma_f32_16x16x32_bf16 v[116:119], v[48:51], a[76:79], v[116:119]\n"
"s_add_u32 s76, s76, 0x3000\n"
"v_mfma_f32_16x16x32_bf16 v[116:119], v[52:55], a[92:95], v[116:119]\n"
"s_waitcnt vmcnt(14)\n"
"v_mfma_f32_16x16x32_bf16 v[104:107], v[56:59], a[96:99], v[104:107]\n"
"s_waitcnt vmcnt(13)\n"
"s_bfm_b64 exec, 8, 0\n"
"buffer_atomic_add v27, v31, s[24:27], s21 offen offset:528 sc1\n"
"s_mov_b64 exec, -1\n"
"v_mfma_f32_16x16x32_bf16 v[104:107], v[60:63], a[112:115], v[104:107]\n"
"buffer_load_dwordx4 v[44:47], v1, s[24:27], s76 offen sc1\n"
"v_mfma_f32_16x16x32_bf16 v[104:107], v[64:67], a[128:131], v[104:107]\n"
"v_mfma_f32_16x16x32_bf16 v[108:111], v[56:59], a[100:103], v[108:111]\n"
"v_mfma_f32_16x16x32_bf16 v[108:111], v[60:63], a[116:119], v[108:111]\n"
"v_mfma_f32_16x16x32_bf16 v[108:111], v[64:67], a[132:135], v[108:111]\n"
"buffer_load_dwordx4 v[48:51], v1, s[24:27], s76 offen offset:1024 sc1\n"
"v_mfma_f32_16x16x32_bf16 v[112:115], v[56:59], a[104:107], v[112:115]\n"
"v_mfma_f32_16x16x32_bf16 v[112:115], v[60:63], a[120:123], v[112:115]\n"
"v_mfma_f32_16x16x32_bf16 v[112:115], v[64:67], a[136:139], v[112:115]\n"
"v_mfma_f32_16x16x32_bf16 v[116:119], v[56:59], a[108:111], v[116:119]\n"
"buffer_load_dwordx4 v[52:55], v1, s[24:27], s76 offen offset:2048 sc1\n"
"v_mfma_f32_16x16x32_bf16 v[116:119], v[60:63], a[124:127], v[116:119]\n"
"s_add_u32 s76, s76, 0x3000\n"
"v_mfma_f32_16x16x32_bf16 v[116:119], v[64:67], a[140:143], v[116:119]\n"
"s_waitcnt lgkmcnt(0)\n"
"s_waitcnt vmcnt(14)\n"
"v_mfma_f32_16x16x32_bf16 v[104:107], v[68:71], a[144:147], v[104:107]\n"
"v_lshlrev_b32 v176, 16, v200\n"
"v_and_b32 v177, 0xffff0000, v200\n"
"v_mfma_f32_16x16x32_bf16 v[104:107], v[72:75], a[160:163], v[104:107]\n"
"buffer_load_dwordx4 v[56:59], v1, s[24:27], s76 offen sc1\n"
"v_mfma_f32_16x16x32_bf16 v[104:107], v[76:79], a[176:179], v[104:107]\n"
"v_mfma_f32_16x16x32_bf16 v[108:111], v[68:71], a[148:151], v[108:111]\n"
"v_lshlrev_b32 v178, 16, v201\n"
"v_and_b32 v179, 0xffff0000, v201\n"
"v_mfma_f32_16x16x32_bf16 v[108:111], v[72:75], a[164:167], v[108:111]\n"
"v_lshlrev_b32 v180, 16, v202\n"
"v_and_b32 v181, 0xffff0000, v202\n"
"v_mfma_f32_16x16x32_bf16 v[108:111], v[76:79], a[180:183], v[108:111]\n"
"buffer_load_dwordx4 v[60:63], v1, s[24:27], s76 offen offset:1024 sc1\n"
"v_mfma_f32_16x16x32_bf16 v[112:115], v[68:71], a[152:155], v[112:115]\n"
"v_mfma_f32_16x16x32_bf16 v[112:115], v[72:75], a[168:171], v[112:115]\n"
"v_lshlrev_b32 v182, 16, v203\n"
"v_and_b32 v183, 0xffff0000, v203\n"
"v_mfma_f32_16x16x32_bf16 v[112:115], v[76:79], a[184:187], v[112:115]\n"
"v_lshlrev_b32 v184, 16, v204\n"
"v_and_b32 v185, 0xffff0000, v204\n"
"v_mfma_f32_16x16x32_bf16 v[116:119], v[68:71], a[156:159], v[116:119]\n"
"buffer_load_dwordx4 v[64:67], v1, s[24:27], s76 offen offset:2048 sc1\n"
"v_mfma_f32_16x16x32_bf16 v[116:119], v[72:75], a[172:175], v[116:119]\n"
"s_add_u32 s76, s76, 0x3000\n"
"v_mfma_f32_16x16x32_bf16 v[116:119], v[76:79], a[188:191], v[116:119]\n"
"v_lshlrev_b32 v186, 16, v205\n"
"v_and_b32 v187, 0xffff0000, v205\n"
"s_waitcnt vmcnt(14)\n"
"v_mfma_f32_16x16x32_bf16 v[104:107], v[80:83], a[192:195], v[104:107]\n"
"buffer_store_dwordx4 v[140:143], v1, s[24:27], s72 offen offset:1024 sc1\n"
"v_lshlrev_b32 v188, 16, v206\n"
"v_and_b32 v189, 0xffff0000, v206\n"
"v_mfma_f32_16x16x32_bf16 v[104:107], v[84:87], a[208:211], v[104:107]\n"
"buffer_load_dwordx4 v[68:71], v1, s[24:27], s76 offen sc1\n"
"v_mfma_f32_16x16x32_bf16 v[104:107], v[88:91], a[224:227], v[104:107]\n"
"v_mfma_f32_16x16x32_bf16 v[108:111], v[80:83], a[196:199], v[108:111]\n"
"s_lshl_b32 s62, s77, 11\n"
"s_add_u32 s74, s21, s62\n"
"v_lshlrev_b32 v190, 16, v207\n"
"v_and_b32 v191, 0xffff0000, v207\n"
"v_mfma_f32_16x16x32_bf16 v[108:111], v[84:87], a[212:215], v[108:111]\n"
"s_add_u32 s77, s58, 2\n"
"s_and_b32 s77, s77, 7\n"
"v_mfma_f32_16x16x32_bf16 v[108:111], v[88:91], a[228:231], v[108:111]\n"
"buffer_load_dwordx4 v[72:75], v1, s[24:27], s76 offen offset:1024 sc1\n"
"v_mfma_f32_16x16x32_bf16 v[112:115], v[80:83], a[200:203], v[112:115]\n"
"v_mfma_f32_16x16x32_bf16 v[112:115], v[84:87], a[216:219], v[112:115]\n"
"s_lshl3_add_u32 s62, s77, s58\n"
"s_lshl_b32 s62, s62, 13\n"
"v_mfma_f32_16x16x32_bf16 v[112:115], v[88:91], a[232:235], v[112:115]\n"
"s_add_u32 s72, s82, s62\n"
"v_mfma_f32_16x16x32_bf16 v[116:119], v[80:83], a[204:207], v[116:119]\n"
"buffer_load_dwordx4 v[76:79], v1, s[24:27], s76 offen offset:2048 sc1\n"
"v_mfma_f32_16x16x32_bf16 v[116:119], v[84:87], a[220:223], v[116:119]\n"
"s_add_u32 s76, s76, 0x3000\n"
"v_mfma_f32_16x16x32_bf16 v[116:119], v[88:91], a[236:239], v[116:119]\n"
"s_waitcnt vmcnt(15)\n"
"v_mfma_f32_16x16x32_bf16 v[104:107], v[92:95], a[240:243], v[104:107]\n"
"v_mfma_f32_16x16x32_bf16 v[104:107], v[96:99], v[224:227], v[104:107]\n"
"buffer_load_dwordx4 v[80:83], v1, s[24:27], s76 offen sc1\n"
"v_mfma_f32_16x16x32_bf16 v[120:123], v[100:103], v[240:243], v[104:107]\n"
"v_mfma_f32_16x16x32_bf16 v[108:111], v[92:95], a[244:247], v[108:111]\n"
"v_mfma_f32_16x16x32_bf16 v[108:111], v[96:99], v[228:231], v[108:111]\n"
"v_mfma_f32_16x16x32_bf16 v[124:127], v[100:103], v[244:247], v[108:111]\n"
"buffer_load_dwordx4 v[84:87], v1, s[24:27], s76 offen offset:1024 sc1\n"
"v_mfma_f32_16x16x32_bf16 v[112:115], v[92:95], a[248:251], v[112:115]\n"
"v_mfma_f32_16x16x32_bf16 v[112:115], v[96:99], v[232:235], v[112:115]\n"
"v_mfma_f32_16x16x32_bf16 v[128:131], v[100:103], v[248:251], v[112:115]\n"
"v_mfma_f32_16x16x32_bf16 v[116:119], v[92:95], a[252:255], v[116:119]\n"
"buffer_load_dwordx4 v[88:91], v1, s[24:27], s76 offen offset:2048 sc1\n"
"v_mfma_f32_16x16x32_bf16 v[116:119], v[96:99], v[236:239], v[116:119]\n"
"s_add_u32 s76, s76, 0x3000\n"
"v_mfma_f32_16x16x32_bf16 v[132:135], v[100:103], v[252:255], v[116:119]\n"
"s_waitcnt vmcnt(14)\n"
"v_mfma_f32_16x16x32_bf16 v[104:107], v[32:35], a[0:3], 0\n"
"v_mfma_f32_16x16x32_bf16 v[104:107], v[36:39], a[16:19], v[104:107]\n"
"buffer_load_dwordx4 v[92:95], v1, s[24:27], s76 offen sc1\n"
"v_mfma_f32_16x16x32_bf16 v[104:107], v[40:43], a[32:35], v[104:107]\n"
"v_mfma_f32_16x16x32_bf16 v[108:111], v[32:35], a[4:7], 0\n"
"v_cvt_pk_bf16_f32 v136, v120, v121\n"
"s_lshl_b32 s64, s58, 5\n"
"v_mfma_f32_16x16x32_bf16 v[108:111], v[36:39], a[20:23], v[108:111]\n"
"v_cvt_pk_bf16_f32 v137, v122, v123\n"
"s_add_u32 s64, s64, 0x27000\n"
"v_mfma_f32_16x16x32_bf16 v[108:111], v[40:43], a[36:39], v[108:111]\n"
"buffer_load_dwordx4 v[96:99], v1, s[24:27], s76 offen offset:1024 sc1\n"
"v_mfma_f32_16x16x32_bf16 v[112:115], v[32:35], a[8:11], 0\n"
"v_mfma_f32_16x16x32_bf16 v[112:115], v[36:39], a[24:27], v[112:115]\n"
"v_cvt_pk_bf16_f32 v138, v124, v125\n"
"v_and_b32 v11, 7, v0\n"
"v_mfma_f32_16x16x32_bf16 v[112:115], v[40:43], a[40:43], v[112:115]\n"
"v_cvt_pk_bf16_f32 v139, v126, v127\n"
"s_mov_b64 exec, 1\n"
"v_mov_b32 v16, 0\n"
"buffer_atomic_add v16, off, s[24:27], s75 offset:528 sc0 sc1\n"
"s_mov_b64 exec, -1\n"
"v_lshl_add_u32 v11, v11, 2, s64\n"
"v_mfma_f32_16x16x32_bf16 v[116:119], v[32:35], a[12:15], 0\n"
"buffer_load_dwordx4 v[100:103], v1, s[24:27], s76 offen offset:2048 sc1\n"
"v_mfma_f32_16x16x32_bf16 v[116:119], v[36:39], a[28:31], v[116:119]\n"
"s_add_u32 s76, s76, 0x3000\n"
"v_mfma_f32_16x16x32_bf16 v[116:119], v[40:43], a[44:47], v[116:119]\n"
"v_cvt_pk_bf16_f32 v140, v128, v129\n"
"s_lshr_b32 s63, s58, 1\n"
"s_waitcnt vmcnt(14)\n"
"v_mfma_f32_16x16x32_bf16 v[104:107], v[44:47], a[48:51], v[104:107]\n"
"buffer_store_dwordx4 v[136:139], v1, s[24:27], s72 offen sc1\n"
"s_mul_i32 s63, s63, 0x90\n"
"v_mfma_f32_16x16x32_bf16 v[104:107], v[48:51], a[64:67], v[104:107]\n"
"buffer_load_dwordx4 v[32:35], v1, s[24:27], s76 offen sc1\n"
"v_mfma_f32_16x16x32_bf16 v[104:107], v[52:55], a[80:83], v[104:107]\n"
"v_mfma_f32_16x16x32_bf16 v[108:111], v[44:47], a[52:55], v[108:111]\n"
"v_cvt_pk_bf16_f32 v141, v130, v131\n"
"v_cvt_pk_bf16_f32 v142, v132, v133\n"
"s_lshl2_add_u32 s63, s56, s63\n"
"v_mfma_f32_16x16x32_bf16 v[108:111], v[48:51], a[68:71], v[108:111]\n"
"v_cvt_pk_bf16_f32 v143, v134, v135\n"
"s_and_b32 s64, s58, 1\n"
"v_mfma_f32_16x16x32_bf16 v[108:111], v[52:55], a[84:87], v[108:111]\n"
"buffer_load_dwordx4 v[36:39], v1, s[24:27], s76 offen offset:1024 sc1\n"
"v_mfma_f32_16x16x32_bf16 v[112:115], v[44:47], a[56:59], v[112:115]\n"
"v_mfma_f32_16x16x32_bf16 v[112:115], v[48:51], a[72:75], v[112:115]\n"
"s_add_u32 s63, s63, s64\n"
"v_mfma_f32_16x16x32_bf16 v[112:115], v[52:55], a[88:91], v[112:115]\n"
"s_add_u32 s63, s63, 0x80\n"
"v_mfma_f32_16x16x32_bf16 v[116:119], v[44:47], a[60:63], v[116:119]\n"
"buffer_load_dwordx4 v[40:43], v1, s[24:27], s76 offen offset:2048 sc1\n"
"v_mfma_f32_16x16x32_bf16 v[116:119], v[48:51], a[76:79], v[116:119]\n"
"s_add_u32 s76, s76, 0x3000\n"
"v_mfma_f32_16x16x32_bf16 v[116:119], v[52:55], a[92:95], v[116:119]\n"
"s_lshl_b32 s63, s63, 7\n"
"s_waitcnt vmcnt(15)\n"
"v_mfma_f32_16x16x32_bf16 v[104:107], v[56:59], a[96:99], v[104:107]\n"
"s_waitcnt vmcnt(14)\n"
"s_mov_b64 exec, 1\n"
"buffer_store_dword v27, off, s[24:27], s74 offset:532 sc1\n"
"s_mov_b64 exec, -1\n"
"v_lshl_add_u32 v222, v222, 3, s63\n"
"v_mfma_f32_16x16x32_bf16 v[104:107], v[60:63], a[112:115], v[104:107]\n"
"buffer_load_dwordx4 v[44:47], v1, s[24:27], s76 offen sc1\n"
"v_mfma_f32_16x16x32_bf16 v[104:107], v[64:67], a[128:131], v[104:107]\n"
"v_mfma_f32_16x16x32_bf16 v[108:111], v[56:59], a[100:103], v[108:111]\n"
"v_mfma_f32_16x16x32_bf16 v[108:111], v[60:63], a[116:119], v[108:111]\n"
"v_mfma_f32_16x16x32_bf16 v[108:111], v[64:67], a[132:135], v[108:111]\n"
"buffer_load_dwordx4 v[48:51], v1, s[24:27], s76 offen offset:1024 sc1\n"
"v_mfma_f32_16x16x32_bf16 v[112:115], v[56:59], a[104:107], v[112:115]\n"
"ds_read_b64 v[200:201], v222\n"
"v_mfma_f32_16x16x32_bf16 v[112:115], v[60:63], a[120:123], v[112:115]\n"
"v_and_b32 v18, 0x70, v1\n"
"v_lshrrev_b32 v19, 4, v0\n"
"v_mfma_f32_16x16x32_bf16 v[112:115], v[64:67], a[136:139], v[112:115]\n"
"v_lshl_add_u32 v18, v19, 13, v18\n"
"v_and_b32 v19, 8, v0\n"
"v_mfma_f32_16x16x32_bf16 v[116:119], v[56:59], a[108:111], v[116:119]\n"
"buffer_load_dwordx4 v[52:55], v1, s[24:27], s76 offen offset:2048 sc1\n"
"v_mfma_f32_16x16x32_bf16 v[116:119], v[60:63], a[124:127], v[116:119]\n"
"ds_read_b32 v212, v11\n"
"s_add_u32 s76, s76, 0x3000\n"
"v_mfma_f32_16x16x32_bf16 v[116:119], v[64:67], a[140:143], v[116:119]\n"
"v_lshl_add_u32 v18, v19, 6, v18\n"
"s_and_b32 s63, s58, 4\n"
"s_waitcnt vmcnt(15)\n"
"v_mfma_f32_16x16x32_bf16 v[104:107], v[68:71], a[144:147], v[104:107]\n"
"s_add_u32 s63, s63, s58\n"
"s_lshl_b32 s63, s63, 7\n"
"v_mfma_f32_16x16x32_bf16 v[104:107], v[72:75], a[160:163], v[104:107]\n"
"buffer_load_dwordx4 v[56:59], v1, s[24:27], s76 offen sc1\n"
"v_mfma_f32_16x16x32_bf16 v[104:107], v[76:79], a[176:179], v[104:107]\n"
"v_mfma_f32_16x16x32_bf16 v[108:111], v[68:71], a[148:151], v[108:111]\n"
"s_add_u32 s63, s63, s83\n"
"s_add_u32 s64, s63, 0x8000\n"
"v_mfma_f32_16x16x32_bf16 v[108:111], v[72:75], a[164:167], v[108:111]\n"
"v_mfma_f32_16x16x32_bf16 v[108:111], v[76:79], a[180:183], v[108:111]\n"
"buffer_load_dwordx4 v[60:63], v1, s[24:27], s76 offen offset:1024 sc1\n"
"v_mfma_f32_16x16x32_bf16 v[112:115], v[68:71], a[152:155], v[112:115]\n"
"v_mfma_f32_16x16x32_bf16 v[112:115], v[72:75], a[168:171], v[112:115]\n"
"v_mfma_f32_16x16x32_bf16 v[112:115], v[76:79], a[184:187], v[112:115]\n"
"v_mfma_f32_16x16x32_bf16 v[116:119], v[68:71], a[156:159], v[116:119]\n"
"buffer_load_dwordx4 v[64:67], v1, s[24:27], s76 offen offset:2048 sc1\n"
"v_mfma_f32_16x16x32_bf16 v[116:119], v[72:75], a[172:175], v[116:119]\n"
"s_waitcnt vmcnt(12)\n"
"v_readfirstlane_b32 vcc_lo, v16\n"
"s_nop 0\n"
"s_cmp_ge_u32 vcc_lo, 8\n"
"s_cbranch_scc1 Lc0_d%=\n"
"Lc0%=:\n"
"s_mov_b64 exec, 1\n"
"v_mov_b32 v16, 0\n"
"buffer_atomic_add v16, off, s[24:27], s75 offset:528 sc0 sc1\n"
"s_mov_b64 exec, -1\n"
"s_waitcnt vmcnt(0)\n"
"v_readfirstlane_b32 vcc_lo, v16\n"
"s_nop 0\n"
"s_cmp_ge_u32 vcc_lo, 8\n"
"s_cbranch_scc1 Lc0_d%=\n"
"s_sleep 2\n"
"s_sub_u32 s60, s60, 1\n"
"s_cbranch_scc0 Lc0%=\n"
"s_mov_b32 s60, 0\n"
"Lc0_d%=:\n"
"s_add_u32 s76, s76, 0x3000\n"
"v_mfma_f32_16x16x32_bf16 v[116:119], v[76:79], a[188:191], v[116:119]\n"
"buffer_load_dwordx4 v[168:171], v18, s[24:27], s63 offen sc1\n"
"v_mfma_f32_16x16x32_bf16 v[104:107], v[80:83], a[192:195], v[104:107]\n"
"buffer_store_dwordx4 v[140:143], v1, s[24:27], s72 offen offset:1024 sc1\n"
"v_mfma_f32_16x16x32_bf16 v[104:107], v[84:87], a[208:211], v[104:107]\n"
"buffer_load_dwordx4 v[68:71], v1, s[24:27], s76 offen sc1\n"
"v_mfma_f32_16x16x32_bf16 v[104:107], v[88:91], a[224:227], v[104:107]\n"
"v_mfma_f32_16x16x32_bf16 v[108:111], v[80:83], a[196:199], v[108:111]\n"
"s_lshl_b32 s62, s77, 11\n"
"s_add_u32 s74, s21, s62\n"
"buffer_load_dwordx4 v[172:175], v18, s[24:27], s64 offen sc1\n"
"v_mfma_f32_16x16x32_bf16 v[108:111], v[84:87], a[212:215], v[108:111]\n"
"s_add_u32 s77, s58, 3\n"
"s_and_b32 s77, s77, 7\n"
"s_waitcnt lgkmcnt(0)\n"
"v_lshlrev_b32 v208, 16, v200\n"
"v_and_b32 v209, 0xffff0000, v200\n"
"v_mfma_f32_16x16x32_bf16 v[108:111], v[88:91], a[228:231], v[108:111]\n"
"buffer_load_dwordx4 v[72:75], v1, s[24:27], s76 offen offset:1024 sc1\n"
"v_mfma_f32_16x16x32_bf16 v[112:115], v[80:83], a[200:203], v[112:115]\n"
"v_mfma_f32_16x16x32_bf16 v[112:115], v[84:87], a[216:219], v[112:115]\n"
"s_lshl3_add_u32 s62, s77, s58\n"
"s_lshl_b32 s62, s62, 13\n"
"v_lshlrev_b32 v210, 16, v201\n"
"v_and_b32 v211, 0xffff0000, v201\n"
"v_mfma_f32_16x16x32_bf16 v[112:115], v[88:91], a[232:235], v[112:115]\n"
"s_add_u32 s72, s82, s62\n"
"v_and_b32 v19, 1, v0\n"
"v_lshlrev_b32 v19, 4, v19\n"
"v_mfma_f32_16x16x32_bf16 v[116:119], v[80:83], a[204:207], v[116:119]\n"
"buffer_load_dwordx4 v[76:79], v1, s[24:27], s76 offen offset:2048 sc1\n"
"v_mfma_f32_16x16x32_bf16 v[116:119], v[84:87], a[220:223], v[116:119]\n"
"s_add_u32 s76, s76, 0x3000\n"
"v_mfma_f32_16x16x32_bf16 v[116:119], v[88:91], a[236:239], v[116:119]\n"
"v_mov_b32 v217, 0x3f80\n"
"v_lshlrev_b32 v19, v19, v217\n"
"s_waitcnt vmcnt(17)\n"
"v_mfma_f32_16x16x32_bf16 v[104:107], v[92:95], a[240:243], v[104:107]\n"
"v_mfma_f32_16x16x32_bf16 v[104:107], v[96:99], v[224:227], v[104:107]\n"
"buffer_load_dwordx4 v[80:83], v1, s[24:27], s76 offen sc1\n"
"v_mfma_f32_16x16x32_bf16 v[120:123], v[100:103], v[240:243], v[104:107]\n"
"v_mfma_f32_16x16x32_bf16 v[108:111], v[92:95], a[244:247], v[108:111]\n"
"v_mfma_f32_16x16x32_bf16 v[108:111], v[96:99], v[228:231], v[108:111]\n"
"v_mfma_f32_16x16x32_bf16 v[124:127], v[100:103], v[244:247], v[108:111]\n"
"buffer_load_dwordx4 v[84:87], v1, s[24:27], s76 offen offset:1024 sc1\n"
"v_mfma_f32_16x16x32_bf16 v[112:115], v[92:95], a[248:251], v[112:115]\n"
"v_mfma_f32_16x16x32_bf16 v[112:115], v[96:99], v[232:235], v[112:115]\n"
"v_mfma_f32_16x16x32_bf16 v[128:131], v[100:103], v[248:251], v[112:115]\n"
"v_mfma_f32_16x16x32_bf16 v[116:119], v[92:95], a[252:255], v[116:119]\n"
"buffer_load_dwordx4 v[88:91], v1, s[24:27], s76 offen offset:2048 sc1\n"
"v_mfma_f32_16x16x32_bf16 v[116:119], v[96:99], v[236:239], v[116:119]\n"
"s_add_u32 s76, s76, 0x3000\n"
"v_mfma_f32_16x16x32_bf16 v[132:135], v[100:103], v[252:255], v[116:119]\n"
"s_waitcnt vmcnt(16)\n"
"v_mfma_f32_16x16x32_bf16 v[104:107], v[32:35], a[0:3], 0\n"
"v_mfma_f32_16x16x32_bf16 v[104:107], v[36:39], a[16:19], v[104:107]\n"
"buffer_load_dwordx4 v[92:95], v1, s[24:27], s76 offen sc1\n"
"v_mfma_f32_16x16x32_bf16 v[104:107], v[40:43], a[32:35], v[104:107]\n"
"s_mov_b32 vcc_lo, 0x30003\n"
"s_mov_b32 vcc_hi, 0x30003\n"
"v_mfma_f32_16x16x32_bf16 v[108:111], v[32:35], a[4:7], 0\n"
"v_cvt_pk_bf16_f32 v136, v120, v121\n"
"v_mfma_f32_16x16x32_bf16 v[108:111], v[36:39], a[20:23], v[108:111]\n"
"v_cvt_pk_bf16_f32 v137, v122, v123\n"
"v_cndmask_b32 v214, 0, v19, vcc\n"
"v_mfma_f32_16x16x32_bf16 v[108:111], v[40:43], a[36:39], v[108:111]\n"
"buffer_load_dwordx4 v[96:99], v1, s[24:27], s76 offen offset:1024 sc1\n"
"v_mfma_f32_16x16x32_bf16 v[112:115], v[32:35], a[8:11], 0\n"
"s_mov_b32 vcc_lo, 0xc000c\n"
"s_mov_b32 vcc_hi, 0xc000c\n"
"v_mfma_f32_16x16x32_bf16 v[112:115], v[36:39], a[24:27], v[112:115]\n"
"v_cvt_pk_bf16_f32 v138, v124, v125\n"
"v_mfma_f32_16x16x32_bf16 v[112:115], v[40:43], a[40:43], v[112:115]\n"
"v_cvt_pk_bf16_f32 v139, v126, v127\n"
"s_mov_b64 exec, 1\n"
"v_mov_b32 v16, 0\n"
"buffer_atomic_add v16, off, s[24:27], s75 offset:532 sc0 sc1\n"
"s_mov_b64 exec, -1\n"
"v_cndmask_b32 v215, 0, v19, vcc\n"
"v_mfma_f32_16x16x32_bf16 v[116:119], v[32:35], a[12:15], 0\n"
"buffer_load_dwordx4 v[100:103], v1, s[24:27], s76 offen offset:2048 sc1\n"
"v_mfma_f32_16x16x32_bf16 v[116:119], v[36:39], a[28:31], v[116:119]\n"
"s_mov_b32 vcc_lo, 0x300030\n"
"s_mov_b32 vcc_hi, 0x300030\n"
"s_add_u32 s76, s76, 0x3000\n"
"v_mfma_f32_16x16x32_bf16 v[116:119], v[40:43], a[44:47], v[116:119]\n"
"v_cvt_pk_bf16_f32 v140, v128, v129\n"
"s_waitcnt vmcnt(16)\n"
"v_mfma_f32_16x16x32_bf16 v[104:107], v[44:47], a[48:51], v[104:107]\n"
"buffer_store_dwordx4 v[136:139], v1, s[24:27], s72 offen sc1\n"
"v_cndmask_b32 v216, 0, v19, vcc\n"
"v_mfma_f32_16x16x32_bf16 v[104:107], v[48:51], a[64:67], v[104:107]\n"
"buffer_load_dwordx4 v[32:35], v1, s[24:27], s76 offen sc1\n"
"v_mfma_f32_16x16x32_bf16 v[104:107], v[52:55], a[80:83], v[104:107]\n"
"s_mov_b32 vcc_lo, 0xc000c0\n"
"s_mov_b32 vcc_hi, 0xc000c0\n"
"v_mfma_f32_16x16x32_bf16 v[108:111], v[44:47], a[52:55], v[108:111]\n"
"v_cvt_pk_bf16_f32 v141, v130, v131\n"
"v_cvt_pk_bf16_f32 v142, v132, v133\n"
"v_mfma_f32_16x16x32_bf16 v[108:111], v[48:51], a[68:71], v[108:111]\n"
"v_cvt_pk_bf16_f32 v143, v134, v135\n"
"v_cndmask_b32 v217, 0, v19, vcc\n"
"v_mfma_f32_16x16x32_bf16 v[108:111], v[52:55], a[84:87], v[108:111]\n"
"buffer_load_dwordx4 v[36:39], v1, s[24:27], s76 offen offset:1024 sc1\n"
"v_mfma_f32_16x16x32_bf16 v[112:115], v[44:47], a[56:59], v[112:115]\n"
"s_cmp_lg_u32 s59, 0\n"
"s_cbranch_scc1 Ld0%=\n"
"s_mov_b64 s[40:41], s[24:25]\n"
"s_mov_b64 s[42:43], s[26:27]\n"
"v_bfe_u32 v18, v0, 4, 1\n"
"v_mul_u32_u24 v18, 0x47f8, v18\n"
"v_add_u32 v12, v12, v18\n"
"s_mul_i32 s63, s57, 0x12000\n"
"s_add_u32 s92, s63, 0x1600000\n"
"s_mov_b32 s96, s92\n"
"Ld0%=:\n"
"s_cmp_lg_u32 s59, 1\n"
"s_cbranch_scc1 Ld1%=\n"
"s_mov_b64 s[40:41], s[54:55]\n"
"s_and_b32 s41, s41, 0xffff\n"
"s_mov_b32 s42, 0x240000\n"
"s_mov_b32 s43, 0x20000\n"
"v_and_b32 v18, 15, v0\n"
"v_and_b32 v19, 12, v0\n"
"v_add_u32 v18, v18, v19\n"
"v_mul_u32_u24 v18, 0x480, v18\n"
"v_bfe_u32 v19, v0, 4, 1\n"
"v_mul_u32_u24 v19, 0x1200, v19\n"
"v_add_u32 v18, v18, v19\n"
"v_lshrrev_b32 v19, 5, v0\n"
"v_lshl_add_u32 v12, v19, 4, v18\n"
"s_mul_i32 s64, s57, 0x12000\n"
"s_lshl_b32 s63, s56, 5\n"
"s_add_u32 s64, s64, s63\n"
"s_lshl_b32 s63, s58, 7\n"
"s_add_u32 s92, s64, s63\n"
"s_lshr_b32 s63, s58, 2\n"
"s_mul_i32 s63, s63, 0x9000\n"
"s_add_u32 s64, s64, s63\n"
"s_and_b32 s63, s58, 1\n"
"s_mul_i32 s63, s63, 0x4800\n"
"s_add_u32 s64, s64, s63\n"
"s_and_b32 s63, s58, 2\n"
"s_mul_i32 s63, s63, 0x900\n"
"s_add_u32 s64, s64, s63\n"
"s_add_u32 s96, s64, 0x400\n"
"v_and_b32 v18, 7, v0\n"
"v_and_b32 v19, 4, v0\n"
"v_add_u32 v18, v18, v19\n"
"v_mul_u32_u24 v18, 0x480, v18\n"
"v_and_b32 v19, 8, v0\n"
"v_lshl_add_u32 v222, v19, 1, v18\n"
"Ld1%=:\n"
"v_mfma_f32_16x16x32_bf16 v[112:115], v[48:51], a[72:75], v[112:115]\n"
"s_add_u32 s93, s92, 0x9000\n"
"v_mfma_f32_16x16x32_bf16 v[112:115], v[52:55], a[88:91], v[112:115]\n"
"v_mfma_f32_16x16x32_bf16 v[116:119], v[44:47], a[60:63], v[116:119]\n"
"buffer_load_dwordx4 v[40:43], v1, s[24:27], s76 offen offset:2048 sc1\n"
"v_mfma_f32_16x16x32_bf16 v[116:119], v[48:51], a[76:79], v[116:119]\n"
"s_add_u32 s76, s76, 0x3000\n"
"v_mfma_f32_16x16x32_bf16 v[116:119], v[52:55], a[92:95], v[116:119]\n"
"s_waitcnt vmcnt(17)\n"
"v_mfma_f32_16x16x32_bf16 v[104:107], v[56:59], a[96:99], v[104:107]\n"
"s_waitcnt vmcnt(15)\n"
"s_mov_b64 exec, 1\n"
"buffer_store_dword v27, off, s[24:27], s74 offset:536 sc1\n"
"s_mov_b64 exec, -1\n"
"v_mfma_f32_16x16x32_bf16 v[104:107], v[60:63], a[112:115], v[104:107]\n"
"buffer_load_dwordx4 v[44:47], v1, s[24:27], s76 offen sc1\n"
"v_mfma_f32_16x16x32_bf16 v[104:107], v[64:67], a[128:131], v[104:107]\n"
"v_mfma_f32_16x16x32_bf16 v[108:111], v[56:59], a[100:103], v[108:111]\n"
"v_mfma_f32_16x16x32_bf16 v[108:111], v[60:63], a[116:119], v[108:111]\n"
"v_mfma_f32_16x16x32_bf16 v[108:111], v[64:67], a[132:135], v[108:111]\n"
"buffer_load_dwordx4 v[48:51], v1, s[24:27], s76 offen offset:1024 sc1\n"
"v_mfma_f32_16x16x32_bf16 v[112:115], v[56:59], a[104:107], v[112:115]\n"
"v_mfma_f32_16x16x32_bf16 v[112:115], v[60:63], a[120:123], v[112:115]\n"
"v_mfma_f32_16x16x32_bf16 v[112:115], v[64:67], a[136:139], v[112:115]\n"
"v_mfma_f32_16x16x32_bf16 v[116:119], v[56:59], a[108:111], v[116:119]\n"
"buffer_load_dwordx4 v[52:55], v1, s[24:27], s76 offen offset:2048 sc1\n"
"v_mfma_f32_16x16x32_bf16 v[116:119], v[60:63], a[124:127], v[116:119]\n"
"s_add_u32 s76, s76, 0x3000\n"
"v_mfma_f32_16x16x32_bf16 v[116:119], v[64:67], a[140:143], v[116:119]\n"
"s_waitcnt vmcnt(17)\n"
"v_mfma_f32_16x16x32_bf16 v[218:221], v[214:217], v[168:171], 0\n"
"v_mfma_f32_16x16x32_bf16 v[218:221], v[214:217], v[172:175], v[218:221]\n"
"s_waitcnt vmcnt(15)\n"
"v_mfma_f32_16x16x32_bf16 v[104:107], v[68:71], a[144:147], v[104:107]\n"
"v_mfma_f32_16x16x32_bf16 v[104:107], v[72:75], a[160:163], v[104:107]\n"
"buffer_load_dwordx4 v[56:59], v1, s[24:27], s76 offen sc1\n"
"v_mfma_f32_16x16x32_bf16 v[104:107], v[76:79], a[176:179], v[104:107]\n"
"v_mfma_f32_16x16x32_bf16 v[108:111], v[68:71], a[148:151], v[108:111]\n"
"s_sub_u32 s63, s58, 1\n"
"s_and_b32 s63, s63, 7\n"
"v_mfma_f32_16x16x32_bf16 v[108:111], v[72:75], a[164:167], v[108:111]\n"
"s_lshl3_add_u32 s63, s58, s63\n"
"s_lshl_b32 s63, s63, 13\n"
"v_mfma_f32_16x16x32_bf16 v[108:111], v[76:79], a[180:183], v[108:111]\n"
"buffer_load_dwordx4 v[60:63], v1, s[24:27], s76 offen offset:1024 sc1\n"
"v_mfma_f32_16x16x32_bf16 v[112:115], v[68:71], a[152:155], v[112:115]\n"
"v_mfma_f32_16x16x32_bf16 v[112:115], v[72:75], a[168:171], v[112:115]\n"
"s_add_u32 s64, s82, s63\n"
"v_mfma_f32_16x16x32_bf16 v[112:115], v[76:79], a[184:187], v[112:115]\n"
"v_mfma_f32_16x16x32_bf16 v[116:119], v[68:71], a[156:159], v[116:119]\n"
"buffer_load_dwordx4 v[64:67], v1, s[24:27], s76 offen offset:2048 sc1\n"
"v_mfma_f32_16x16x32_bf16 v[116:119], v[72:75], a[172:175], v[116:119]\n"
"s_waitcnt vmcnt(12)\n"
"v_readfirstlane_b32 vcc_lo, v16\n"
"s_nop 0\n"
"s_cmp_ge_u32 vcc_lo, 1\n"
"s_cbranch_scc1 Lc1_d%=\n"
"Lc1%=:\n"
"s_mov_b64 exec, 1\n"
"v_mov_b32 v16, 0\n"
"buffer_atomic_add v16, off, s[24:27], s75 offset:532 sc0 sc1\n"
"s_mov_b64 exec, -1\n"
"s_waitcnt vmcnt(0)\n"
"v_readfirstlane_b32 vcc_lo, v16\n"
"s_nop 0\n"
"s_cmp_ge_u32 vcc_lo, 1\n"
"s_cbranch_scc1 Lc1_d%=\n"
"s_sleep 2\n"
"s_sub_u32 s60, s60, 1\n"
"s_cbranch_scc0 Lc1%=\n"
"s_mov_b32 s60, 0\n"
"Lc1_d%=:\n"
"s_add_u32 s76, s76, 0x3000\n"
"v_mfma_f32_16x16x32_bf16 v[116:119], v[76:79], a[188:191], v[116:119]\n"
"buffer_load_dwordx4 v[160:163], v1, s[24:27], s64 offen sc1\n"
"v_mfma_f32_16x16x32_bf16 v[104:107], v[80:83], a[192:195], v[104:107]\n"
"buffer_store_dwordx4 v[140:143], v1, s[24:27], s72 offen offset:1024 sc1\n"
"v_mfma_f32_16x16x32_bf16 v[104:107], v[84:87], a[208:211], v[104:107]\n"
"buffer_load_dwordx4 v[68:71], v1, s[24:27], s76 offen sc1\n"
"v_mfma_f32_16x16x32_bf16 v[104:107], v[88:91], a[224:227], v[104:107]\n"
"v_mfma_f32_16x16x32_bf16 v[108:111], v[80:83], a[196:199], v[108:111]\n"
"s_lshl_b32 s62, s77, 11\n"
"s_add_u32 s74, s21, s62\n"
"v_mfma_f32_16x16x32_bf16 v[108:111], v[84:87], a[212:215], v[108:111]\n"
"s_add_u32 s77, s58, 4\n"
"s_and_b32 s77, s77, 7\n"
"v_fma_f32 v218, v218, v212, v208\n"
"v_fma_f32 v219, v219, v212, v209\n"
"v_mfma_f32_16x16x32_bf16 v[108:111], v[88:91], a[228:231], v[108:111]\n"
"buffer_load_dwordx4 v[72:75], v1, s[24:27], s76 offen offset:1024 sc1\n"
"v_mfma_f32_16x16x32_bf16 v[112:115], v[80:83], a[200:203], v[112:115]\n"
"v_mfma_f32_16x16x32_bf16 v[112:115], v[84:87], a[216:219], v[112:115]\n"
"s_lshl3_add_u32 s62, s77, s58\n"
"s_lshl_b32 s62, s62, 13\n"
"v_fma_f32 v220, v220, v212, v210\n"
"v_fma_f32 v221, v221, v212, v211\n"
"v_mfma_f32_16x16x32_bf16 v[112:115], v[88:91], a[232:235], v[112:115]\n"
"s_add_u32 s72, s82, s62\n"
"v_cvt_pk_bf16_f32 v208, v218, v219\n"
"v_cvt_pk_bf16_f32 v209, v220, v221\n"
"v_mfma_f32_16x16x32_bf16 v[116:119], v[80:83], a[204:207], v[116:119]\n"
"buffer_load_dwordx4 v[76:79], v1, s[24:27], s76 offen offset:2048 sc1\n"
"v_mfma_f32_16x16x32_bf16 v[116:119], v[84:87], a[220:223], v[116:119]\n"
"s_add_u32 s76, s76, 0x3000\n"
"v_mfma_f32_16x16x32_bf16 v[116:119], v[88:91], a[236:239], v[116:119]\n"
"v_mov_b32 v210, v208\n"
"v_mov_b32 v211, v209\n"
"s_waitcnt vmcnt(16)\n"
"v_mfma_f32_16x16x32_bf16 v[104:107], v[92:95], a[240:243], v[104:107]\n"
"v_mfma_f32_16x16x32_bf16 v[104:107], v[96:99], v[224:227], v[104:107]\n"
"buffer_load_dwordx4 v[80:83], v1, s[24:27], s76 offen sc1\n"
"v_mfma_f32_16x16x32_bf16 v[120:123], v[100:103], v[240:243], v[104:107]\n"
"v_permlane16_swap_b32 v208, v210\n"
"v_permlane16_swap_b32 v209, v211\n"
"v_mfma_f32_16x16x32_bf16 v[108:111], v[92:95], a[244:247], v[108:111]\n"
"v_mfma_f32_16x16x32_bf16 v[108:111], v[96:99], v[228:231], v[108:111]\n"
"s_mov_b64 exec, 0xffff\n"
"s_cmp_lg_u32 s59, 0\n"
"s_cbranch_scc1 Lo80%=\n"
"buffer_store_dwordx4 v[208:211], v222, s[40:43], s96 offen sc1\n"
"Lo80%=:\n"
"s_cmp_lg_u32 s59, 1\n"
"s_cbranch_scc1 Lo81%=\n"
"buffer_store_dwordx4 v[208:211], v222, s[40:43], s96 offen\n"
"Lo81%=:\n"
"s_mov_b64 exec, -1\n"
"v_mfma_f32_16x16x32_bf16 v[124:127], v[100:103], v[244:247], v[108:111]\n"
"buffer_load_dwordx4 v[84:87], v1, s[24:27], s76 offen offset:1024 sc1\n"
"v_mfma_f32_16x16x32_bf16 v[112:115], v[92:95], a[248:251], v[112:115]\n"
"v_mfma_f32_16x16x32_bf16 v[112:115], v[96:99], v[232:235], v[112:115]\n"
"buffer_load_dwordx4 v[164:167], v1, s[24:27], s64 offen offset:1024 sc1\n"
"v_mfma_f32_16x16x32_bf16 v[128:131], v[100:103], v[248:251], v[112:115]\n"
"v_mfma_f32_16x16x32_bf16 v[116:119], v[92:95], a[252:255], v[116:119]\n"
"buffer_load_dwordx4 v[88:91], v1, s[24:27], s76 offen offset:2048 sc1\n"
"v_mfma_f32_16x16x32_bf16 v[116:119], v[96:99], v[236:239], v[116:119]\n"
"s_add_u32 s76, s76, 0x3000\n"
"v_mfma_f32_16x16x32_bf16 v[132:135], v[100:103], v[252:255], v[116:119]\n"
"s_waitcnt vmcnt(17)\n"
"v_mfma_f32_16x16x32_bf16 v[104:107], v[32:35], a[0:3], 0\n"
"v_mfma_f32_16x16x32_bf16 v[104:107], v[36:39], a[16:19], v[104:107]\n"
"buffer_load_dwordx4 v[92:95], v1, s[24:27], s76 offen sc1\n"
"v_mfma_f32_16x16x32_bf16 v[104:107], v[40:43], a[32:35], v[104:107]\n"
"v_mfma_f32_16x16x32_bf16 v[108:111], v[32:35], a[4:7], 0\n"
"v_cvt_pk_bf16_f32 v136, v120, v121\n"
"v_mfma_f32_16x16x32_bf16 v[108:111], v[36:39], a[20:23], v[108:111]\n"
"v_cvt_pk_bf16_f32 v137, v122, v123\n"
"v_mfma_f32_16x16x32_bf16 v[108:111], v[40:43], a[36:39], v[108:111]\n"
"buffer_load_dwordx4 v[96:99], v1, s[24:27], s76 offen offset:1024 sc1\n"
"v_mfma_f32_16x16x32_bf16 v[112:115], v[32:35], a[8:11], 0\n"
"v_mfma_f32_16x16x32_bf16 v[112:115], v[36:39], a[24:27], v[112:115]\n"
"v_cvt_pk_bf16_f32 v138, v124, v125\n"
"v_mfma_f32_16x16x32_bf16 v[112:115], v[40:43], a[40:43], v[112:115]\n"
"v_cvt_pk_bf16_f32 v139, v126, v127\n"
"s_mov_b64 exec, 1\n"
"v_mov_b32 v16, 0\n"
"buffer_atomic_add v16, off, s[24:27], s75 offset:536 sc0 sc1\n"
"s_mov_b64 exec, -1\n"
"v_mfma_f32_16x16x32_bf16 v[116:119], v[32:35], a[12:15], 0\n"
"buffer_load_dwordx4 v[100:103], v1, s[24:27], s76 offen offset:2048 sc1\n"
"v_mfma_f32_16x16x32_bf16 v[116:119], v[36:39], a[28:31], v[116:119]\n"
"s_add_u32 s76, s76, 0x3000\n"
"v_mfma_f32_16x16x32_bf16 v[116:119], v[40:43], a[44:47], v[116:119]\n"
"v_cvt_pk_bf16_f32 v140, v128, v129\n"
"s_waitcnt vmcnt(17)\n"
"v_mfma_f32_16x16x32_bf16 v[104:107], v[44:47], a[48:51], v[104:107]\n"
"buffer_store_dwordx4 v[136:139], v1, s[24:27], s72 offen sc1\n"
"v_mfma_f32_16x16x32_bf16 v[104:107], v[48:51], a[64:67], v[104:107]\n"
"buffer_load_dwordx4 v[32:35], v1, s[24:27], s76 offen sc1\n"
"v_mfma_f32_16x16x32_bf16 v[104:107], v[52:55], a[80:83], v[104:107]\n"
"v_mfma_f32_16x16x32_bf16 v[108:111], v[44:47], a[52:55], v[108:111]\n"
"v_cvt_pk_bf16_f32 v141, v130, v131\n"
"v_cvt_pk_bf16_f32 v142, v132, v133\n"
"v_mfma_f32_16x16x32_bf16 v[108:111], v[48:51], a[68:71], v[108:111]\n"
"v_cvt_pk_bf16_f32 v143, v134, v135\n"
"v_mfma_f32_16x16x32_bf16 v[108:111], v[52:55], a[84:87], v[108:111]\n"
"buffer_load_dwordx4 v[36:39], v1, s[24:27], s76 offen offset:1024 sc1\n"
"v_mfma_f32_16x16x32_bf16 v[112:115], v[44:47], a[56:59], v[112:115]\n"
"v_mfma_f32_16x16x32_bf16 v[112:115], v[48:51], a[72:75], v[112:115]\n"
"v_mfma_f32_16x16x32_bf16 v[112:115], v[52:55], a[88:91], v[112:115]\n"
"v_mfma_f32_16x16x32_bf16 v[116:119], v[44:47], a[60:63], v[116:119]\n"
"buffer_load_dwordx4 v[40:43], v1, s[24:27], s76 offen offset:2048 sc1\n"
"v_mfma_f32_16x16x32_bf16 v[116:119], v[48:51], a[76:79], v[116:119]\n"
"s_add_u32 s76, s76, 0x3000\n"
"v_mfma_f32_16x16x32_bf16 v[116:119], v[52:55], a[92:95], v[116:119]\n"
"s_waitcnt vmcnt(18)\n"
"v_mfma_f32_16x16x32_bf16 v[104:107], v[56:59], a[96:99], v[104:107]\n"
"s_waitcnt vmcnt(16)\n"
"s_mov_b64 exec, 1\n"
"buffer_store_dword v27, off, s[24:27], s74 offset:540 sc1\n"
"s_mov_b64 exec, -1\n"
"v_mfma_f32_16x16x32_bf16 v[104:107], v[60:63], a[112:115], v[104:107]\n"
"buffer_load_dwordx4 v[44:47], v1, s[24:27], s76 offen sc1\n"
"v_mfma_f32_16x16x32_bf16 v[104:107], v[64:67], a[128:131], v[104:107]\n"
"v_mfma_f32_16x16x32_bf16 v[108:111], v[56:59], a[100:103], v[108:111]\n"
"v_mfma_f32_16x16x32_bf16 v[108:111], v[60:63], a[116:119], v[108:111]\n"
"v_mfma_f32_16x16x32_bf16 v[108:111], v[64:67], a[132:135], v[108:111]\n"
"buffer_load_dwordx4 v[48:51], v1, s[24:27], s76 offen offset:1024 sc1\n"
"v_mfma_f32_16x16x32_bf16 v[112:115], v[56:59], a[104:107], v[112:115]\n"
"v_mfma_f32_16x16x32_bf16 v[112:115], v[60:63], a[120:123], v[112:115]\n"
"v_mfma_f32_16x16x32_bf16 v[112:115], v[64:67], a[136:139], v[112:115]\n"
"v_mfma_f32_16x16x32_bf16 v[116:119], v[56:59], a[108:111], v[116:119]\n"
"buffer_load_dwordx4 v[52:55], v1, s[24:27], s76 offen offset:2048 sc1\n"
"v_mfma_f32_16x16x32_bf16 v[116:119], v[60:63], a[124:127], v[116:119]\n"
"s_add_u32 s76, s76, 0x3000\n"
"v_mfma_f32_16x16x32_bf16 v[116:119], v[64:67], a[140:143], v[116:119]\n"
"s_waitcnt vmcnt(17)\n"
"v_mfma_f32_16x16x32_bf16 v[104:107], v[68:71], a[144:147], v[104:107]\n"
"v_mfma_f32_16x16x32_bf16 v[104:107], v[72:75], a[160:163], v[104:107]\n"
"buffer_load_dwordx4 v[56:59], v1, s[24:27], s76 offen sc1\n"
"v_mfma_f32_16x16x32_bf16 v[104:107], v[76:79], a[176:179], v[104:107]\n"
"v_mfma_f32_16x16x32_bf16 v[108:111], v[68:71], a[148:151], v[108:111]\n"
"s_sub_u32 s63, s58, 2\n"
"s_and_b32 s63, s63, 7\n"
"v_mfma_f32_16x16x32_bf16 v[108:111], v[72:75], a[164:167], v[108:111]\n"
"s_lshl3_add_u32 s63, s58, s63\n"
"s_lshl_b32 s63, s63, 13\n"
"v_mfma_f32_16x16x32_bf16 v[108:111], v[76:79], a[180:183], v[108:111]\n"
"buffer_load_dwordx4 v[60:63], v1, s[24:27], s76 offen offset:1024 sc1\n"
"v_mfma_f32_16x16x32_bf16 v[112:115], v[68:71], a[152:155], v[112:115]\n"
"v_mfma_f32_16x16x32_bf16 v[112:115], v[72:75], a[168:171], v[112:115]\n"
"s_add_u32 s64, s82, s63\n"
"v_mfma_f32_16x16x32_bf16 v[112:115], v[76:79], a[184:187], v[112:115]\n"
"v_mfma_f32_16x16x32_bf16 v[116:119], v[68:71], a[156:159], v[116:119]\n"
"buffer_load_dwordx4 v[64:67], v1, s[24:27], s76 offen offset:2048 sc1\n"
"v_mfma_f32_16x16x32_bf16 v[116:119], v[72:75], a[172:175], v[116:119]\n"
"s_waitcnt vmcnt(12)\n"
"v_readfirstlane_b32 vcc_lo, v16\n"
"s_nop 0\n"
"s_cmp_ge_u32 vcc_lo, 1\n"
"s_cbranch_scc1 Lc2_d%=\n"
"Lc2%=:\n"
"s_mov_b64 exec, 1\n"
"v_mov_b32 v16, 0\n"
"buffer_atomic_add v16, off, s[24:27], s75 offset:536 sc0 sc1\n"
"s_mov_b64 exec, -1\n"
"s_waitcnt vmcnt(0)\n"
"v_readfirstlane_b32 vcc_lo, v16\n"
"s_nop 0\n"
"s_cmp_ge_u32 vcc_lo, 1\n"
"s_cbranch_scc1 Lc2_d%=\n"
"s_sleep 2\n"
"s_sub_u32 s60, s60, 1\n"
"s_cbranch_scc0 Lc2%=\n"
"s_mov_b32 s60, 0\n"
"Lc2_d%=:\n"
"s_add_u32 s76, s76, 0x3000\n"
"v_mfma_f32_16x16x32_bf16 v[116:119], v[76:79], a[188:191], v[116:119]\n"
"v_mfma_f32_16x16x32_bf16 v[144:147], v[22:25], v[160:163], 0\n"
"v_mfma_f32_16x16x32_bf16 v[148:151], v[20:23], v[160:163], 0\n"
"v_mfma_f32_16x16x32_bf16 v[152:155], v[22:25], v[164:167], 0\n"
"v_mfma_f32_16x16x32_bf16 v[156:159], v[20:23], v[164:167], 0\n"
"buffer_load_dwordx4 v[160:163], v1, s[24:27], s64 offen sc1\n"
"v_mfma_f32_16x16x32_bf16 v[104:107], v[80:83], a[192:195], v[104:107]\n"
"buffer_store_dwordx4 v[140:143], v1, s[24:27], s72 offen offset:1024 sc1\n"
"v_mfma_f32_16x16x32_bf16 v[104:107], v[84:87], a[208:211], v[104:107]\n"
"buffer_load_dwordx4 v[68:71], v1, s[24:27], s76 offen sc1\n"
"v_mfma_f32_16x16x32_bf16 v[104:107], v[88:91], a[224:227], v[104:107]\n"
"v_mfma_f32_16x16x32_bf16 v[108:111], v[80:83], a[196:199], v[108:111]\n"
"s_lshl_b32 s62, s77, 11\n"
"s_add_u32 s74, s21, s62\n"
"v_mfma_f32_16x16x32_bf16 v[108:111], v[84:87], a[212:215], v[108:111]\n"
"s_add_u32 s77, s58, 5\n"
"s_and_b32 s77, s77, 7\n"
"v_mfma_f32_16x16x32_bf16 v[108:111], v[88:91], a[228:231], v[108:111]\n"
"buffer_load_dwordx4 v[72:75], v1, s[24:27], s76 offen offset:1024 sc1\n"
"v_mfma_f32_16x16x32_bf16 v[112:115], v[80:83], a[200:203], v[112:115]\n"
"v_mfma_f32_16x16x32_bf16 v[112:115], v[84:87], a[216:219], v[112:115]\n"
"s_lshl3_add_u32 s62, s77, s58\n"
"s_lshl_b32 s62, s62, 13\n"
"v_mfma_f32_16x16x32_bf16 v[112:115], v[88:91], a[232:235], v[112:115]\n"
"s_add_u32 s72, s82, s62\n"
"v_mfma_f32_16x16x32_bf16 v[116:119], v[80:83], a[204:207], v[116:119]\n"
"buffer_load_dwordx4 v[76:79], v1, s[24:27], s76 offen offset:2048 sc1\n"
"v_mfma_f32_16x16x32_bf16 v[116:119], v[84:87], a[220:223], v[116:119]\n"
"s_add_u32 s76, s76, 0x3000\n"
"v_mfma_f32_16x16x32_bf16 v[116:119], v[88:91], a[236:239], v[116:119]\n"
"s_waitcnt vmcnt(16)\n"
"v_mfma_f32_16x16x32_bf16 v[104:107], v[92:95], a[240:243], v[104:107]\n"
"v_mfma_f32_16x16x32_bf16 v[104:107], v[96:99], v[224:227], v[104:107]\n"
"buffer_load_dwordx4 v[80:83], v1, s[24:27], s76 offen sc1\n"
"v_mfma_f32_16x16x32_bf16 v[120:123], v[100:103], v[240:243], v[104:107]\n"
"v_mfma_f32_16x16x32_bf16 v[108:111], v[92:95], a[244:247], v[108:111]\n"
"v_mfma_f32_16x16x32_bf16 v[108:111], v[96:99], v[228:231], v[108:111]\n"
"v_mfma_f32_16x16x32_bf16 v[124:127], v[100:103], v[244:247], v[108:111]\n"
"buffer_load_dwordx4 v[84:87], v1, s[24:27], s76 offen offset:1024 sc1\n"
"v_mfma_f32_16x16x32_bf16 v[112:115], v[92:95], a[248:251], v[112:115]\n"
"v_mfma_f32_16x16x32_bf16 v[112:115], v[96:99], v[232:235], v[112:115]\n"
"buffer_load_dwordx4 v[164:167], v1, s[24:27], s64 offen offset:1024 sc1\n"
"v_mfma_f32_16x16x32_bf16 v[128:131], v[100:103], v[248:251], v[112:115]\n"
"v_mfma_f32_16x16x32_bf16 v[116:119], v[92:95], a[252:255], v[116:119]\n"
"buffer_load_dwordx4 v[88:91], v1, s[24:27], s76 offen offset:2048 sc1\n"
"v_mfma_f32_16x16x32_bf16 v[116:119], v[96:99], v[236:239], v[116:119]\n"
"s_add_u32 s76, s76, 0x3000\n"
"v_mfma_f32_16x16x32_bf16 v[132:135], v[100:103], v[252:255], v[116:119]\n"
"s_waitcnt vmcnt(16)\n"
"v_mfma_f32_16x16x32_bf16 v[104:107], v[32:35], a[0:3], 0\n"
"v_mfma_f32_16x16x32_bf16 v[104:107], v[36:39], a[16:19], v[104:107]\n"
"buffer_load_dwordx4 v[92:95], v1, s[24:27], s76 offen sc1\n"
"v_mfma_f32_16x16x32_bf16 v[104:107], v[40:43], a[32:35], v[104:107]\n"
"v_mfma_f32_16x16x32_bf16 v[108:111], v[32:35], a[4:7], 0\n"
"v_cvt_pk_bf16_f32 v136, v120, v121\n"
"v_mfma_f32_16x16x32_bf16 v[108:111], v[36:39], a[20:23], v[108:111]\n"
"v_cvt_pk_bf16_f32 v137, v122, v123\n"
"v_mfma_f32_16x16x32_bf16 v[108:111], v[40:43], a[36:39], v[108:111]\n"
"buffer_load_dwordx4 v[96:99], v1, s[24:27], s76 offen offset:1024 sc1\n"
"v_mfma_f32_16x16x32_bf16 v[112:115], v[32:35], a[8:11], 0\n"
"v_mfma_f32_16x16x32_bf16 v[112:115], v[36:39], a[24:27], v[112:115]\n"
"v_cvt_pk_bf16_f32 v138, v124, v125\n"
"v_mfma_f32_16x16x32_bf16 v[112:115], v[40:43], a[40:43], v[112:115]\n"
"v_cvt_pk_bf16_f32 v139, v126, v127\n"
"s_mov_b64 exec, 1\n"
"v_mov_b32 v16, 0\n"
"buffer_atomic_add v16, off, s[24:27], s75 offset:540 sc0 sc1\n"
"s_mov_b64 exec, -1\n"
"v_mfma_f32_16x16x32_bf16 v[116:119], v[32:35], a[12:15], 0\n"
"buffer_load_dwordx4 v[100:103], v1, s[24:27], s76 offen offset:2048 sc1\n"
"v_mfma_f32_16x16x32_bf16 v[116:119], v[36:39], a[28:31], v[116:119]\n"
"s_add_u32 s76, s76, 0x3000\n"
"v_mfma_f32_16x16x32_bf16 v[116:119], v[40:43], a[44:47], v[116:119]\n"
"v_cvt_pk_bf16_f32 v140, v128, v129\n"
"s_waitcnt vmcnt(16)\n"
"v_mfma_f32_16x16x32_bf16 v[104:107], v[44:47], a[48:51], v[104:107]\n"
"buffer_store_dwordx4 v[136:139], v1, s[24:27], s72 offen sc1\n"
"v_mfma_f32_16x16x32_bf16 v[104:107], v[48:51], a[64:67], v[104:107]\n"
"buffer_load_dwordx4 v[32:35], v1, s[24:27], s76 offen sc1\n"
"v_mfma_f32_16x16x32_bf16 v[104:107], v[52:55], a[80:83], v[104:107]\n"
"v_mfma_f32_16x16x32_bf16 v[108:111], v[44:47], a[52:55], v[108:111]\n"
"v_cvt_pk_bf16_f32 v141, v130, v131\n"
"v_cvt_pk_bf16_f32 v142, v132, v133\n"
"v_mfma_f32_16x16x32_bf16 v[108:111], v[48:51], a[68:71], v[108:111]\n"
"v_cvt_pk_bf16_f32 v143, v134, v135\n"
"v_mfma_f32_16x16x32_bf16 v[108:111], v[52:55], a[84:87], v[108:111]\n"
"buffer_load_dwordx4 v[36:39], v1, s[24:27], s76 offen offset:1024 sc1\n"
"v_mfma_f32_16x16x32_bf16 v[112:115], v[44:47], a[56:59], v[112:115]\n"
"v_mfma_f32_16x16x32_bf16 v[112:115], v[48:51], a[72:75], v[112:115]\n"
"v_mfma_f32_16x16x32_bf16 v[112:115], v[52:55], a[88:91], v[112:115]\n"
"v_mfma_f32_16x16x32_bf16 v[116:119], v[44:47], a[60:63], v[116:119]\n"
"buffer_load_dwordx4 v[40:43], v1, s[24:27], s76 offen offset:2048 sc1\n"
"v_mfma_f32_16x16x32_bf16 v[116:119], v[48:51], a[76:79], v[116:119]\n"
"s_add_u32 s76, s76, 0x3000\n"
"v_mfma_f32_16x16x32_bf16 v[116:119], v[52:55], a[92:95], v[116:119]\n"
"s_waitcnt vmcnt(17)\n"
"v_mfma_f32_16x16x32_bf16 v[104:107], v[56:59], a[96:99], v[104:107]\n"
"s_waitcnt vmcnt(15)\n"
"s_mov_b64 exec, 1\n"
"buffer_store_dword v27, off, s[24:27], s74 offset:544 sc1\n"
"s_mov_b64 exec, -1\n"
"v_mfma_f32_16x16x32_bf16 v[104:107], v[60:63], a[112:115], v[104:107]\n"
"buffer_load_dwordx4 v[44:47], v1, s[24:27], s76 offen sc1\n"
"v_mfma_f32_16x16x32_bf16 v[104:107], v[64:67], a[128:131], v[104:107]\n"
"v_mfma_f32_16x16x32_bf16 v[108:111], v[56:59], a[100:103], v[108:111]\n"
"v_mfma_f32_16x16x32_bf16 v[108:111], v[60:63], a[116:119], v[108:111]\n"
"v_mfma_f32_16x16x32_bf16 v[108:111], v[64:67], a[132:135], v[108:111]\n"
"buffer_load_dwordx4 v[48:51], v1, s[24:27], s76 offen offset:1024 sc1\n"
"v_mfma_f32_16x16x32_bf16 v[112:115], v[56:59], a[104:107], v[112:115]\n"
"v_mfma_f32_16x16x32_bf16 v[112:115], v[60:63], a[120:123], v[112:115]\n"
"v_mfma_f32_16x16x32_bf16 v[112:115], v[64:67], a[136:139], v[112:115]\n"
"v_mfma_f32_16x16x32_bf16 v[116:119], v[56:59], a[108:111], v[116:119]\n"
"buffer_load_dwordx4 v[52:55], v1, s[24:27], s76 offen offset:2048 sc1\n"
"v_mfma_f32_16x16x32_bf16 v[116:119], v[60:63], a[124:127], v[116:119]\n"
"s_add_u32 s76, s76, 0x3000\n"
"v_mfma_f32_16x16x32_bf16 v[116:119], v[64:67], a[140:143], v[116:119]\n"
"s_waitcnt vmcnt(16)\n"
"v_mfma_f32_16x16x32_bf16 v[104:107], v[68:71], a[144:147], v[104:107]\n"
"v_mfma_f32_16x16x32_bf16 v[104:107], v[72:75], a[160:163], v[104:107]\n"
"buffer_load_dwordx4 v[56:59], v1, s[24:27], s76 offen sc1\n"
"v_mfma_f32_16x16x32_bf16 v[104:107], v[76:79], a[176:179], v[104:107]\n"
"v_mfma_f32_16x16x32_bf16 v[108:111], v[68:71], a[148:151], v[108:111]\n"
"s_sub_u32 s63, s58, 3\n"
"s_and_b32 s63, s63, 7\n"
"v_mfma_f32_16x16x32_bf16 v[108:111], v[72:75], a[164:167], v[108:111]\n"
"s_lshl3_add_u32 s63, s58, s63\n"
"s_lshl_b32 s63, s63, 13\n"
"v_mfma_f32_16x16x32_bf16 v[108:111], v[76:79], a[180:183], v[108:111]\n"
"buffer_load_dwordx4 v[60:63], v1, s[24:27], s76 offen offset:1024 sc1\n"
"v_mfma_f32_16x16x32_bf16 v[112:115], v[68:71], a[152:155], v[112:115]\n"
"v_mfma_f32_16x16x32_bf16 v[112:115], v[72:75], a[168:171], v[112:115]\n"
"s_add_u32 s64, s82, s63\n"
"v_mfma_f32_16x16x32_bf16 v[112:115], v[76:79], a[184:187], v[112:115]\n"
"v_mfma_f32_16x16x32_bf16 v[116:119], v[68:71], a[156:159], v[116:119]\n"
"buffer_load_dwordx4 v[64:67], v1, s[24:27], s76 offen offset:2048 sc1\n"
"v_mfma_f32_16x16x32_bf16 v[116:119], v[72:75], a[172:175], v[116:119]\n"
"s_waitcnt vmcnt(12)\n"
"v_readfirstlane_b32 vcc_lo, v16\n"
"s_nop 0\n"
"s_cmp_ge_u32 vcc_lo, 1\n"
"s_cbranch_scc1 Lc3_d%=\n"
"Lc3%=:\n"
"s_mov_b64 exec, 1\n"
"v_mov_b32 v16, 0\n"
"buffer_atomic_add v16, off, s[24:27], s75 offset:540 sc0 sc1\n"
"s_mov_b64 exec, -1\n"
"s_waitcnt vmcnt(0)\n"
"v_readfirstlane_b32 vcc_lo, v16\n"
"s_nop 0\n"
"s_cmp_ge_u32 vcc_lo, 1\n"
"s_cbranch_scc1 Lc3_d%=\n"
"s_sleep 2\n"
"s_sub_u32 s60, s60, 1\n"
"s_cbranch_scc0 Lc3%=\n"
"s_mov_b32 s60, 0\n"
"Lc3_d%=:\n"
"s_add_u32 s76, s76, 0x3000\n"
"v_mfma_f32_16x16x32_bf16 v[116:119], v[76:79], a[188:191], v[116:119]\n"
"v_mfma_f32_16x16x32_bf16 v[144:147], v[22:25], v[160:163], v[144:147]\n"
"v_mfma_f32_16x16x32_bf16 v[148:151], v[20:23], v[160:163], v[148:151]\n"
"v_mfma_f32_16x16x32_bf16 v[152:155], v[22:25], v[164:167], v[152:155]\n"
"v_mfma_f32_16x16x32_bf16 v[156:159], v[20:23], v[164:167], v[156:159]\n"
"buffer_load_dwordx4 v[160:163], v1, s[24:27], s64 offen sc1\n"
"v_mfma_f32_16x16x32_bf16 v[104:107], v[80:83], a[192:195], v[104:107]\n"
"buffer_store_dwordx4 v[140:143], v1, s[24:27], s72 offen offset:1024 sc1\n"
"v_mfma_f32_16x16x32_bf16 v[104:107], v[84:87], a[208:211], v[104:107]\n"
"buffer_load_dwordx4 v[68:71], v1, s[24:27], s76 offen sc1\n"
"v_mfma_f32_16x16x32_bf16 v[104:107], v[88:91], a[224:227], v[104:107]\n"
"v_mfma_f32_16x16x32_bf16 v[108:111], v[80:83], a[196:199], v[108:111]\n"
"s_lshl_b32 s62, s77, 11\n"
"s_add_u32 s74, s21, s62\n"
"v_mfma_f32_16x16x32_bf16 v[108:111], v[84:87], a[212:215], v[108:111]\n"
"s_add_u32 s77, s58, 6\n"
"s_and_b32 s77, s77, 7\n"
"v_mfma_f32_16x16x32_bf16 v[108:111], v[88:91], a[228:231], v[108:111]\n"
"buffer_load_dwordx4 v[72:75], v1, s[24:27], s76 offen offset:1024 sc1\n"
"v_mfma_f32_16x16x32_bf16 v[112:115], v[80:83], a[200:203], v[112:115]\n"
"v_mfma_f32_16x16x32_bf16 v[112:115], v[84:87], a[216:219], v[112:115]\n"
"s_lshl3_add_u32 s62, s77, s58\n"
"s_lshl_b32 s62, s62, 13\n"
"v_mfma_f32_16x16x32_bf16 v[112:115], v[88:91], a[232:235], v[112:115]\n"
"s_add_u32 s72, s82, s62\n"
"v_mfma_f32_16x16x32_bf16 v[116:119], v[80:83], a[204:207], v[116:119]\n"
"buffer_load_dwordx4 v[76:79], v1, s[24:27], s76 offen offset:2048 sc1\n"
"v_mfma_f32_16x16x32_bf16 v[116:119], v[84:87], a[220:223], v[116:119]\n"
"s_add_u32 s76, s76, 0x3000\n"
"v_mfma_f32_16x16x32_bf16 v[116:119], v[88:91], a[236:239], v[116:119]\n"
"s_waitcnt vmcnt(16)\n"
"v_mfma_f32_16x16x32_bf16 v[104:107], v[92:95], a[240:243], v[104:107]\n"
"v_mfma_f32_16x16x32_bf16 v[104:107], v[96:99], v[224:227], v[104:107]\n"
"buffer_load_dwordx4 v[80:83], v1, s[24:27], s76 offen sc1\n"
"v_mfma_f32_16x16x32_bf16 v[120:123], v[100:103], v[240:243], v[104:107]\n"
"v_mfma_f32_16x16x32_bf16 v[108:111], v[92:95], a[244:247], v[108:111]\n"
"v_mfma_f32_16x16x32_bf16 v[108:111], v[96:99], v[228:231], v[108:111]\n"
"v_mfma_f32_16x16x32_bf16 v[124:127], v[100:103], v[244:247], v[108:111]\n"
"buffer_load_dwordx4 v[84:87], v1, s[24:27], s76 offen offset:1024 sc1\n"
"v_mfma_f32_16x16x32_bf16 v[112:115], v[92:95], a[248:251], v[112:115]\n"
"v_mfma_f32_16x16x32_bf16 v[112:115], v[96:99], v[232:235], v[112:115]\n"
"buffer_load_dwordx4 v[164:167], v1, s[24:27], s64 offen offset:1024 sc1\n"
"v_mfma_f32_16x16x32_bf16 v[128:131], v[100:103], v[248:251], v[112:115]\n"
"v_mfma_f32_16x16x32_bf16 v[116:119], v[92:95], a[252:255], v[116:119]\n"
"buffer_load_dwordx4 v[88:91], v1, s[24:27], s76 offen offset:2048 sc1\n"
"v_mfma_f32_16x16x32_bf16 v[116:119], v[96:99], v[236:239], v[116:119]\n"
"s_add_u32 s76, s76, 0x3000\n"
"v_mfma_f32_16x16x32_bf16 v[132:135], v[100:103], v[252:255], v[116:119]\n"
"s_waitcnt vmcnt(16)\n"
"v_mfma_f32_16x16x32_bf16 v[104:107], v[32:35], a[0:3], 0\n"
"v_mfma_f32_16x16x32_bf16 v[104:107], v[36:39], a[16:19], v[104:107]\n"
"buffer_load_dwordx4 v[92:95], v1, s[24:27], s76 offen sc1\n"
"v_mfma_f32_16x16x32_bf16 v[104:107], v[40:43], a[32:35], v[104:107]\n"
"v_cvt_pk_bf16_f32 v136, v120, v121\n"
"v_cvt_pk_bf16_f32 v137, v122, v123\n"
"v_mfma_f32_16x16x32_bf16 v[108:111], v[32:35], a[4:7], 0\n"
"v_cvt_pk_bf16_f32 v138, v124, v125\n"
"v_cvt_pk_bf16_f32 v139, v126, v127\n"
"v_mfma_f32_16x16x32_bf16 v[108:111], v[36:39], a[20:23], v[108:111]\n"
"v_mfma_f32_16x16x32_bf16 v[108:111], v[40:43], a[36:39], v[108:111]\n"
"buffer_load_dwordx4 v[96:99], v1, s[24:27], s76 offen offset:1024 sc1\n"
"v_mfma_f32_16x16x32_bf16 v[112:115], v[32:35], a[8:11], 0\n"
"buffer_store_dwordx4 v[136:139], v1, s[24:27], s72 offen sc1\n"
"v_mfma_f32_16x16x32_bf16 v[112:115], v[36:39], a[24:27], v[112:115]\n"
"v_cvt_pk_bf16_f32 v140, v128, v129\n"
"v_cvt_pk_bf16_f32 v141, v130, v131\n"
"v_mfma_f32_16x16x32_bf16 v[112:115], v[40:43], a[40:43], v[112:115]\n"
"v_cvt_pk_bf16_f32 v142, v132, v133\n"
"v_cvt_pk_bf16_f32 v143, v134, v135\n"
"s_mov_b64 exec, 1\n"
"v_mov_b32 v16, 0\n"
"buffer_atomic_add v16, off, s[24:27], s75 offset:544 sc0 sc1\n"
"s_mov_b64 exec, -1\n"
"v_mfma_f32_16x16x32_bf16 v[116:119], v[32:35], a[12:15], 0\n"
"buffer_load_dwordx4 v[100:103], v1, s[24:27], s76 offen offset:2048 sc1\n"
"v_mfma_f32_16x16x32_bf16 v[116:119], v[36:39], a[28:31], v[116:119]\n"
"s_add_u32 s76, s76, 0x3000\n"
"v_mfma_f32_16x16x32_bf16 v[116:119], v[40:43], a[44:47], v[116:119]\n"
"buffer_store_dwordx4 v[140:143], v1, s[24:27], s72 offen offset:1024 sc1\n"
"s_waitcnt vmcnt(18)\n"
"v_mfma_f32_16x16x32_bf16 v[104:107], v[44:47], a[48:51], v[104:107]\n"
"v_mfma_f32_16x16x32_bf16 v[104:107], v[48:51], a[64:67], v[104:107]\n"
"buffer_load_dwordx4 v[32:35], v1, s[24:27], s76 offen sc1\n"
"v_mfma_f32_16x16x32_bf16 v[104:107], v[52:55], a[80:83], v[104:107]\n"
"v_mfma_f32_16x16x32_bf16 v[108:111], v[44:47], a[52:55], v[108:111]\n"
"v_mfma_f32_16x16x32_bf16 v[108:111], v[48:51], a[68:71], v[108:111]\n"
"v_mfma_f32_16x16x32_bf16 v[108:111], v[52:55], a[84:87], v[108:111]\n"
"buffer_load_dwordx4 v[36:39], v1, s[24:27], s76 offen offset:1024 sc1\n"
"v_mfma_f32_16x16x32_bf16 v[112:115], v[44:47], a[56:59], v[112:115]\n"
"v_mfma_f32_16x16x32_bf16 v[112:115], v[48:51], a[72:75], v[112:115]\n"
"v_mfma_f32_16x16x32_bf16 v[112:115], v[52:55], a[88:91], v[112:115]\n"
"s_waitcnt vmcnt(15)\n"
"s_mov_b64 exec, 1\n"
"buffer_store_dword v27, off, s[24:27], s74 offset:548 sc1\n"
"s_mov_b64 exec, -1\n"
"v_mfma_f32_16x16x32_bf16 v[116:119], v[44:47], a[60:63], v[116:119]\n"
"buffer_load_dwordx4 v[40:43], v1, s[24:27], s76 offen offset:2048 sc1\n"
"v_mfma_f32_16x16x32_bf16 v[116:119], v[48:51], a[76:79], v[116:119]\n"
"s_add_u32 s76, s76, 0x3000\n"
"v_mfma_f32_16x16x32_bf16 v[116:119], v[52:55], a[92:95], v[116:119]\n"
"v_mfma_f32_16x16x32_bf16 v[104:107], v[56:59], a[96:99], v[104:107]\n"
"v_mfma_f32_16x16x32_bf16 v[104:107], v[60:63], a[112:115], v[104:107]\n"
"buffer_load_dwordx4 v[44:47], v1, s[24:27], s76 offen sc1\n"
"v_mfma_f32_16x16x32_bf16 v[104:107], v[64:67], a[128:131], v[104:107]\n"
"v_mfma_f32_16x16x32_bf16 v[108:111], v[56:59], a[100:103], v[108:111]\n"
"s_lshl_b32 s62, s77, 11\n"
"s_add_u32 s74, s21, s62\n"
"v_mfma_f32_16x16x32_bf16 v[108:111], v[60:63], a[116:119], v[108:111]\n"
"s_add_u32 s77, s58, 7\n"
"s_and_b32 s77, s77, 7\n"
"v_mfma_f32_16x16x32_bf16 v[108:111], v[64:67], a[132:135], v[108:111]\n"
"buffer_load_dwordx4 v[48:51], v1, s[24:27], s76 offen offset:1024 sc1\n"
"v_mfma_f32_16x16x32_bf16 v[112:115], v[56:59], a[104:107], v[112:115]\n"
"v_mfma_f32_16x16x32_bf16 v[112:115], v[60:63], a[120:123], v[112:115]\n"
"s_lshl3_add_u32 s62, s77, s58\n"
"s_lshl_b32 s62, s62, 13\n"
"v_mfma_f32_16x16x32_bf16 v[112:115], v[64:67], a[136:139], v[112:115]\n"
"s_add_u32 s72, s82, s62\n"
"v_mfma_f32_16x16x32_bf16 v[116:119], v[56:59], a[108:111], v[116:119]\n"
"buffer_load_dwordx4 v[52:55], v1, s[24:27], s76 offen offset:2048 sc1\n"
"v_mfma_f32_16x16x32_bf16 v[116:119], v[60:63], a[124:127], v[116:119]\n"
"s_add_u32 s76, s76, 0x3000\n"
"v_mfma_f32_16x16x32_bf16 v[116:119], v[64:67], a[140:143], v[116:119]\n"
"s_waitcnt vmcnt(17)\n"
"v_mfma_f32_16x16x32_bf16 v[104:107], v[68:71], a[144:147], v[104:107]\n"
"v_mfma_f32_16x16x32_bf16 v[104:107], v[72:75], a[160:163], v[104:107]\n"
"buffer_load_dwordx4 v[56:59], v1, s[24:27], s76 offen sc1\n"
"v_mfma_f32_16x16x32_bf16 v[104:107], v[76:79], a[176:179], v[104:107]\n"
"v_mfma_f32_16x16x32_bf16 v[108:111], v[68:71], a[148:151], v[108:111]\n"
"s_sub_u32 s63, s58, 4\n"
"s_and_b32 s63, s63, 7\n"
"v_mfma_f32_16x16x32_bf16 v[108:111], v[72:75], a[164:167], v[108:111]\n"
"s_lshl3_add_u32 s63, s58, s63\n"
"s_lshl_b32 s63, s63, 13\n"
"v_mfma_f32_16x16x32_bf16 v[108:111], v[76:79], a[180:183], v[108:111]\n"
"buffer_load_dwordx4 v[60:63], v1, s[24:27], s76 offen offset:1024 sc1\n"
"v_mfma_f32_16x16x32_bf16 v[112:115], v[68:71], a[152:155], v[112:115]\n"
"v_mfma_f32_16x16x32_bf16 v[112:115], v[72:75], a[168:171], v[112:115]\n"
"s_add_u32 s64, s82, s63\n"
"v_mfma_f32_16x16x32_bf16 v[112:115], v[76:79], a[184:187], v[112:115]\n"
"v_mfma_f32_16x16x32_bf16 v[116:119], v[68:71], a[156:159], v[116:119]\n"
"buffer_load_dwordx4 v[64:67], v1, s[24:27], s76 offen offset:2048 sc1\n"
"v_mfma_f32_16x16x32_bf16 v[116:119], v[72:75], a[172:175], v[116:119]\n"
"s_waitcnt vmcnt(12)\n"
"v_readfirstlane_b32 vcc_lo, v16\n"
"s_nop 0\n"
"s_cmp_ge_u32 vcc_lo, 1\n"
"s_cbranch_scc1 Lc4_d%=\n"
"Lc4%=:\n"
"s_mov_b64 exec, 1\n"
"v_mov_b32 v16, 0\n"
"buffer_atomic_add v16, off, s[24:27], s75 offset:544 sc0 sc1\n"
"s_mov_b64 exec, -1\n"
"s_waitcnt vmcnt(0)\n"
"v_readfirstlane_b32 vcc_lo, v16\n"
"s_nop 0\n"
"s_cmp_ge_u32 vcc_lo, 1\n"
"s_cbranch_scc1 Lc4_d%=\n"
"s_sleep 2\n"
"s_sub_u32 s60, s60, 1\n"
"s_cbranch_scc0 Lc4%=\n"
"s_mov_b32 s60, 0\n"
"Lc4_d%=:\n"
"s_add_u32 s76, s76, 0x3000\n"
"v_mfma_f32_16x16x32_bf16 v[116:119], v[76:79], a[188:191], v[116:119]\n"
"v_mfma_f32_16x16x32_bf16 v[144:147], v[22:25], v[160:163], v[144:147]\n"
"v_mfma_f32_16x16x32_bf16 v[148:151], v[20:23], v[160:163], v[148:151]\n"
"v_mfma_f32_16x16x32_bf16 v[152:155], v[22:25], v[164:167], v[152:155]\n"
"v_mfma_f32_16x16x32_bf16 v[156:159], v[20:23], v[164:167], v[156:159]\n"
"buffer_load_dwordx4 v[160:163], v1, s[24:27], s64 offen sc1\n"
"v_mfma_f32_16x16x32_bf16 v[104:107], v[80:83], a[192:195], v[104:107]\n"
"v_mfma_f32_16x16x32_bf16 v[104:107], v[84:87], a[208:211], v[104:107]\n"
"buffer_load_dwordx4 v[68:71], v1, s[24:27], s76 offen sc1\n"
"v_mfma_f32_16x16x32_bf16 v[104:107], v[88:91], a[224:227], v[104:107]\n"
"v_mfma_f32_16x16x32_bf16 v[108:111], v[80:83], a[196:199], v[108:111]\n"
"v_mfma_f32_16x16x32_bf16 v[108:111], v[84:87], a[212:215], v[108:111]\n"
"v_mfma_f32_16x16x32_bf16 v[108:111], v[88:91], a[228:231], v[108:111]\n"
"buffer_load_dwordx4 v[72:75], v1, s[24:27], s76 offen offset:1024 sc1\n"
"v_mfma_f32_16x16x32_bf16 v[112:115], v[80:83], a[200:203], v[112:115]\n"
"v_mfma_f32_16x16x32_bf16 v[112:115], v[84:87], a[216:219], v[112:115]\n"
"v_mfma_f32_16x16x32_bf16 v[112:115], v[88:91], a[232:235], v[112:115]\n"
"v_mfma_f32_16x16x32_bf16 v[116:119], v[80:83], a[204:207], v[116:119]\n"
"buffer_load_dwordx4 v[76:79], v1, s[24:27], s76 offen offset:2048 sc1\n"
"v_mfma_f32_16x16x32_bf16 v[116:119], v[84:87], a[220:223], v[116:119]\n"
"s_add_u32 s76, s76, 0x3000\n"
"v_mfma_f32_16x16x32_bf16 v[116:119], v[88:91], a[236:239], v[116:119]\n"
"s_waitcnt vmcnt(15)\n"
"v_mfma_f32_16x16x32_bf16 v[104:107], v[92:95], a[240:243], v[104:107]\n"
"v_mfma_f32_16x16x32_bf16 v[104:107], v[96:99], v[224:227], v[104:107]\n"
"buffer_load_dwordx4 v[80:83], v1, s[24:27], s76 offen sc1\n"
"v_mfma_f32_16x16x32_bf16 v[120:123], v[100:103], v[240:243], v[104:107]\n"
"v_mfma_f32_16x16x32_bf16 v[108:111], v[92:95], a[244:247], v[108:111]\n"
"v_mfma_f32_16x16x32_bf16 v[108:111], v[96:99], v[228:231], v[108:111]\n"
"v_mfma_f32_16x16x32_bf16 v[124:127], v[100:103], v[244:247], v[108:111]\n"
"buffer_load_dwordx4 v[84:87], v1, s[24:27], s76 offen offset:1024 sc1\n"
"v_mfma_f32_16x16x32_bf16 v[112:115], v[92:95], a[248:251], v[112:115]\n"
"v_mfma_f32_16x16x32_bf16 v[112:115], v[96:99], v[232:235], v[112:115]\n"
"buffer_load_dwordx4 v[164:167], v1, s[24:27], s64 offen offset:1024 sc1\n"
"v_mfma_f32_16x16x32_bf16 v[128:131], v[100:103], v[248:251], v[112:115]\n"
"s_waitcnt vmcnt(17)\n"
"s_mov_b64 exec, 1\n"
"buffer_store_dword v27, off, s[24:27], s74 offset:552 sc1\n"
"s_mov_b64 exec, -1\n"
"v_mfma_f32_16x16x32_bf16 v[116:119], v[92:95], a[252:255], v[116:119]\n"
"buffer_load_dwordx4 v[88:91], v1, s[24:27], s76 offen offset:2048 sc1\n"
"v_mfma_f32_16x16x32_bf16 v[116:119], v[96:99], v[236:239], v[116:119]\n"
"s_add_u32 s76, s76, 0x3000\n"
"v_mfma_f32_16x16x32_bf16 v[132:135], v[100:103], v[252:255], v[116:119]\n"
"s_waitcnt vmcnt(15)\n"
"v_mfma_f32_16x16x32_bf16 v[104:107], v[32:35], a[0:3], v[144:147]\n"
"s_mov_b64 exec, 1\n"
"v_mov_b32 v16, 0\n"
"buffer_atomic_add v16, off, s[24:27], s75 offset:548 sc0 sc1\n"
"s_mov_b64 exec, -1\n"
"v_mfma_f32_16x16x32_bf16 v[104:107], v[36:39], a[16:19], v[104:107]\n"
"buffer_load_dwordx4 v[92:95], v1, s[24:27], s76 offen sc1\n"
"v_mfma_f32_16x16x32_bf16 v[104:107], v[40:43], a[32:35], v[104:107]\n"
"v_cvt_pk_bf16_f32 v136, v120, v121\n"
"v_cvt_pk_bf16_f32 v137, v122, v123\n"
"v_mfma_f32_16x16x32_bf16 v[108:111], v[32:35], a[4:7], v[148:151]\n"
"v_cvt_pk_bf16_f32 v138, v124, v125\n"
"v_cvt_pk_bf16_f32 v139, v126, v127\n"
"v_mfma_f32_16x16x32_bf16 v[108:111], v[36:39], a[20:23], v[108:111]\n"
"v_mfma_f32_16x16x32_bf16 v[108:111], v[40:43], a[36:39], v[108:111]\n"
"buffer_load_dwordx4 v[96:99], v1, s[24:27], s76 offen offset:1024 sc1\n"
"v_mfma_f32_16x16x32_bf16 v[112:115], v[32:35], a[8:11], v[152:155]\n"
"buffer_store_dwordx4 v[136:139], v1, s[24:27], s72 offen sc1\n"
"v_mfma_f32_16x16x32_bf16 v[112:115], v[36:39], a[24:27], v[112:115]\n"
"v_cvt_pk_bf16_f32 v140, v128, v129\n"
"v_cvt_pk_bf16_f32 v141, v130, v131\n"
"v_mfma_f32_16x16x32_bf16 v[112:115], v[40:43], a[40:43], v[112:115]\n"
"v_cvt_pk_bf16_f32 v142, v132, v133\n"
"v_cvt_pk_bf16_f32 v143, v134, v135\n"
"v_mfma_f32_16x16x32_bf16 v[116:119], v[32:35], a[12:15], v[156:159]\n"
"buffer_load_dwordx4 v[100:103], v1, s[24:27], s76 offen offset:2048 sc1\n"
"v_mfma_f32_16x16x32_bf16 v[116:119], v[36:39], a[28:31], v[116:119]\n"
"s_add_u32 s76, s76, 0x3000\n"
"v_mfma_f32_16x16x32_bf16 v[116:119], v[40:43], a[44:47], v[116:119]\n"
"buffer_store_dwordx4 v[140:143], v1, s[24:27], s72 offen offset:1024 sc1\n"
"s_waitcnt vmcnt(18)\n"
"v_mfma_f32_16x16x32_bf16 v[104:107], v[44:47], a[48:51], v[104:107]\n"
"v_mfma_f32_16x16x32_bf16 v[104:107], v[48:51], a[64:67], v[104:107]\n"
"v_mfma_f32_16x16x32_bf16 v[104:107], v[52:55], a[80:83], v[104:107]\n"
"v_mfma_f32_16x16x32_bf16 v[108:111], v[44:47], a[52:55], v[108:111]\n"
"v_mfma_f32_16x16x32_bf16 v[108:111], v[48:51], a[68:71], v[108:111]\n"
"v_mfma_f32_16x16x32_bf16 v[108:111], v[52:55], a[84:87], v[108:111]\n"
"v_mfma_f32_16x16x32_bf16 v[112:115], v[44:47], a[56:59], v[112:115]\n"
"v_mfma_f32_16x16x32_bf16 v[112:115], v[48:51], a[72:75], v[112:115]\n"
"v_mfma_f32_16x16x32_bf16 v[112:115], v[52:55], a[88:91], v[112:115]\n"
"v_mfma_f32_16x16x32_bf16 v[116:119], v[44:47], a[60:63], v[116:119]\n"
"v_mfma_f32_16x16x32_bf16 v[116:119], v[48:51], a[76:79], v[116:119]\n"
"v_mfma_f32_16x16x32_bf16 v[116:119], v[52:55], a[92:95], v[116:119]\n"
"s_waitcnt vmcnt(15)\n"
"v_mfma_f32_16x16x32_bf16 v[104:107], v[56:59], a[96:99], v[104:107]\n"
"v_mfma_f32_16x16x32_bf16 v[104:107], v[60:63], a[112:115], v[104:107]\n"
"v_mfma_f32_16x16x32_bf16 v[104:107], v[64:67], a[128:131], v[104:107]\n"
"v_mfma_f32_16x16x32_bf16 v[108:111], v[56:59], a[100:103], v[108:111]\n"
"s_lshl_b32 s62, s77, 11\n"
"s_add_u32 s74, s21, s62\n"
"s_sub_u32 s63, s58, 5\n"
"s_and_b32 s63, s63, 7\n"
"v_mfma_f32_16x16x32_bf16 v[108:111], v[60:63], a[116:119], v[108:111]\n"
"s_lshl3_add_u32 s63, s58, s63\n"
"s_lshl_b32 s63, s63, 13\n"
"v_mfma_f32_16x16x32_bf16 v[108:111], v[64:67], a[132:135], v[108:111]\n"
"v_mfma_f32_16x16x32_bf16 v[112:115], v[56:59], a[104:107], v[112:115]\n"
"v_mfma_f32_16x16x32_bf16 v[112:115], v[60:63], a[120:123], v[112:115]\n"
"s_add_u32 s64, s82, s63\n"
"v_mfma_f32_16x16x32_bf16 v[112:115], v[64:67], a[136:139], v[112:115]\n"
"v_mfma_f32_16x16x32_bf16 v[116:119], v[56:59], a[108:111], v[116:119]\n"
"v_mfma_f32_16x16x32_bf16 v[116:119], v[60:63], a[124:127], v[116:119]\n"
"s_waitcnt vmcnt(5)\n"
"v_readfirstlane_b32 vcc_lo, v16\n"
"s_nop 0\n"
"s_cmp_ge_u32 vcc_lo, 1\n"
"s_cbranch_scc1 Lc5_d%=\n"
"Lc5%=:\n"
"s_mov_b64 exec, 1\n"
"v_mov_b32 v16, 0\n"
"buffer_atomic_add v16, off, s[24:27], s75 offset:548 sc0 sc1\n"
"s_mov_b64 exec, -1\n"
"s_waitcnt vmcnt(0)\n"
"v_readfirstlane_b32 vcc_lo, v16\n"
"s_nop 0\n"
"s_cmp_ge_u32 vcc_lo, 1\n"
"s_cbranch_scc1 Lc5_d%=\n"
"s_sleep 2\n"
"s_sub_u32 s60, s60, 1\n"
"s_cbranch_scc0 Lc5%=\n"
"s_mov_b32 s60, 0\n"
"Lc5_d%=:\n"
"v_mfma_f32_16x16x32_bf16 v[116:119], v[64:67], a[140:143], v[116:119]\n"
"v_mfma_f32_16x16x32_bf16 v[104:107], v[22:25], v[160:163], v[104:107]\n"
"v_mfma_f32_16x16x32_bf16 v[108:111], v[20:23], v[160:163], v[108:111]\n"
"v_mfma_f32_16x16x32_bf16 v[112:115], v[22:25], v[164:167], v[112:115]\n"
"v_mfma_f32_16x16x32_bf16 v[116:119], v[20:23], v[164:167], v[116:119]\n"
"buffer_load_dwordx4 v[160:163], v1, s[24:27], s64 offen sc1\n"
"v_mfma_f32_16x16x32_bf16 v[104:107], v[68:71], a[144:147], v[104:107]\n"
"v_mfma_f32_16x16x32_bf16 v[104:107], v[72:75], a[160:163], v[104:107]\n"
"v_mfma_f32_16x16x32_bf16 v[104:107], v[76:79], a[176:179], v[104:107]\n"
"v_mfma_f32_16x16x32_bf16 v[108:111], v[68:71], a[148:151], v[108:111]\n"
"buffer_load_dwordx4 v[164:167], v1, s[24:27], s64 offen offset:1024 sc1\n"
"v_mfma_f32_16x16x32_bf16 v[108:111], v[72:75], a[164:167], v[108:111]\n"
"s_mov_b64 exec, 1\n"
"v_mov_b32 v16, 0\n"
"buffer_atomic_add v16, off, s[24:27], s75 offset:552 sc0 sc1\n"
"s_mov_b64 exec, -1\n"
"v_mfma_f32_16x16x32_bf16 v[108:111], v[76:79], a[180:183], v[108:111]\n"
"v_mfma_f32_16x16x32_bf16 v[112:115], v[68:71], a[152:155], v[112:115]\n"
"v_mfma_f32_16x16x32_bf16 v[112:115], v[72:75], a[168:171], v[112:115]\n"
"v_mfma_f32_16x16x32_bf16 v[112:115], v[76:79], a[184:187], v[112:115]\n"
"v_mfma_f32_16x16x32_bf16 v[116:119], v[68:71], a[156:159], v[116:119]\n"
"v_mfma_f32_16x16x32_bf16 v[116:119], v[72:75], a[172:175], v[116:119]\n"
"v_mfma_f32_16x16x32_bf16 v[116:119], v[76:79], a[188:191], v[116:119]\n"
"s_sub_u32 s62, s58, 6\n"
"s_and_b32 s62, s62, 7\n"
"v_mfma_f32_16x16x32_bf16 v[104:107], v[80:83], a[192:195], v[104:107]\n"
"s_lshl3_add_u32 s62, s58, s62\n"
"s_lshl_b32 s63, s62, 13\n"
"v_mfma_f32_16x16x32_bf16 v[104:107], v[84:87], a[208:211], v[104:107]\n"
"v_mfma_f32_16x16x32_bf16 v[104:107], v[88:91], a[224:227], v[104:107]\n"
"v_mfma_f32_16x16x32_bf16 v[108:111], v[80:83], a[196:199], v[108:111]\n"
"s_add_u32 s98, s82, s63\n"
"s_sub_u32 s62, s58, 7\n"
"v_mfma_f32_16x16x32_bf16 v[108:111], v[84:87], a[212:215], v[108:111]\n"
"s_waitcnt vmcnt(3)\n"
"s_mov_b64 exec, 1\n"
"buffer_store_dword v27, off, s[24:27], s74 offset:556 sc1\n"
"s_mov_b64 exec, -1\n"
"s_and_b32 s62, s62, 7\n"
"s_lshl3_add_u32 s62, s58, s62\n"
"v_mfma_f32_16x16x32_bf16 v[108:111], v[88:91], a[228:231], v[108:111]\n"
"v_mfma_f32_16x16x32_bf16 v[112:115], v[80:83], a[200:203], v[112:115]\n"
"v_mfma_f32_16x16x32_bf16 v[112:115], v[84:87], a[216:219], v[112:115]\n"
"s_lshl_b32 s63, s62, 13\n"
"s_add_u32 s99, s82, s63\n"
"v_mfma_f32_16x16x32_bf16 v[112:115], v[88:91], a[232:235], v[112:115]\n"
"v_mfma_f32_16x16x32_bf16 v[116:119], v[80:83], a[204:207], v[116:119]\n"
"v_mfma_f32_16x16x32_bf16 v[116:119], v[84:87], a[220:223], v[116:119]\n"
"v_mfma_f32_16x16x32_bf16 v[116:119], v[88:91], a[236:239], v[116:119]\n"
"v_mfma_f32_16x16x32_bf16 v[104:107], v[92:95], a[240:243], v[104:107]\n"
"v_mfma_f32_16x16x32_bf16 v[104:107], v[96:99], v[224:227], v[104:107]\n"
"v_mfma_f32_16x16x32_bf16 v[120:123], v[100:103], v[240:243], v[104:107]\n"
"v_mfma_f32_16x16x32_bf16 v[108:111], v[92:95], a[244:247], v[108:111]\n"
"v_mfma_f32_16x16x32_bf16 v[108:111], v[96:99], v[228:231], v[108:111]\n"
"v_mfma_f32_16x16x32_bf16 v[124:127], v[100:103], v[244:247], v[108:111]\n"
"v_mfma_f32_16x16x32_bf16 v[112:115], v[92:95], a[248:251], v[112:115]\n"
"v_mfma_f32_16x16x32_bf16 v[112:115], v[96:99], v[232:235], v[112:115]\n"
"v_mfma_f32_16x16x32_bf16 v[128:131], v[100:103], v[248:251], v[112:115]\n"
"v_mfma_f32_16x16x32_bf16 v[116:119], v[92:95], a[252:255], v[116:119]\n"
"v_mfma_f32_16x16x32_bf16 v[116:119], v[96:99], v[236:239], v[116:119]\n"
"v_mfma_f32_16x16x32_bf16 v[132:135], v[100:103], v[252:255], v[116:119]\n"
"s_waitcnt vmcnt(1)\n"
"v_readfirstlane_b32 vcc_lo, v16\n"
"s_nop 0\n"
"s_cmp_ge_u32 vcc_lo, 1\n"
"s_cbranch_scc1 Lc6_d%=\n"
"Lc6%=:\n"
"s_mov_b64 exec, 1\n"
"v_mov_b32 v16, 0\n"
"buffer_atomic_add v16, off, s[24:27], s75 offset:552 sc0 sc1\n"
"s_mov_b64 exec, -1\n"
"s_waitcnt vmcnt(0)\n"
"v_readfirstlane_b32 vcc_lo, v16\n"
"s_nop 0\n"
"s_cmp_ge_u32 vcc_lo, 1\n"
"s_cbranch_scc1 Lc6_d%=\n"
"s_sleep 2\n"
"s_sub_u32 s60, s60, 1\n"
"s_cbranch_scc0 Lc6%=\n"
"s_mov_b32 s60, 0\n"
"Lc6_d%=:\n"
"buffer_load_dwordx4 v[168:171], v1, s[24:27], s98 offen sc1\n"
"buffer_load_dwordx4 v[172:175], v1, s[24:27], s98 offen offset:1024 sc1\n"
"v_mfma_f32_16x16x32_bf16 v[120:123], v[22:25], v[160:163], v[120:123]\n"
"v_mfma_f32_16x16x32_bf16 v[124:127], v[20:23], v[160:163], v[124:127]\n"
"v_mfma_f32_16x16x32_bf16 v[128:131], v[22:25], v[164:167], v[128:131]\n"
"v_mfma_f32_16x16x32_bf16 v[132:135], v[20:23], v[164:167], v[132:135]\n"
"Lc7%=:\n"
"s_mov_b64 exec, 1\n"
"v_mov_b32 v16, 0\n"
"buffer_atomic_add v16, off, s[24:27], s75 offset:556 sc0 sc1\n"
"s_mov_b64 exec, -1\n"
"s_waitcnt vmcnt(0)\n"
"v_readfirstlane_b32 vcc_lo, v16\n"
"s_nop 0\n"
"s_cmp_ge_u32 vcc_lo, 1\n"
"s_cbranch_scc1 Lc7_d%=\n"
"s_sleep 2\n"
"s_sub_u32 s60, s60, 1\n"
"s_cbranch_scc0 Lc7%=\n"
"s_mov_b32 s60, 0\n"
"Lc7_d%=:\n"
"buffer_load_dwordx4 v[200:203], v1, s[24:27], s99 offen sc1\n"
"buffer_load_dwordx4 v[204:207], v1, s[24:27], s99 offen offset:1024 sc1\n"
"s_cmp_lg_u32 s59, 0\n"
"s_cbranch_scc1 Lw1%=\n"
"s_add_u32 m0, s20, 0\n"
"s_nop 0\n"
"buffer_load_dwordx4 v6, s[48:51], s8 offen lds\n"
"s_add_u32 m0, s20, 0x400\n"
"s_nop 0\n"
"buffer_load_dwordx4 v6, s[48:51], s9 offen lds\n"
"s_add_u32 m0, s20, 0x780\n"
"s_nop 0\n"
"buffer_load_dwordx4 v6, s[48:51], s8 offen offset:128 lds\n"
"s_add_u32 m0, s20, 0xb80\n"
"s_nop 0\n"
"buffer_load_dwordx4 v6, s[48:51], s9 offen offset:128 lds\n"
"s_add_u32 m0, s20, 0xf00\n"
"s_nop 0\n"
"buffer_load_dwordx4 v6, s[48:51], s8 offen offset:256 lds\n"
"s_add_u32 m0, s20, 0x1300\n"
"s_nop 0\n"
"buffer_load_dwordx4 v6, s[48:51], s9 offen offset:256 lds\n"
"Lw1%=:\n"
"v_mfma_f32_16x16x32_bf16 v[120:123], v[22:25], v[168:171], v[120:123]\n"
"v_mfma_f32_16x16x32_bf16 v[124:127], v[20:23], v[168:171], v[124:127]\n"
"v_mfma_f32_16x16x32_bf16 v[128:131], v[22:25], v[172:175], v[128:131]\n"
"v_mfma_f32_16x16x32_bf16 v[132:135], v[20:23], v[172:175], v[132:135]\n"
"s_waitcnt vmcnt(0)\n"
"v_mfma_f32_16x16x32_bf16 v[120:123], v[22:25], v[200:203], v[120:123]\n"
"v_mfma_f32_16x16x32_bf16 v[124:127], v[20:23], v[200:203], v[124:127]\n"
"v_mfma_f32_16x16x32_bf16 v[128:131], v[22:25], v[204:207], v[128:131]\n"
"v_mfma_f32_16x16x32_bf16 v[132:135], v[20:23], v[204:207], v[132:135]\n"
"s_nop 4\n"
"v_fma_f32 v120, v120, v192, v176\n"
"v_fma_f32 v121, v121, v192, v177\n"
"v_fma_f32 v122, v122, v192, v178\n"
"v_fma_f32 v123, v123, v192, v179\n"
"v_fma_f32 v124, v124, v193, v180\n"
"v_fma_f32 v125, v125, v193, v181\n"
"v_fma_f32 v126, v126, v193, v182\n"
"v_fma_f32 v127, v127, v193, v183\n"
"v_cvt_pk_bf16_f32 v136, v120, v121\n"
"v_cvt_pk_bf16_f32 v138, v124, v125\n"
"v_cvt_pk_bf16_f32 v137, v122, v123\n"
"v_cvt_pk_bf16_f32 v139, v126, v127\n"
"v_permlane16_swap_b32 v136, v138\n"
"s_nop 0\n"
"v_permlane16_swap_b32 v137, v139\n"
"s_cmp_lg_u32 s59, 0\n"
"s_cbranch_scc1 Lo00_%=\n"
"buffer_store_dwordx4 v[136:139], v12, s[40:43], s92 offen sc1\n"
"Lo00_%=:\n"
"s_cmp_lg_u32 s59, 1\n"
"s_cbranch_scc1 Lo10_%=\n"
"buffer_store_dwordx4 v[136:139], v12, s[40:43], s92 offen\n"
"Lo10_%=:\n"
"v_fma_f32 v128, v128, v194, v184\n"
"v_fma_f32 v129, v129, v194, v185\n"
"v_fma_f32 v130, v130, v194, v186\n"
"v_fma_f32 v131, v131, v194, v187\n"
"v_fma_f32 v132, v132, v195, v188\n"
"v_fma_f32 v133, v133, v195, v189\n"
"v_fma_f32 v134, v134, v195, v190\n"
"v_fma_f32 v135, v135, v195, v191\n"
"v_cvt_pk_bf16_f32 v140, v128, v129\n"
"v_cvt_pk_bf16_f32 v142, v132, v133\n"
"v_cvt_pk_bf16_f32 v141, v130, v131\n"
"v_cvt_pk_bf16_f32 v143, v134, v135\n"
"v_permlane16_swap_b32 v140, v142\n"
"s_nop 0\n"
"v_permlane16_swap_b32 v141, v143\n"
"s_cmp_lg_u32 s59, 0\n"
"s_cbranch_scc1 Lo01_%=\n"
"buffer_store_dwordx4 v[140:143], v12, s[40:43], s93 offen sc1\n"
"Lo01_%=:\n"
"s_cmp_lg_u32 s59, 1\n"
"s_cbranch_scc1 Lo11_%=\n"
"buffer_store_dwordx4 v[140:143], v12, s[40:43], s93 offen\n"
"Lo11_%=:\n"
"s_cmp_eq_u32 s59, 1\n"
"s_cbranch_scc1 Lend%=\n"
"s_and_b32 s62, s58, 2\n"
"s_xor_b32 s80, s80, s62\n"
"s_lshl_b32 s62, s62, 10\n"
"s_xor_b32 s12, s12, s62\n"
"s_xor_b32 s13, s13, s62\n"
"s_xor_b32 s14, s14, s62\n"
"s_xor_b32 s15, s15, s62\n"
"s_xor_b32 s16, s16, s62\n"
"s_xor_b32 s17, s17, s62\n"
"s_xor_b32 s18, s18, s62\n"
"s_xor_b32 s19, s19, s62\n"
"s_lshl_b32 s63, s56, 10\n"
"s_add_u32 m0, s63, 0x22000\n"
"s_nop 0\n"
"buffer_load_dwordx4 v6, s[48:51], s90 offen lds\n"
"s_add_u32 m0, s63, 0x22f00\n"
"s_nop 0\n"
"buffer_load_dwordx4 v6, s[48:51], s90 offen offset:256 lds\n"
"s_add_u32 m0, s63, 0x23e00\n"
"s_nop 0\n"
"buffer_load_dwordx4 v6, s[48:51], s90 offen offset:512 lds\n"
"s_add_u32 m0, s63, 0x24d00\n"
"s_nop 0\n"
"buffer_load_dwordx4 v6, s[48:51], s90 offen offset:768 lds\n"
"s_add_u32 m0, s63, 0x25c00\n"
"s_nop 0\n"
"buffer_load_dwordx4 v6, s[48:51], s90 offen offset:1024 lds\n"
"s_add_u32 m0, s20, 0x1680\n"
"s_nop 0\n"
"buffer_load_dwordx4 v6, s[48:51], s8 offen offset:384 lds\n"
"s_add_u32 m0, s20, 0x1a80\n"
"s_nop 0\n"
"buffer_load_dwordx4 v6, s[48:51], s9 offen offset:384 lds\n"
"s_add_u32 m0, s20, 0x1e00\n"
"s_nop 0\n"
"buffer_load_dwordx4 v6, s[48:51], s8 offen offset:512 lds\n"
"s_add_u32 m0, s20, 0x2200\n"
"s_nop 0\n"
"buffer_load_dwordx4 v6, s[48:51], s9 offen offset:512 lds\n"
"s_add_u32 m0, s20, 0x2580\n"
"s_nop 0\n"
"buffer_load_dwordx4 v6, s[48:51], s8 offen offset:640 lds\n"
"s_add_u32 m0, s20, 0x2980\n"
"s_nop 0\n"
"buffer_load_dwordx4 v6, s[48:51], s9 offen offset:640 lds\n"
"s_add_u32 m0, s20, 0x2d00\n"
"s_nop 0\n"
"buffer_load_dwordx4 v6, s[48:51], s8 offen offset:768 lds\n"
"s_add_u32 m0, s20, 0x3100\n"
"s_nop 0\n"
"buffer_load_dwordx4 v6, s[48:51], s9 offen offset:768 lds\n"
"s_waitcnt vmcnt(19)\n"
"ds_read_b128 v[32:35], v8\n"
"ds_read_b128 v[36:39], v9\n"
"s_add_u32 m0, s20, 0x3480\n"
"s_nop 0\n"
"buffer_load_dwordx4 v6, s[48:51], s8 offen offset:896 lds\n"
"s_add_u32 m0, s20, 0x3880\n"
"s_nop 0\n"
"buffer_load_dwordx4 v6, s[48:51], s9 offen offset:896 lds\n"
"s_waitcnt vmcnt(15)\n"
"s_barrier\n"
"s_bfm_b64 exec, 8, 0\n"
"buffer_atomic_add v27, v31, s[24:27], s21 offen offset:524 sc1\n"
"s_mov_b64 exec, -1\n"
"ds_read_b128 v[40:43], v8 offset:2048\n"
"ds_read_b128 v[44:47], v9 offset:2048\n"
"s_add_u32 m0, s20, 0xfffffc00\n"
"s_waitcnt lgkmcnt(2)\n"
"buffer_load_dwordx4 v6, s[48:51], s8 offen offset:1024 lds\n"
"s_add_u32 m0, s20, 0\n"
"s_nop 0\n"
"buffer_load_dwordx4 v6, s[48:51], s9 offen offset:1024 lds\n"
"ds_read_b128 v[48:51], v8 offset:4096\n"
"ds_read_b128 v[52:55], v9 offset:4096\n"
"s_add_u32 m0, s20, 0x800\n"
"s_waitcnt lgkmcnt(2)\n"
"buffer_load_dwordx4 v6, s[48:51], s10 offen lds\n"
"s_add_u32 m0, s20, 0xc00\n"
"s_nop 0\n"
"buffer_load_dwordx4 v6, s[48:51], s11 offen lds\n"
"s_waitcnt vmcnt(13)\n"
"ds_read_b128 v[56:59], v8 offset:6144\n"
"ds_read_b128 v[60:63], v9 offset:6144\n"
"s_add_u32 m0, s20, 0xf80\n"
"s_waitcnt lgkmcnt(2)\n"
"buffer_load_dwordx4 v6, s[48:51], s10 offen offset:128 lds\n"
"s_add_u32 m0, s20, 0x1380\n"
"s_nop 0\n"
"buffer_load_dwordx4 v6, s[48:51], s11 offen offset:128 lds\n"
"s_waitcnt vmcnt(13)\n"
"ds_read_b128 v[64:67], v8 offset:8192\n"
"ds_read_b128 v[68:71], v9 offset:8192\n"
"s_add_u32 m0, s20, 0x1700\n"
"s_waitcnt lgkmcnt(2)\n"
"buffer_load_dwordx4 v6, s[48:51], s10 offen offset:256 lds\n"
"s_add_u32 m0, s20, 0x1b00\n"
"s_nop 0\n"
"buffer_load_dwordx4 v6, s[48:51], s11 offen offset:256 lds\n"
"s_waitcnt vmcnt(13)\n"
"ds_read_b128 v[72:75], v8 offset:10240\n"
"ds_read_b128 v[76:79], v9 offset:10240\n"
"s_add_u32 m0, s20, 0x1e80\n"
"s_waitcnt lgkmcnt(2)\n"
"buffer_load_dwordx4 v6, s[48:51], s10 offen offset:384 lds\n"
"s_add_u32 m0, s20, 0x2280\n"
"s_nop 0\n"
"buffer_load_dwordx4 v6, s[48:51], s11 offen offset:384 lds\n"
"s_waitcnt vmcnt(13)\n"
"ds_read_b128 v[80:83], v8 offset:12288\n"
"ds_read_b128 v[84:87], v9 offset:12288\n"
"s_add_u32 m0, s20, 0x2600\n"
"s_waitcnt lgkmcnt(2)\n"
"buffer_load_dwordx4 v6, s[48:51], s10 offen offset:512 lds\n"
"s_add_u32 m0, s20, 0x2a00\n"
"s_nop 0\n"
"buffer_load_dwordx4 v6, s[48:51], s11 offen offset:512 lds\n"
"s_mov_b64 exec, 1\n"
"v_mov_b32 v16, 0\n"
"buffer_atomic_add v16, off, s[24:27], s75 offset:524 sc0 sc1\n"
"s_mov_b64 exec, -1\n"
"s_waitcnt vmcnt(14)\n"
"ds_read_b128 v[88:91], v8 offset:14336\n"
"ds_read_b128 v[92:95], v9 offset:14336\n"
"s_add_u32 m0, s20, 0x2d80\n"
"s_waitcnt lgkmcnt(2)\n"
"buffer_load_dwordx4 v6, s[48:51], s10 offen offset:640 lds\n"
"s_add_u32 m0, s20, 0x3180\n"
"s_nop 0\n"
"buffer_load_dwordx4 v6, s[48:51], s11 offen offset:640 lds\n"
"s_waitcnt vmcnt(13)\n"
"ds_read_b128 v[96:99], v8\n"
"ds_read_b128 v[100:103], v9\n"
"s_add_u32 m0, s20, 0x3500\n"
"s_waitcnt lgkmcnt(2)\n"
"buffer_load_dwordx4 v6, s[48:51], s10 offen offset:768 lds\n"
"s_add_u32 m0, s20, 0x3900\n"
"s_nop 0\n"
"buffer_load_dwordx4 v6, s[48:51], s11 offen offset:768 lds\n"
"s_waitcnt vmcnt(13)\n"
"ds_read_b128 a[136:139], v8 offset:2048\n"
"ds_read_b128 a[140:143], v9 offset:2048\n"
"s_add_u32 m0, s20, 0xfffffc80\n"
"s_waitcnt lgkmcnt(2)\n"
"buffer_load_dwordx4 v6, s[48:51], s10 offen offset:896 lds\n"
"s_add_u32 m0, s20, 0x80\n"
"s_nop 0\n"
"buffer_load_dwordx4 v6, s[48:51], s11 offen offset:896 lds\n"
"s_waitcnt vmcnt(13)\n"
"ds_read_b128 a[144:147], v8 offset:4096\n"
"ds_read_b128 a[148:151], v9 offset:4096\n"
"s_add_u32 m0, s20, 0x400\n"
"s_waitcnt lgkmcnt(2)\n"
"buffer_load_dwordx4 v6, s[48:51], s10 offen offset:1024 lds\n"
"s_add_u32 m0, s20, 0x800\n"
"s_nop 0\n"
"buffer_load_dwordx4 v6, s[48:51], s11 offen offset:1024 lds\n"
"v_mov_b32 v156, 0x3f803f80\n"
"v_mov_b32 v157, 0x3f803f80\n"
"v_mov_b32 v158, 0x3f803f80\n"
"v_mov_b32 v159, 0x3f803f80\n"
"s_lshl2_add_u32 s64, s57, s56\n"
"s_mul_i32 s64, s64, 0x4800\n"
"s_add_u32 s64, s64, 0x1600000\n"
"s_mul_i32 s61, s56, 0x4800\n"
"v_mov_b32 v180, v1\n"
"v_mov_b32 v181, v2\n"
"v_add_u32 v182, 0x400, v1\n"
"v_add_u32 v183, 0x400, v2\n"
"s_waitcnt vmcnt(8)\n"
"v_readfirstlane_b32 vcc_lo, v16\n"
"s_nop 0\n"
"s_cmp_ge_u32 vcc_lo, 8\n"
"s_cbranch_scc1 Ldp_d%=\n"
"Ldp%=:\n"
"s_mov_b64 exec, 1\n"
"v_mov_b32 v16, 0\n"
"buffer_atomic_add v16, off, s[24:27], s75 offset:524 sc0 sc1\n"
"s_mov_b64 exec, -1\n"
"s_waitcnt vmcnt(0)\n"
"v_readfirstlane_b32 vcc_lo, v16\n"
"s_nop 0\n"
"s_cmp_ge_u32 vcc_lo, 8\n"
"s_cbranch_scc1 Ldp_d%=\n"
"s_sleep 2\n"
"s_sub_u32 s60, s60, 1\n"
"s_cbranch_scc0 Ldp%=\n"
"s_mov_b32 s60, 0\n"
"Ldp_d%=:\n"
"s_add_u32 m0, s61, 0\n"
"s_nop 0\n"
"buffer_load_dwordx4 v1, s[24:27], s64 offen sc1 lds\n"
"buffer_load_dwordx4 v1, s[24:27], s64 offen offset:1024 sc1 lds\n"
"buffer_load_dwordx4 v1, s[24:27], s64 offen offset:2048 sc1 lds\n"
"buffer_load_dwordx4 v1, s[24:27], s64 offen offset:3072 sc1 lds\n"
"s_add_u32 s64, s64, 0x1000\n"
"s_add_u32 m0, s61, 0x1000\n"
"s_nop 0\n"
"buffer_load_dwordx4 v1, s[24:27], s64 offen sc1 lds\n"
"buffer_load_dwordx4 v1, s[24:27], s64 offen offset:1024 sc1 lds\n"
"buffer_load_dwordx4 v1, s[24:27], s64 offen offset:2048 sc1 lds\n"
"buffer_load_dwordx4 v1, s[24:27], s64 offen offset:3072 sc1 lds\n"
"s_add_u32 s64, s64, 0x1000\n"
"s_add_u32 m0, s61, 0x2000\n"
"s_nop 0\n"
"buffer_load_dwordx4 v1, s[24:27], s64 offen sc1 lds\n"
"buffer_load_dwordx4 v1, s[24:27], s64 offen offset:1024 sc1 lds\n"
"buffer_load_dwordx4 v1, s[24:27], s64 offen offset:2048 sc1 lds\n"
"buffer_load_dwordx4 v1, s[24:27], s64 offen offset:3072 sc1 lds\n"
"s_add_u32 s64, s64, 0x1000\n"
"s_add_u32 m0, s61, 0x3000\n"
"s_nop 0\n"
"buffer_load_dwordx4 v1, s[24:27], s64 offen sc1 lds\n"
"buffer_load_dwordx4 v1, s[24:27], s64 offen offset:1024 sc1 lds\n"
"buffer_load_dwordx4 v1, s[24:27], s64 offen offset:2048 sc1 lds\n"
"buffer_load_dwordx4 v1, s[24:27], s64 offen offset:3072 sc1 lds\n"
"s_add_u32 s64, s64, 0x1000\n"
"s_add_u32 m0, s61, 0x4000\n"
"s_nop 0\n"
"buffer_load_dwordx4 v1, s[24:27], s64 offen sc1 lds\n"
"buffer_load_dwordx4 v1, s[24:27], s64 offen offset:1024 sc1 lds\n"
"s_waitcnt vmcnt(16)\n"
"ds_read_b128 v[184:187], v5\n"
"ds_read_b128 v[188:191], v5 offset:1024\n"
"ds_read_b128 a[152:155], v8 offset:6144\n"
"ds_read_b128 a[156:159], v9 offset:6144\n"
"s_add_u32 m0, s20, 0x1000\n"
"s_waitcnt lgkmcnt(4)\n"
"buffer_load_dwordx4 v7, s[28:31], s12 offen lds\n"
"s_add_u32 m0, s20, 0x1400\n"
"s_nop 0\n"
"buffer_load_dwordx4 v7, s[28:31], s13 offen lds\n"
"s_waitcnt vmcnt(16)\n"
"ds_read_b128 v[192:195], v5 offset:2048\n"
"ds_read_b128 v[196:199], v5 offset:3072\n"
"s_waitcnt lgkmcnt(4)\n"
"v_mfma_f32_16x16x32_bf16 v[148:151], v[156:159], v[184:187], 0\n"
"v_mfma_f32_16x16x32_bf16 v[152:155], v[184:187], v[184:187], 0\n"
"v_mfma_f32_16x16x32_bf16 v[148:151], v[156:159], v[188:191], v[148:151]\n"
"v_mfma_f32_16x16x32_bf16 v[152:155], v[188:191], v[188:191], v[152:155]\n"
"ds_read_b128 a[160:163], v8 offset:8192\n"
"ds_read_b128 a[164:167], v9 offset:8192\n"
"s_waitcnt vmcnt(14)\n"
"ds_read_b128 v[200:203], v5 offset:4096\n"
"ds_read_b128 v[204:207], v5 offset:5120\n"
"s_waitcnt lgkmcnt(4)\n"
"v_mfma_f32_16x16x32_bf16 v[148:151], v[156:159], v[192:195], v[148:151]\n"
"v_mfma_f32_16x16x32_bf16 v[152:155], v[192:195], v[192:195], v[152:155]\n"
"v_mfma_f32_16x16x32_bf16 v[148:151], v[156:159], v[196:199], v[148:151]\n"
"v_mfma_f32_16x16x32_bf16 v[152:155], v[196:199], v[196:199], v[152:155]\n"
"s_add_u32 m0, s20, 0x1780\n"
"s_nop 0\n"
"buffer_load_dwordx4 v7, s[28:31], s12 offen offset:128 lds\n"
"s_add_u32 m0, s20, 0x1b80\n"
"s_nop 0\n"
"buffer_load_dwordx4 v7, s[28:31], s13 offen offset:128 lds\n"
"ds_read_b128 a[168:171], v8 offset:10240\n"
"ds_read_b128 a[172:175], v9 offset:10240\n"
"s_waitcnt vmcnt(14)\n"
"ds_read_b128 v[208:211], v5 offset:6144\n"
"ds_read_b128 v[212:215], v5 offset:7168\n"
"s_waitcnt lgkmcnt(4)\n"
"v_mfma_f32_16x16x32_bf16 v[148:151], v[156:159], v[200:203], v[148:151]\n"
"v_mfma_f32_16x16x32_bf16 v[152:155], v[200:203], v[200:203], v[152:155]\n"
"v_mfma_f32_16x16x32_bf16 v[148:151], v[156:159], v[204:207], v[148:151]\n"
"v_mfma_f32_16x16x32_bf16 v[152:155], v[204:207], v[204:207], v[152:155]\n"
"s_add_u32 m0, s20, 0x1f00\n"
"s_nop 0\n"
"buffer_load_dwordx4 v7, s[28:31], s12 offen offset:256 lds\n"
"s_add_u32 m0, s20, 0x2300\n"
"s_nop 0\n"
"buffer_load_dwordx4 v7, s[28:31], s13 offen offset:256 lds\n"
"s_waitcnt vmcnt(14)\n"
"ds_read_b128 v[216:219], v5 offset:8192\n"
"ds_read_b128 v[220:223], v5 offset:9216\n"
"s_waitcnt lgkmcnt(2)\n"
"v_mfma_f32_16x16x32_bf16 v[148:151], v[156:159], v[208:211], v[148:151]\n"
"v_mfma_f32_16x16x32_bf16 v[152:155], v[208:211], v[208:211], v[152:155]\n"
"v_mfma_f32_16x16x32_bf16 v[148:151], v[156:159], v[212:215], v[148:151]\n"
"v_mfma_f32_16x16x32_bf16 v[152:155], v[212:215], v[212:215], v[152:155]\n"
"ds_read_b128 a[176:179], v8 offset:12288\n"
"ds_read_b128 a[180:183], v9 offset:12288\n"
"s_waitcnt vmcnt(12)\n"
"ds_read_b128 v[224:227], v5 offset:10240\n"
"ds_read_b128 v[228:231], v5 offset:11264\n"
"s_waitcnt lgkmcnt(4)\n"
"v_mfma_f32_16x16x32_bf16 v[148:151], v[156:159], v[216:219], v[148:151]\n"
"v_mfma_f32_16x16x32_bf16 v[152:155], v[216:219], v[216:219], v[152:155]\n"
"v_mfma_f32_16x16x32_bf16 v[148:151], v[156:159], v[220:223], v[148:151]\n"
"v_mfma_f32_16x16x32_bf16 v[152:155], v[220:223], v[220:223], v[152:155]\n"
"s_add_u32 m0, s20, 0x2680\n"
"s_nop 0\n"
"buffer_load_dwordx4 v7, s[28:31], s12 offen offset:384 lds\n"
"s_add_u32 m0, s20, 0x2a80\n"
"s_nop 0\n"
"buffer_load_dwordx4 v7, s[28:31], s13 offen offset:384 lds\n"
"ds_read_b128 a[184:187], v8 offset:14336\n"
"ds_read_b128 a[188:191], v9 offset:14336\n"
"s_waitcnt vmcnt(12)\n"
"ds_read_b128 v[232:235], v5 offset:12288\n"
"ds_read_b128 v[236:239], v5 offset:13312\n"
"s_waitcnt lgkmcnt(4)\n"
"v_mfma_f32_16x16x32_bf16 v[148:151], v[156:159], v[224:227], v[148:151]\n"
"v_mfma_f32_16x16x32_bf16 v[152:155], v[224:227], v[224:227], v[152:155]\n"
"v_mfma_f32_16x16x32_bf16 v[148:151], v[156:159], v[228:231], v[148:151]\n"
"v_mfma_f32_16x16x32_bf16 v[152:155], v[228:231], v[228:231], v[152:155]\n"
"s_add_u32 m0, s20, 0x2e00\n"
"s_nop 0\n"
"buffer_load_dwordx4 v7, s[28:31], s12 offen offset:512 lds\n"
"s_add_u32 m0, s20, 0x3200\n"
"s_nop 0\n"
"buffer_load_dwordx4 v7, s[28:31], s13 offen offset:512 lds\n"
"s_waitcnt vmcnt(12)\n"
"ds_read_b128 v[240:243], v5 offset:14336\n"
"ds_read_b128 v[244:247], v5 offset:15360\n"
"s_waitcnt lgkmcnt(2)\n"
"v_mfma_f32_16x16x32_bf16 v[148:151], v[156:159], v[232:235], v[148:151]\n"
"v_mfma_f32_16x16x32_bf16 v[152:155], v[232:235], v[232:235], v[152:155]\n"
"v_mfma_f32_16x16x32_bf16 v[148:151], v[156:159], v[236:239], v[148:151]\n"
"v_mfma_f32_16x16x32_bf16 v[152:155], v[236:239], v[236:239], v[152:155]\n"
"ds_read_b128 a[192:195], v8\n"
"ds_read_b128 a[196:199], v9\n"
"s_add_u32 m0, s20, 0x3580\n"
"s_nop 0\n"
"buffer_load_dwordx4 v7, s[28:31], s12 offen offset:640 lds\n"
"s_add_u32 m0, s20, 0x3980\n"
"s_nop 0\n"
"buffer_load_dwordx4 v7, s[28:31], s13 offen offset:640 lds\n"
"s_waitcnt vmcnt(12)\n"
"ds_read_b128 v[248:251], v5 offset:16384\n"
"ds_read_b128 v[252:255], v5 offset:17408\n"
"s_waitcnt lgkmcnt(4)\n"
"v_mfma_f32_16x16x32_bf16 v[148:151], v[156:159], v[240:243], v[148:151]\n"
"v_mfma_f32_16x16x32_bf16 v[152:155], v[240:243], v[240:243], v[152:155]\n"
"v_mfma_f32_16x16x32_bf16 v[148:151], v[156:159], v[244:247], v[148:151]\n"
"v_mfma_f32_16x16x32_bf16 v[152:155], v[244:247], v[244:247], v[152:155]\n"
"ds_read_b128 a[200:203], v8 offset:2048\n"
"ds_read_b128 a[204:207], v9 offset:2048\n"
"s_waitcnt lgkmcnt(2)\n"
"v_mfma_f32_16x16x32_bf16 v[148:151], v[156:159], v[248:251], v[148:151]\n"
"v_mfma_f32_16x16x32_bf16 v[152:155], v[248:251], v[248:251], v[152:155]\n"
"v_mfma_f32_16x16x32_bf16 v[148:151], v[156:159], v[252:255], v[148:151]\n"
"v_mfma_f32_16x16x32_bf16 v[152:155], v[252:255], v[252:255], v[152:155]\n"
"s_add_u32 m0, s20, 0xfffffd00\n"
"s_nop 0\n"
"buffer_load_dwordx4 v7, s[28:31], s12 offen offset:768 lds\n"
"s_add_u32 m0, s20, 0x100\n"
"s_nop 0\n"
"buffer_load_dwordx4 v7, s[28:31], s13 offen offset:768 lds\n"
"s_mov_b32 s59, 1\n"
"s_branch Llayer%=\n"
"Lend%=:\n"
"s_waitcnt vmcnt(0)\n"
    :: "s"(__builtin_amdgcn_kernarg_segment_ptr()), "s"(w), "s"(blockIdx.x), "s"((unsigned)(size_t)smem)
    : "v0", "v1", "v2", "v3", "v4", "v5", "v6", "v7", "v8", "v9", "v10", "v11", "v12", "v13", "v14", "v15", "v16", "v17", "v18", "v19", "v20", "v21", "v22", "v23", "v24", "v25", "v26", "v27", "v28", "v29", "v30", "v31", "v32", "v33", "v34", "v35", "v36", "v37", "v38", "v39", "v40", "v41", "v42", "v43", "v44", "v45", "v46", "v47", "v48", "v49", "v50", "v51", "v52", "v53", "v54", "v55", "v56", "v57", "v58", "v59", "v60", "v61", "v62", "v63", "v64", "v65", "v66", "v67", "v68", "v69", "v70", "v71", "v72", "v73", "v74", "v75", "v76", "v77", "v78", "v79", "v80", "v81", "v82", "v83", "v84", "v85", "v86", "v87", "v88", "v89", "v90", "v91", "v92", "v93", "v94", "v95", "v96", "v97", "v98", "v99", "v100", "v101", "v102", "v103", "v104", "v105", "v106", "v107", "v108", "v109", "v110", "v111", "v112", "v113", "v114", "v115", "v116", "v117", "v118", "v119", "v120", "v121", "v122", "v123", "v124", "v125", "v126", "v127", "v128", "v129", "v130", "v131", "v132", "v133", "v134", "v135", "v136", "v137", "v138", "v139", "v140", "v141", "v142", "v143", "v144", "v145", "v146", "v147", "v148", "v149", "v150", "v151", "v152", "v153", "v154", "v155", "v156", "v157", "v158", "v159", "v160", "v161", "v162", "v163", "v164", "v165", "v166", "v167", "v168", "v169", "v170", "v171", "v172", "v173", "v174", "v175", "v176", "v177", "v178", "v179", "v180", "v181", "v182", "v183", "v184", "v185", "v186", "v187", "v188", "v189", "v190", "v191", "v192", "v193", "v194", "v195", "v196", "v197", "v198", "v199", "v200", "v201", "v202", "v203", "v204", "v205", "v206", "v207", "v208", "v209", "v210", "v211", "v212", "v213", "v214", "v215", "v216", "v217", "v218", "v219", "v220", "v221", "v222", "v223", "v224", "v225", "v226", "v227", "v228", "v229", "v230", "v231", "v232", "v233", "v234", "v235", "v236", "v237", "v238", "v239", "v240", "v241", "v242", "v243", "v244", "v245", "v246", "v247", "v248", "v249", "v250", "v251", "v252", "v253", "v254", "v255", "a0", "a1", "a2", "a3", "a4", "a5", "a6", "a7", "a8", "a9", "a10", "a11", "a12", "a13", "a14", "a15", "a16", "a17", "a18", "a19", "a20", "a21", "a22", "a23", "a24", "a25", "a26", "a27", "a28", "a29", "a30", "a31", "a32", "a33", "a34", "a35", "a36", "a37", "a38", "a39", "a40", "a41", "a42", "a43", "a44", "a45", "a46", "a47", "a48", "a49", "a50", "a51", "a52", "a53", "a54", "a55", "a56", "a57", "a58", "a59", "a60", "a61", "a62", "a63", "a64", "a65", "a66", "a67", "a68", "a69", "a70", "a71", "a72", "a73", "a74", "a75", "a76", "a77", "a78", "a79", "a80", "a81", "a82", "a83", "a84", "a85", "a86", "a87", "a88", "a89", "a90", "a91", "a92", "a93", "a94", "a95", "a96", "a97", "a98", "a99", "a100", "a101", "a102", "a103", "a104", "a105", "a106", "a107", "a108", "a109", "a110", "a111", "a112", "a113", "a114", "a115", "a116", "a117", "a118", "a119", "a120", "a121", "a122", "a123", "a124", "a125", "a126", "a127", "a128", "a129", "a130", "a131", "a132", "a133", "a134", "a135", "a136", "a137", "a138", "a139", "a140", "a141", "a142", "a143", "a144", "a145", "a146", "a147", "a148", "a149", "a150", "a151", "a152", "a153", "a154", "a155", "a156", "a157", "a158", "a159", "a160", "a161", "a162", "a163", "a164", "a165", "a166", "a167", "a168", "a169", "a170", "a171", "a172", "a173", "a174", "a175", "a176", "a177", "a178", "a179", "a180", "a181", "a182", "a183", "a184", "a185", "a186", "a187", "a188", "a189", "a190", "a191", "a192", "a193", "a194", "a195", "a196", "a197", "a198", "a199", "a200", "a201", "a202", "a203", "a204", "a205", "a206", "a207", "a208", "a209", "a210", "a211", "a212", "a213", "a214", "a215", "a216", "a217", "a218", "a219", "a220", "a221", "a222", "a223", "a224", "a225", "a226", "a227", "a228", "a229", "a230", "a231", "a232", "a233", "a234", "a235", "a236", "a237", "a238", "a239", "a240", "a241", "a242", "a243", "a244", "a245", "a246", "a247", "a248", "a249", "a250", "a251", "a252", "a253", "a254", "a255", "s8", "s9", "s10", "s11", "s12", "s13", "s14", "s15", "s16", "s17", "s18", "s19", "s20", "s21", "s22", "s23", "s24", "s25", "s26", "s27", "s28", "s29", "s30", "s31", "s33", "s34", "s35", "s36", "s37", "s38", "s39", "s40", "s41", "s42", "s43", "s44", "s45", "s46", "s47", "s48", "s49", "s50", "s51", "s52", "s53", "s54", "s55", "s56", "s57", "s58", "s59", "s60", "s61", "s62", "s63", "s64", "s65", "s66", "s67", "s68", "s69", "s70", "s71", "s72", "s73", "s74", "s75", "s76", "s77", "s78", "s79", "s80", "s81", "s82", "s83", "s84", "s85", "s86", "s87", "s88", "s89", "s90", "s91", "s92", "s93", "s94", "s95", "s96", "s97", "s98", "s99", "vcc", "scc", "memory");
}

extern "C" void kernel_launch(void* const* d_in, const int* in_sizes, int n_in,
                              void* d_out, int out_size, void* d_ws, size_t ws_size,
                              hipStream_t stream) {
    fnet_ffn_persistent<<<256, 256, 0, stream>>>(d_ws, d_in[0], d_in[1], d_in[2], d_in[3], d_in[4], d_in[6], d_in[7], d_out);
}
```
